```python
import torch

def _softmax(x, axis=-1):
    return torch.softmax(x, dim=axis)
def _gelu(x):
    return torch.nn.functional.gelu(x, approximate="tanh")
def _silu(x):
    return torch.nn.functional.silu(x)
def _sigmoid(x):
    return torch.sigmoid(x)
def _layernorm(x, axis=-1, eps=1e-5):
    m = x.mean(dim=axis, keepdim=True)
    v = x.var(dim=axis, keepdim=True, unbiased=False)
    return (x - m) / torch.sqrt(v + eps)
def _scatter_add(tgt, idx, src, axis=0):
    return tgt.clone().index_add_(axis, idx, src)
def _segment_sum(src, seg, n):
    out = torch.zeros((n,) + src.shape[1:], dtype=src.dtype)
    return out.index_add_(0, seg, src)
def _pad(x, pw):
    flat = tuple(v for pair in reversed(pw) for v in pair)
    return torch.nn.functional.pad(x, flat)

kernel_name = 'gated_mix__ssm_conv_h3__glu_gated_glu__prompt'

def setup_inputs():

    _w = (lambda a: (lambda t: t.to(torch.bfloat16) if t.is_floating_point() else t)(torch.as_tensor(a)))
    def _A_setup():
        B, S, D = 1, 2048, 576
        ks = 4
        return {
            "q": _w((lambda *s: torch.randn(*s, dtype=torch.bfloat16) * 0.05)(B, S, D)),
            "k": _w((lambda *s: torch.randn(*s, dtype=torch.bfloat16) * 0.8)(B, S, D)),
            "v": _w((lambda *s: torch.randn(*s, dtype=torch.bfloat16) * 0.8)(B, S, D)),
            "shift_k": _w((lambda *s: torch.randn(*s, dtype=torch.bfloat16) * 0.8)(D, ks) * 0.5),
            "ssm_k": _w((lambda *s: torch.randn(*s, dtype=torch.bfloat16) * 0.8)(D, S) * (1.0 / S ** 0.5)),
            "ssm_D": _w((lambda *s: torch.randn(*s, dtype=torch.bfloat16) * 0.8)(D)),
        }
    _A_in = _A_setup()

    def _B_setup():
        B, S, D, F = 1, 2048, 576, 4608
        return {
            "x": _w((lambda *s: torch.randn(*s, dtype=torch.bfloat16) * 0.05)(B, S, D)),
            "w_gate": _w((lambda *s: torch.randn(*s, dtype=torch.bfloat16) * 0.4)(D, F)),
            "w_up": _w((lambda *s: torch.randn(*s, dtype=torch.bfloat16) * 0.05)(D, F)),
            "w_down": _w((lambda *s: torch.randn(*s, dtype=torch.bfloat16) * 0.05)(F, D)),
        }
    _B_in = _B_setup()

    return {
        "A_q": _A_in["q"],
        "A_k": _A_in["k"],
        "A_v": _A_in["v"],
        "A_shift_k": _A_in["shift_k"],
        "A_ssm_k": _A_in["ssm_k"],
        "A_ssm_D": _A_in["ssm_D"],
        "B_x": _B_in["x"],
        "B_w_gate": _B_in["w_gate"],
        "B_w_up": _B_in["w_up"],
        "B_w_down": _B_in["w_down"],
        "gate_w": _w((lambda *s: torch.randn(*s, dtype=torch.bfloat16) * 0.05)(576) * 16.0),
    }

def reference(A_q, A_k, A_v, A_shift_k, A_ssm_k, A_ssm_D, B_x, B_w_gate, B_w_up, B_w_down, gate_w):
    def _A_ref(q, k, v, shift_k, ssm_k, ssm_D):
        B, S, D = q.shape
        ks = shift_k.shape[-1]
        k_pad = _pad(k, ((0, 0), (ks - 1, 0), (0, 0)))
        k_shift = torch.zeros_like(k)
        for t in range(ks):
            k_shift = k_shift + k_pad[:, t:t + S, :] * shift_k[:, ks - 1 - t][None, None, :]
        kv = k_shift * v
        L = 2 * S
        kv_pad = _pad(kv, ((0, 0), (0, S), (0, 0)))
        kf_pad = _pad(ssm_k, ((0, 0), (0, S)))
        kv_f = (lambda x, axes=(-2,-1): torch.fft.rfft2(x.float(), dim=axes))(kv_pad[:, None, :, :], axes=(1, 2))
        kf_f = (lambda x, axes=(-2,-1): torch.fft.rfft2(x.float(), dim=axes))(kf_pad[:, None, :], axes=(1, 2))
        kf_f = (lambda x, *d: x.permute(*d))(kf_f, 1, 2, 0)
        y_f = kv_f * kf_f[None, :, :, :]
        y = (lambda x, s, axes=(-2,-1): torch.fft.irfft2(x, s=s, dim=axes).to(torch.bfloat16))(y_f, (1, L), axes=(1, 2))[:, 0, :S, :]
        y = y + kv * ssm_D[None, None, :]
        return q * y

    def _B_ref(x, w_gate, w_up, w_down):
        g = torch.einsum("bsd,df->bsf", x, w_gate)
        u = torch.einsum("bsd,df->bsf", x, w_up)
        h = _sigmoid(g) * u
        return torch.einsum("bsf,fd->bsd", h, w_down)

    x = A_q
    g = _sigmoid((x * gate_w).sum(-1, keepdims=True))
    a = _A_ref(x, A_k, A_v, A_shift_k, A_ssm_k, A_ssm_D)
    b = _B_ref(x, B_w_gate, B_w_up, B_w_down)
    return g * a + (1 - g) * b
```

```cpp
#include <hip/hip_runtime.h>
#include <hip/hip_bf16.h>
typedef unsigned short u16;
typedef short bf16x8 __attribute__((ext_vector_type(8)));
typedef float f32x4 __attribute__((ext_vector_type(4)));
constexpr int S=2048, D=576, F=4608;
__device__ __forceinline__ float b2f(u16 x){ return __uint_as_float(((unsigned)x)<<16); }
__device__ __forceinline__ u16 f2b(float f){ __hip_bfloat16 b=__float2bfloat16(f); return *(u16*)&b; }
__device__ __forceinline__ float rb(float f){ return b2f(f2b(f)); }

__global__ void k_prep(const u16* q,const u16* k,const u16* v,const u16* shk,const u16* gw,u16* kv,u16* kvT,float* g){
  int s=blockIdx.x; int t=threadIdx.x;
  float acc=0.f;
  for(int tt=0;tt<4;++tt){ int ss=s+tt-3; float kk= ss>=0? b2f(k[ss*D+t]):0.f; acc=rb(acc+rb(kk*b2f(shk[t*4+3-tt]))); }
  u16 r=f2b(acc*b2f(v[s*D+t]));
  kv[s*D+t]=r; kvT[t*S+s]=r;
  float p=rb(b2f(q[s*D+t])*b2f(gw[t]));
  __shared__ float red[9];
  for(int o=32;o>0;o>>=1) p+=__shfl_down(p,o);
  if((t&63)==0) red[t>>6]=p;
  __syncthreads();
  if(t==0){ float z=0; for(int i=0;i<9;++i) z+=red[i]; z=rb(z); g[s]=rb(1.f/(1.f+__expf(-z))); }
}
__global__ void k_conv(const u16* kvT,const u16* h,const u16* q,const u16* kv,const u16* sD,u16* a){
  int d=blockIdx.x; __shared__ float xs[S], hs[S];
  for(int i=threadIdx.x;i<S;i+=256){ xs[i]=b2f(kvT[d*S+i]); hs[i]=b2f(h[d*S+i]); }
  __syncthreads();
  for(int s=threadIdx.x;s<S;s+=256){
    float acc=0.f;
    for(int j=0;j<=s;++j) acc+=hs[j]*xs[s-j];
    float y=rb(acc); y=rb(y+rb(xs[s]*b2f(sD[d])));
    a[s*D+d]=f2b(b2f(q[s*D+d])*y);
  }
}
__global__ void k_gemm1(const u16* x,const u16* wg,const u16* wu,u16* H){
  int lane=threadIdx.x&63, w=threadIdx.x>>6;
  int m0=blockIdx.y*64+w*16, n0=blockIdx.x*16;
  f32x4 ag={0,0,0,0}, au={0,0,0,0};
  for(int k0=0;k0<D;k0+=32){
    bf16x8 a=*(const bf16x8*)(x+(m0+(lane&15))*D+k0+(lane>>4)*8);
    bf16x8 bg,bu;
    for(int j=0;j<8;++j){ int kk=k0+(lane>>4)*8+j; bg[j]=wg[kk*F+n0+(lane&15)]; bu[j]=wu[kk*F+n0+(lane&15)]; }
    ag=__builtin_amdgcn_mfma_f32_16x16x32_bf16(a,bg,ag,0,0,0);
    au=__builtin_amdgcn_mfma_f32_16x16x32_bf16(a,bu,au,0,0,0);
  }
  for(int j=0;j<4;++j){ int m=m0+(lane>>4)*4+j; float g=ag[j],u=au[j]; float h=u/(1.f+__expf(-g)); H[m*F+n0+(lane&15)]=f2b(h);}
}
__global__ void k_gemm2(const u16* H,const u16* wd,const u16* a,const float* g,u16* out){
  int lane=threadIdx.x&63, w=threadIdx.x>>6;
  int m0=blockIdx.y*64+w*16, n0=blockIdx.x*16;
  f32x4 ac={0,0,0,0};
  for(int k0=0;k0<F;k0+=32){
    bf16x8 av=*(const bf16x8*)(H+(m0+(lane&15))*F+k0+(lane>>4)*8);
    bf16x8 b;
    for(int j=0;j<8;++j){ int kk=k0+(lane>>4)*8+j; b[j]=wd[kk*D+n0+(lane&15)]; }
    ac=__builtin_amdgcn_mfma_f32_16x16x32_bf16(av,b,ac,0,0,0);
  }
  for(int j=0;j<4;++j){ int m=m0+(lane>>4)*4+j; int n=n0+(lane&15); float gg=g[m]; float b=rb(ac[j]);
    out[m*D+n]=f2b(rb(gg*b2f(a[m*D+n]))+rb(rb(1.f-gg)*b)); }
}
extern "C" void kernel_launch(void* const* d_in, const int* in_sizes, int n_in, void* d_out, int out_size, void* d_ws, size_t ws_size, hipStream_t stream) {
  const u16* q=(const u16*)d_in[0]; const u16* k=(const u16*)d_in[1]; const u16* v=(const u16*)d_in[2];
  const u16* shk=(const u16*)d_in[3]; const u16* h=(const u16*)d_in[4]; const u16* sD=(const u16*)d_in[5];
  const u16* wg=(const u16*)d_in[7]; const u16* wu=(const u16*)d_in[8]; const u16* wd=(const u16*)d_in[9]; const u16* gw=(const u16*)d_in[10];
  char* ws=(char*)d_ws;
  u16* kv=(u16*)ws; u16* kvT=(u16*)(ws+4*1024*1024); u16* a=(u16*)(ws+8*1024*1024); float* g=(float*)(ws+12*1024*1024); u16* H=(u16*)(ws+16*1024*1024);
  k_prep<<<S,576,0,stream>>>(q,k,v,shk,gw,kv,kvT,g);
  k_conv<<<D,256,0,stream>>>(kvT,h,q,kv,sD,a);
  k_gemm1<<<dim3(F/16,S/64),256,0,stream>>>(q,wg,wu,H);
  k_gemm2<<<dim3(D/16,S/64),256,0,stream>>>(H,wd,a,g,(u16*)d_out);
}
```

```cpp
#include <hip/hip_runtime.h>
static __device__ const unsigned char kTab[256*64]={
0,1,2,5,6,7,10,11,12,15,16,3,8,13,17,4,9,14,14,14,14,14,14,14,14,14,14,14,22,40,58,18,0,1,2,3,4,5,6,7,8,9,10,11,12,13,14,15,16,17,18,19,20,21,21,21,21,21,21,21,21,21,21,21,
0,1,2,5,6,7,10,11,12,15,16,3,8,13,17,4,9,14,14,14,14,14,14,14,14,14,14,14,22,40,58,18,0,1,2,3,4,5,6,7,8,9,10,11,12,13,14,15,16,17,18,19,20,21,21,21,21,21,21,21,21,21,21,21,
18,19,20,23,24,25,28,29,30,33,34,21,26,31,35,22,27,32,32,32,32,32,32,32,32,32,32,32,40,58,4,18,0,1,2,3,4,5,6,7,8,9,10,11,12,13,14,15,16,17,18,19,20,21,21,21,21,21,21,21,21,21,21,21,
18,19,20,23,24,25,28,29,30,33,34,21,26,31,35,22,27,32,32,32,32,32,32,32,32,32,32,32,40,58,4,18,0,1,2,3,4,5,6,7,8,9,10,11,12,13,14,15,16,17,18,19,20,21,21,21,21,21,21,21,21,21,21,21,
36,37,38,41,42,43,46,47,48,51,52,39,44,49,53,40,45,50,50,50,50,50,50,50,50,50,50,50,58,4,22,18,0,1,2,3,4,5,6,7,8,9,10,11,12,13,14,15,16,17,18,19,20,21,21,21,21,21,21,21,21,21,21,21,
36,37,38,41,42,43,46,47,48,51,52,39,44,49,53,40,45,50,50,50,50,50,50,50,50,50,50,50,58,4,22,18,0,1,2,3,4,5,6,7,8,9,10,11,12,13,14,15,16,17,18,19,20,21,21,21,21,21,21,21,21,21,21,21,
54,55,56,59,60,61,64,65,66,69,70,57,62,67,71,58,63,68,68,68,68,68,68,68,68,68,68,68,4,22,40,18,0,1,2,3,4,5,6,7,8,9,10,11,12,13,14,15,16,17,18,19,20,21,21,21,21,21,21,21,21,21,21,21,
54,55,56,59,60,61,64,65,66,69,70,57,62,67,71,58,63,68,68,68,68,68,68,68,68,68,68,68,4,22,40,18,0,1,2,3,4,5,6,7,8,9,10,11,12,13,14,15,16,17,18,19,20,21,21,21,21,21,21,21,21,21,21,21,
5,1,2,6,7,10,11,12,15,16,0,3,8,13,17,4,9,14,14,14,14,14,14,14,14,14,14,14,22,40,58,18,0,1,2,3,4,5,6,7,8,9,10,11,12,13,14,15,16,17,18,19,20,21,21,21,21,21,21,21,21,21,21,21,
5,1,2,6,7,10,11,12,15,16,0,3,8,13,17,4,9,14,14,14,14,14,14,14,14,14,14,14,22,40,58,18,0,1,2,3,4,5,6,7,8,9,10,11,12,13,14,15,16,17,18,19,20,21,21,21,21,21,21,21,21,21,21,21,
23,19,20,24,25,28,29,30,33,34,18,21,26,31,35,22,27,32,32,32,32,32,32,32,32,32,32,32,40,58,4,18,0,1,2,3,4,5,6,7,8,9,10,11,12,13,14,15,16,17,18,19,20,21,21,21,21,21,21,21,21,21,21,21,
23,19,20,24,25,28,29,30,33,34,18,21,26,31,35,22,27,32,32,32,32,32,32,32,32,32,32,32,40,58,4,18,0,1,2,3,4,5,6,7,8,9,10,11,12,13,14,15,16,17,18,19,20,21,21,21,21,21,21,21,21,21,21,21,
41,37,38,42,43,46,47,48,51,52,36,39,44,49,53,40,45,50,50,50,50,50,50,50,50,50,50,50,58,4,22,18,0,1,2,3,4,5,6,7,8,9,10,11,12,13,14,15,16,17,18,19,20,21,21,21,21,21,21,21,21,21,21,21,
41,37,38,42,43,46,47,48,51,52,36,39,44,49,53,40,45,50,50,50,50,50,50,50,50,50,50,50,58,4,22,18,0,1,2,3,4,5,6,7,8,9,10,11,12,13,14,15,16,17,18,19,20,21,21,21,21,21,21,21,21,21,21,21,
59,55,56,60,61,64,65,66,69,70,54,57,62,67,71,58,63,68,68,68,68,68,68,68,68,68,68,68,4,22,40,18,0,1,2,3,4,5,6,7,8,9,10,11,12,13,14,15,16,17,18,19,20,21,21,21,21,21,21,21,21,21,21,21,
59,55,56,60,61,64,65,66,69,70,54,57,62,67,71,58,63,68,68,68,68,68,68,68,68,68,68,68,4,22,40,18,0,1,2,3,4,5,6,7,8,9,10,11,12,13,14,15,16,17,18,19,20,21,21,21,21,21,21,21,21,21,21,21,
5,2,6,7,10,11,12,15,16,0,1,3,8,13,17,4,9,14,14,14,14,14,14,14,14,14,14,14,22,40,58,18,0,1,2,3,4,5,6,7,8,9,10,11,12,13,14,15,16,17,18,19,20,21,21,21,21,21,21,21,21,21,21,21,
5,2,6,7,10,11,12,15,16,0,1,3,8,13,17,4,9,14,14,14,14,14,14,14,14,14,14,14,22,40,58,18,0,1,2,3,4,5,6,7,8,9,10,11,12,13,14,15,16,17,18,19,20,21,21,21,21,21,21,21,21,21,21,21,
23,20,24,25,28,29,30,33,34,18,19,21,26,31,35,22,27,32,32,32,32,32,32,32,32,32,32,32,40,58,4,18,0,1,2,3,4,5,6,7,8,9,10,11,12,13,14,15,16,17,18,19,20,21,21,21,21,21,21,21,21,21,21,21,
23,20,24,25,28,29,30,33,34,18,19,21,26,31,35,22,27,32,32,32,32,32,32,32,32,32,32,32,40,58,4,18,0,1,2,3,4,5,6,7,8,9,10,11,12,13,14,15,16,17,18,19,20,21,21,21,21,21,21,21,21,21,21,21,
41,38,42,43,46,47,48,51,52,36,37,39,44,49,53,40,45,50,50,50,50,50,50,50,50,50,50,50,58,4,22,18,0,1,2,3,4,5,6,7,8,9,10,11,12,13,14,15,16,17,18,19,20,21,21,21,21,21,21,21,21,21,21,21,
41,38,42,43,46,47,48,51,52,36,37,39,44,49,53,40,45,50,50,50,50,50,50,50,50,50,50,50,58,4,22,18,0,1,2,3,4,5,6,7,8,9,10,11,12,13,14,15,16,17,18,19,20,21,21,21,21,21,21,21,21,21,21,21,
59,56,60,61,64,65,66,69,70,54,55,57,62,67,71,58,63,68,68,68,68,68,68,68,68,68,68,68,4,22,40,18,0,1,2,3,4,5,6,7,8,9,10,11,12,13,14,15,16,17,18,19,20,21,21,21,21,21,21,21,21,21,21,21,
59,56,60,61,64,65,66,69,70,54,55,57,62,67,71,58,63,68,68,68,68,68,68,68,68,68,68,68,4,22,40,18,0,1,2,3,4,5,6,7,8,9,10,11,12,13,14,15,16,17,18,19,20,21,21,21,21,21,21,21,21,21,21,21,
10,6,7,11,12,15,16,0,1,2,5,8,13,3,17,9,14,4,4,4,4,4,4,4,4,4,4,4,22,40,58,18,0,1,2,3,4,5,6,7,8,9,10,11,12,13,14,15,16,17,18,19,20,21,21,21,21,21,21,21,21,21,21,21,
10,6,7,11,12,15,16,0,1,2,5,8,13,3,17,9,14,4,4,4,4,4,4,4,4,4,4,4,22,40,58,18,0,1,2,3,4,5,6,7,8,9,10,11,12,13,14,15,16,17,18,19,20,21,21,21,21,21,21,21,21,21,21,21,
28,24,25,29,30,33,34,18,19,20,23,26,31,21,35,27,32,22,22,22,22,22,22,22,22,22,22,22,40,58,4,18,0,1,2,3,4,5,6,7,8,9,10,11,12,13,14,15,16,17,18,19,20,21,21,21,21,21,21,21,21,21,21,21,
28,24,25,29,30,33,34,18,19,20,23,26,31,21,35,27,32,22,22,22,22,22,22,22,22,22,22,22,40,58,4,18,0,1,2,3,4,5,6,7,8,9,10,11,12,13,14,15,16,17,18,19,20,21,21,21,21,21,21,21,21,21,21,21,
46,42,43,47,48,51,52,36,37,38,41,44,49,39,53,45,50,40,40,40,40,40,40,40,40,40,40,40,58,4,22,18,0,1,2,3,4,5,6,7,8,9,10,11,12,13,14,15,16,17,18,19,20,21,21,21,21,21,21,21,21,21,21,21,
46,42,43,47,48,51,52,36,37,38,41,44,49,39,53,45,50,40,40,40,40,40,40,40,40,40,40,40,58,4,22,18,0,1,2,3,4,5,6,7,8,9,10,11,12,13,14,15,16,17,18,19,20,21,21,21,21,21,21,21,21,21,21,21,
64,60,61,65,66,69,70,54,55,56,59,62,67,57,71,63,68,58,58,58,58,58,58,58,58,58,58,58,4,22,40,18,0,1,2,3,4,5,6,7,8,9,10,11,12,13,14,15,16,17,18,19,20,21,21,21,21,21,21,21,21,21,21,21,
64,60,61,65,66,69,70,54,55,56,59,62,67,57,71,63,68,58,58,58,58,58,58,58,58,58,58,58,4,22,40,18,0,1,2,3,4,5,6,7,8,9,10,11,12,13,14,15,16,17,18,19,20,21,21,21,21,21,21,21,21,21,21,21,
10,7,11,12,15,16,0,1,2,5,6,8,13,3,17,9,14,4,4,4,4,4,4,4,4,4,4,4,22,40,58,18,0,1,2,3,4,5,6,7,8,9,10,11,12,13,14,15,16,17,18,19,20,21,21,21,21,21,21,21,21,21,21,21,
10,7,11,12,15,16,0,1,2,5,6,8,13,3,17,9,14,4,4,4,4,4,4,4,4,4,4,4,22,40,58,18,0,1,2,3,4,5,6,7,8,9,10,11,12,13,14,15,16,17,18,19,20,21,21,21,21,21,21,21,21,21,21,21,
28,25,29,30,33,34,18,19,20,23,24,26,31,21,35,27,32,22,22,22,22,22,22,22,22,22,22,22,40,58,4,18,0,1,2,3,4,5,6,7,8,9,10,11,12,13,14,15,16,17,18,19,20,21,21,21,21,21,21,21,21,21,21,21,
28,25,29,30,33,34,18,19,20,23,24,26,31,21,35,27,32,22,22,22,22,22,22,22,22,22,22,22,40,58,4,18,0,1,2,3,4,5,6,7,8,9,10,11,12,13,14,15,16,17,18,19,20,21,21,21,21,21,21,21,21,21,21,21,
46,43,47,48,51,52,36,37,38,41,42,44,49,39,53,45,50,40,40,40,40,40,40,40,40,40,40,40,58,4,22,18,0,1,2,3,4,5,6,7,8,9,10,11,12,13,14,15,16,17,18,19,20,21,21,21,21,21,21,21,21,21,21,21,
46,43,47,48,51,52,36,37,38,41,42,44,49,39,53,45,50,40,40,40,40,40,40,40,40,40,40,40,58,4,22,18,0,1,2,3,4,5,6,7,8,9,10,11,12,13,14,15,16,17,18,19,20,21,21,21,21,21,21,21,21,21,21,21,
64,61,65,66,69,70,54,55,56,59,60,62,67,57,71,63,68,58,58,58,58,58,58,58,58,58,58,58,4,22,40,18,0,1,2,3,4,5,6,7,8,9,10,11,12,13,14,15,16,17,18,19,20,21,21,21,21,21,21,21,21,21,21,21,
64,61,65,66,69,70,54,55,56,59,60,62,67,57,71,63,68,58,58,58,58,58,58,58,58,58,58,58,4,22,40,18,0,1,2,3,4,5,6,7,8,9,10,11,12,13,14,15,16,17,18,19,20,21,21,21,21,21,21,21,21,21,21,21,
10,11,12,15,16,0,1,2,5,6,7,8,13,3,17,9,14,4,4,4,4,4,4,4,4,4,4,4,22,40,58,18,0,1,2,3,4,5,6,7,8,9,10,11,12,13,14,15,16,17,18,19,20,21,21,21,21,21,21,21,21,21,21,21,
10,11,12,15,16,0,1,2,5,6,7,8,13,3,17,9,14,4,4,4,4,4,4,4,4,4,4,4,22,40,58,18,0,1,2,3,4,5,6,7,8,9,10,11,12,13,14,15,16,17,18,19,20,21,21,21,21,21,21,21,21,21,21,21,
28,29,30,33,34,18,19,20,23,24,25,26,31,21,35,27,32,22,22,22,22,22,22,22,22,22,22,22,40,58,4,18,0,1,2,3,4,5,6,7,8,9,10,11,12,13,14,15,16,17,18,19,20,21,21,21,21,21,21,21,21,21,21,21,
28,29,30,33,34,18,19,20,23,24,25,26,31,21,35,27,32,22,22,22,22,22,22,22,22,22,22,22,40,58,4,18,0,1,2,3,4,5,6,7,8,9,10,11,12,13,14,15,16,17,18,19,20,21,21,21,21,21,21,21,21,21,21,21,
46,47,48,51,52,36,37,38,41,42,43,44,49,39,53,45,50,40,40,40,40,40,40,40,40,40,40,40,58,4,22,18,0,1,2,3,4,5,6,7,8,9,10,11,12,13,14,15,16,17,18,19,20,21,21,21,21,21,21,21,21,21,21,21,
46,47,48,51,52,36,37,38,41,42,43,44,49,39,53,45,50,40,40,40,40,40,40,40,40,40,40,40,58,4,22,18,0,1,2,3,4,5,6,7,8,9,10,11,12,13,14,15,16,17,18,19,20,21,21,21,21,21,21,21,21,21,21,21,
64,65,66,69,70,54,55,56,59,60,61,62,67,57,71,63,68,58,58,58,58,58,58,58,58,58,58,58,4,22,40,18,0,1,2,3,4,5,6,7,8,9,10,11,12,13,14,15,16,17,18,19,20,21,21,21,21,21,21,21,21,21,21,21,
64,65,66,69,70,54,55,56,59,60,61,62,67,57,71,63,68,58,58,58,58,58,58,58,58,58,58,58,4,22,40,18,0,1,2,3,4,5,6,7,8,9,10,11,12,13,14,15,16,17,18,19,20,21,21,21,21,21,21,21,21,21,21,21,
0,12,15,16,1,2,5,6,7,10,11,13,3,8,17,14,4,9,9,9,9,9,9,9,9,9,9,9,22,40,58,18,0,1,2,3,4,5,6,7,8,9,10,11,12,13,14,15,16,17,18,19,20,21,21,21,21,21,21,21,21,21,21,21,
0,12,15,16,1,2,5,6,7,10,11,13,3,8,17,14,4,9,9,9,9,9,9,9,9,9,9,9,22,40,58,18,0,1,2,3,4,5,6,7,8,9,10,11,12,13,14,15,16,17,18,19,20,21,21,21,21,21,21,21,21,21,21,21,
18,30,33,34,19,20,23,24,25,28,29,31,21,26,35,32,22,27,27,27,27,27,27,27,27,27,27,27,40,58,4,18,0,1,2,3,4,5,6,7,8,9,10,11,12,13,14,15,16,17,18,19,20,21,21,21,21,21,21,21,21,21,21,21,
18,30,33,34,19,20,23,24,25,28,29,31,21,26,35,32,22,27,27,27,27,27,27,27,27,27,27,27,40,58,4,18,0,1,2,3,4,5,6,7,8,9,10,11,12,13,14,15,16,17,18,19,20,21,21,21,21,21,21,21,21,21,21,21,
36,48,51,52,37,38,41,42,43,46,47,49,39,44,53,50,40,45,45,45,45,45,45,45,45,45,45,45,58,4,22,18,0,1,2,3,4,5,6,7,8,9,10,11,12,13,14,15,16,17,18,19,20,21,21,21,21,21,21,21,21,21,21,21,
36,48,51,52,37,38,41,42,43,46,47,49,39,44,53,50,40,45,45,45,45,45,45,45,45,45,45,45,58,4,22,18,0,1,2,3,4,5,6,7,8,9,10,11,12,13,14,15,16,17,18,19,20,21,21,21,21,21,21,21,21,21,21,21,
54,66,69,70,55,56,59,60,61,64,65,67,57,62,71,68,58,63,63,63,63,63,63,63,63,63,63,63,4,22,40,18,0,1,2,3,4,5,6,7,8,9,10,11,12,13,14,15,16,17,18,19,20,21,21,21,21,21,21,21,21,21,21,21,
54,66,69,70,55,56,59,60,61,64,65,67,57,62,71,68,58,63,63,63,63,63,63,63,63,63,63,63,4,22,40,18,0,1,2,3,4,5,6,7,8,9,10,11,12,13,14,15,16,17,18,19,20,21,21,21,21,21,21,21,21,21,21,21,
0,15,16,1,2,5,6,7,10,11,12,13,3,8,17,14,4,9,9,9,9,9,9,9,9,9,9,9,22,40,58,18,0,1,2,3,4,5,6,7,8,9,10,11,12,13,14,15,16,17,18,19,20,21,21,21,21,21,21,21,21,21,21,21,
0,15,16,1,2,5,6,7,10,11,12,13,3,8,17,14,4,9,9,9,9,9,9,9,9,9,9,9,22,40,58,18,0,1,2,3,4,5,6,7,8,9,10,11,12,13,14,15,16,17,18,19,20,21,21,21,21,21,21,21,21,21,21,21,
18,33,34,19,20,23,24,25,28,29,30,31,21,26,35,32,22,27,27,27,27,27,27,27,27,27,27,27,40,58,4,18,0,1,2,3,4,5,6,7,8,9,10,11,12,13,14,15,16,17,18,19,20,21,21,21,21,21,21,21,21,21,21,21,
18,33,34,19,20,23,24,25,28,29,30,31,21,26,35,32,22,27,27,27,27,27,27,27,27,27,27,27,40,58,4,18,0,1,2,3,4,5,6,7,8,9,10,11,12,13,14,15,16,17,18,19,20,21,21,21,21,21,21,21,21,21,21,21,
36,51,52,37,38,41,42,43,46,47,48,49,39,44,53,50,40,45,45,45,45,45,45,45,45,45,45,45,58,4,22,18,0,1,2,3,4,5,6,7,8,9,10,11,12,13,14,15,16,17,18,19,20,21,21,21,21,21,21,21,21,21,21,21,
36,51,52,37,38,41,42,43,46,47,48,49,39,44,53,50,40,45,45,45,45,45,45,45,45,45,45,45,58,4,22,18,0,1,2,3,4,5,6,7,8,9,10,11,12,13,14,15,16,17,18,19,20,21,21,21,21,21,21,21,21,21,21,21,
54,69,70,55,56,59,60,61,64,65,66,67,57,62,71,68,58,63,63,63,63,63,63,63,63,63,63,63,4,22,40,18,0,1,2,3,4,5,6,7,8,9,10,11,12,13,14,15,16,17,18,19,20,21,21,21,21,21,21,21,21,21,21,21,
54,69,70,55,56,59,60,61,64,65,66,67,57,62,71,68,58,63,63,63,63,63,63,63,63,63,63,63,4,22,40,18,0,1,2,3,4,5,6,7,8,9,10,11,12,13,14,15,16,17,18,19,20,21,21,21,21,21,21,21,21,21,21,21,
5,1,2,6,7,10,11,12,15,16,0,8,13,3,17,9,14,4,4,4,4,4,4,4,4,4,4,4,27,45,63,18,21,22,23,24,25,26,27,28,29,30,31,32,33,34,35,36,37,38,39,40,41,42,42,42,42,42,42,42,42,42,42,42,
5,1,2,6,7,10,11,12,15,16,0,8,13,3,17,9,14,4,4,4,4,4,4,4,4,4,4,4,27,45,63,18,21,22,23,24,25,26,27,28,29,30,31,32,33,34,35,36,37,38,39,40,41,42,42,42,42,42,42,42,42,42,42,42,
23,19,20,24,25,28,29,30,33,34,18,26,31,21,35,27,32,22,22,22,22,22,22,22,22,22,22,22,45,63,9,18,21,22,23,24,25,26,27,28,29,30,31,32,33,34,35,36,37,38,39,40,41,42,42,42,42,42,42,42,42,42,42,42,
23,19,20,24,25,28,29,30,33,34,18,26,31,21,35,27,32,22,22,22,22,22,22,22,22,22,22,22,45,63,9,18,21,22,23,24,25,26,27,28,29,30,31,32,33,34,35,36,37,38,39,40,41,42,42,42,42,42,42,42,42,42,42,42,
41,37,38,42,43,46,47,48,51,52,36,44,49,39,53,45,50,40,40,40,40,40,40,40,40,40,40,40,63,9,27,18,21,22,23,24,25,26,27,28,29,30,31,32,33,34,35,36,37,38,39,40,41,42,42,42,42,42,42,42,42,42,42,42,
41,37,38,42,43,46,47,48,51,52,36,44,49,39,53,45,50,40,40,40,40,40,40,40,40,40,40,40,63,9,27,18,21,22,23,24,25,26,27,28,29,30,31,32,33,34,35,36,37,38,39,40,41,42,42,42,42,42,42,42,42,42,42,42,
59,55,56,60,61,64,65,66,69,70,54,62,67,57,71,63,68,58,58,58,58,58,58,58,58,58,58,58,9,27,45,18,21,22,23,24,25,26,27,28,29,30,31,32,33,34,35,36,37,38,39,40,41,42,42,42,42,42,42,42,42,42,42,42,
59,55,56,60,61,64,65,66,69,70,54,62,67,57,71,63,68,58,58,58,58,58,58,58,58,58,58,58,9,27,45,18,21,22,23,24,25,26,27,28,29,30,31,32,33,34,35,36,37,38,39,40,41,42,42,42,42,42,42,42,42,42,42,42,
5,2,6,7,10,11,12,15,16,0,1,8,13,3,17,9,14,4,4,4,4,4,4,4,4,4,4,4,27,45,63,18,21,22,23,24,25,26,27,28,29,30,31,32,33,34,35,36,37,38,39,40,41,42,42,42,42,42,42,42,42,42,42,42,
5,2,6,7,10,11,12,15,16,0,1,8,13,3,17,9,14,4,4,4,4,4,4,4,4,4,4,4,27,45,63,18,21,22,23,24,25,26,27,28,29,30,31,32,33,34,35,36,37,38,39,40,41,42,42,42,42,42,42,42,42,42,42,42,
23,20,24,25,28,29,30,33,34,18,19,26,31,21,35,27,32,22,22,22,22,22,22,22,22,22,22,22,45,63,9,18,21,22,23,24,25,26,27,28,29,30,31,32,33,34,35,36,37,38,39,40,41,42,42,42,42,42,42,42,42,42,42,42,
23,20,24,25,28,29,30,33,34,18,19,26,31,21,35,27,32,22,22,22,22,22,22,22,22,22,22,22,45,63,9,18,21,22,23,24,25,26,27,28,29,30,31,32,33,34,35,36,37,38,39,40,41,42,42,42,42,42,42,42,42,42,42,42,
41,38,42,43,46,47,48,51,52,36,37,44,49,39,53,45,50,40,40,40,40,40,40,40,40,40,40,40,63,9,27,18,21,22,23,24,25,26,27,28,29,30,31,32,33,34,35,36,37,38,39,40,41,42,42,42,42,42,42,42,42,42,42,42,
41,38,42,43,46,47,48,51,52,36,37,44,49,39,53,45,50,40,40,40,40,40,40,40,40,40,40,40,63,9,27,18,21,22,23,24,25,26,27,28,29,30,31,32,33,34,35,36,37,38,39,40,41,42,42,42,42,42,42,42,42,42,42,42,
59,56,60,61,64,65,66,69,70,54,55,62,67,57,71,63,68,58,58,58,58,58,58,58,58,58,58,58,9,27,45,18,21,22,23,24,25,26,27,28,29,30,31,32,33,34,35,36,37,38,39,40,41,42,42,42,42,42,42,42,42,42,42,42,
59,56,60,61,64,65,66,69,70,54,55,62,67,57,71,63,68,58,58,58,58,58,58,58,58,58,58,58,9,27,45,18,21,22,23,24,25,26,27,28,29,30,31,32,33,34,35,36,37,38,39,40,41,42,42,42,42,42,42,42,42,42,42,42,
5,6,7,10,11,12,15,16,0,1,2,8,13,3,17,9,14,4,4,4,4,4,4,4,4,4,4,4,27,45,63,18,21,22,23,24,25,26,27,28,29,30,31,32,33,34,35,36,37,38,39,40,41,42,42,42,42,42,42,42,42,42,42,42,
5,6,7,10,11,12,15,16,0,1,2,8,13,3,17,9,14,4,4,4,4,4,4,4,4,4,4,4,27,45,63,18,21,22,23,24,25,26,27,28,29,30,31,32,33,34,35,36,37,38,39,40,41,42,42,42,42,42,42,42,42,42,42,42,
23,24,25,28,29,30,33,34,18,19,20,26,31,21,35,27,32,22,22,22,22,22,22,22,22,22,22,22,45,63,9,18,21,22,23,24,25,26,27,28,29,30,31,32,33,34,35,36,37,38,39,40,41,42,42,42,42,42,42,42,42,42,42,42,
23,24,25,28,29,30,33,34,18,19,20,26,31,21,35,27,32,22,22,22,22,22,22,22,22,22,22,22,45,63,9,18,21,22,23,24,25,26,27,28,29,30,31,32,33,34,35,36,37,38,39,40,41,42,42,42,42,42,42,42,42,42,42,42,
41,42,43,46,47,48,51,52,36,37,38,44,49,39,53,45,50,40,40,40,40,40,40,40,40,40,40,40,63,9,27,18,21,22,23,24,25,26,27,28,29,30,31,32,33,34,35,36,37,38,39,40,41,42,42,42,42,42,42,42,42,42,42,42,
41,42,43,46,47,48,51,52,36,37,38,44,49,39,53,45,50,40,40,40,40,40,40,40,40,40,40,40,63,9,27,18,21,22,23,24,25,26,27,28,29,30,31,32,33,34,35,36,37,38,39,40,41,42,42,42,42,42,42,42,42,42,42,42,
59,60,61,64,65,66,69,70,54,55,56,62,67,57,71,63,68,58,58,58,58,58,58,58,58,58,58,58,9,27,45,18,21,22,23,24,25,26,27,28,29,30,31,32,33,34,35,36,37,38,39,40,41,42,42,42,42,42,42,42,42,42,42,42,
59,60,61,64,65,66,69,70,54,55,56,62,67,57,71,63,68,58,58,58,58,58,58,58,58,58,58,58,9,27,45,18,21,22,23,24,25,26,27,28,29,30,31,32,33,34,35,36,37,38,39,40,41,42,42,42,42,42,42,42,42,42,42,42,
10,7,11,12,15,16,0,1,2,5,6,13,3,8,17,14,4,9,9,9,9,9,9,9,9,9,9,9,27,45,63,18,21,22,23,24,25,26,27,28,29,30,31,32,33,34,35,36,37,38,39,40,41,42,42,42,42,42,42,42,42,42,42,42,
10,7,11,12,15,16,0,1,2,5,6,13,3,8,17,14,4,9,9,9,9,9,9,9,9,9,9,9,27,45,63,18,21,22,23,24,25,26,27,28,29,30,31,32,33,34,35,36,37,38,39,40,41,42,42,42,42,42,42,42,42,42,42,42,
28,25,29,30,33,34,18,19,20,23,24,31,21,26,35,32,22,27,27,27,27,27,27,27,27,27,27,27,45,63,9,18,21,22,23,24,25,26,27,28,29,30,31,32,33,34,35,36,37,38,39,40,41,42,42,42,42,42,42,42,42,42,42,42,
28,25,29,30,33,34,18,19,20,23,24,31,21,26,35,32,22,27,27,27,27,27,27,27,27,27,27,27,45,63,9,18,21,22,23,24,25,26,27,28,29,30,31,32,33,34,35,36,37,38,39,40,41,42,42,42,42,42,42,42,42,42,42,42,
46,43,47,48,51,52,36,37,38,41,42,49,39,44,53,50,40,45,45,45,45,45,45,45,45,45,45,45,63,9,27,18,21,22,23,24,25,26,27,28,29,30,31,32,33,34,35,36,37,38,39,40,41,42,42,42,42,42,42,42,42,42,42,42,
46,43,47,48,51,52,36,37,38,41,42,49,39,44,53,50,40,45,45,45,45,45,45,45,45,45,45,45,63,9,27,18,21,22,23,24,25,26,27,28,29,30,31,32,33,34,35,36,37,38,39,40,41,42,42,42,42,42,42,42,42,42,42,42,
64,61,65,66,69,70,54,55,56,59,60,67,57,62,71,68,58,63,63,63,63,63,63,63,63,63,63,63,9,27,45,18,21,22,23,24,25,26,27,28,29,30,31,32,33,34,35,36,37,38,39,40,41,42,42,42,42,42,42,42,42,42,42,42,
64,61,65,66,69,70,54,55,56,59,60,67,57,62,71,68,58,63,63,63,63,63,63,63,63,63,63,63,9,27,45,18,21,22,23,24,25,26,27,28,29,30,31,32,33,34,35,36,37,38,39,40,41,42,42,42,42,42,42,42,42,42,42,42,
10,11,12,15,16,0,1,2,5,6,7,13,3,8,17,14,4,9,9,9,9,9,9,9,9,9,9,9,27,45,63,18,21,22,23,24,25,26,27,28,29,30,31,32,33,34,35,36,37,38,39,40,41,42,42,42,42,42,42,42,42,42,42,42,
10,11,12,15,16,0,1,2,5,6,7,13,3,8,17,14,4,9,9,9,9,9,9,9,9,9,9,9,27,45,63,18,21,22,23,24,25,26,27,28,29,30,31,32,33,34,35,36,37,38,39,40,41,42,42,42,42,42,42,42,42,42,42,42,
28,29,30,33,34,18,19,20,23,24,25,31,21,26,35,32,22,27,27,27,27,27,27,27,27,27,27,27,45,63,9,18,21,22,23,24,25,26,27,28,29,30,31,32,33,34,35,36,37,38,39,40,41,42,42,42,42,42,42,42,42,42,42,42,
28,29,30,33,34,18,19,20,23,24,25,31,21,26,35,32,22,27,27,27,27,27,27,27,27,27,27,27,45,63,9,18,21,22,23,24,25,26,27,28,29,30,31,32,33,34,35,36,37,38,39,40,41,42,42,42,42,42,42,42,42,42,42,42,
46,47,48,51,52,36,37,38,41,42,43,49,39,44,53,50,40,45,45,45,45,45,45,45,45,45,45,45,63,9,27,18,21,22,23,24,25,26,27,28,29,30,31,32,33,34,35,36,37,38,39,40,41,42,42,42,42,42,42,42,42,42,42,42,
46,47,48,51,52,36,37,38,41,42,43,49,39,44,53,50,40,45,45,45,45,45,45,45,45,45,45,45,63,9,27,18,21,22,23,24,25,26,27,28,29,30,31,32,33,34,35,36,37,38,39,40,41,42,42,42,42,42,42,42,42,42,42,42,
64,65,66,69,70,54,55,56,59,60,61,67,57,62,71,68,58,63,63,63,63,63,63,63,63,63,63,63,9,27,45,18,21,22,23,24,25,26,27,28,29,30,31,32,33,34,35,36,37,38,39,40,41,42,42,42,42,42,42,42,42,42,42,42,
64,65,66,69,70,54,55,56,59,60,61,67,57,62,71,68,58,63,63,63,63,63,63,63,63,63,63,63,9,27,45,18,21,22,23,24,25,26,27,28,29,30,31,32,33,34,35,36,37,38,39,40,41,42,42,42,42,42,42,42,42,42,42,42,
0,11,12,15,16,1,2,5,6,7,10,13,3,8,17,14,4,9,9,9,9,9,9,9,9,9,9,9,27,45,63,18,21,22,23,24,25,26,27,28,29,30,31,32,33,34,35,36,37,38,39,40,41,42,42,42,42,42,42,42,42,42,42,42,
0,11,12,15,16,1,2,5,6,7,10,13,3,8,17,14,4,9,9,9,9,9,9,9,9,9,9,9,27,45,63,18,21,22,23,24,25,26,27,28,29,30,31,32,33,34,35,36,37,38,39,40,41,42,42,42,42,42,42,42,42,42,42,42,
18,29,30,33,34,19,20,23,24,25,28,31,21,26,35,32,22,27,27,27,27,27,27,27,27,27,27,27,45,63,9,18,21,22,23,24,25,26,27,28,29,30,31,32,33,34,35,36,37,38,39,40,41,42,42,42,42,42,42,42,42,42,42,42,
18,29,30,33,34,19,20,23,24,25,28,31,21,26,35,32,22,27,27,27,27,27,27,27,27,27,27,27,45,63,9,18,21,22,23,24,25,26,27,28,29,30,31,32,33,34,35,36,37,38,39,40,41,42,42,42,42,42,42,42,42,42,42,42,
36,47,48,51,52,37,38,41,42,43,46,49,39,44,53,50,40,45,45,45,45,45,45,45,45,45,45,45,63,9,27,18,21,22,23,24,25,26,27,28,29,30,31,32,33,34,35,36,37,38,39,40,41,42,42,42,42,42,42,42,42,42,42,42,
36,47,48,51,52,37,38,41,42,43,46,49,39,44,53,50,40,45,45,45,45,45,45,45,45,45,45,45,63,9,27,18,21,22,23,24,25,26,27,28,29,30,31,32,33,34,35,36,37,38,39,40,41,42,42,42,42,42,42,42,42,42,42,42,
54,65,66,69,70,55,56,59,60,61,64,67,57,62,71,68,58,63,63,63,63,63,63,63,63,63,63,63,9,27,45,18,21,22,23,24,25,26,27,28,29,30,31,32,33,34,35,36,37,38,39,40,41,42,42,42,42,42,42,42,42,42,42,42,
54,65,66,69,70,55,56,59,60,61,64,67,57,62,71,68,58,63,63,63,63,63,63,63,63,63,63,63,9,27,45,18,21,22,23,24,25,26,27,28,29,30,31,32,33,34,35,36,37,38,39,40,41,42,42,42,42,42,42,42,42,42,42,42,
0,15,16,1,2,5,6,7,10,11,12,3,8,13,17,4,9,14,14,14,14,14,14,14,14,14,14,14,27,45,63,18,21,22,23,24,25,26,27,28,29,30,31,32,33,34,35,36,37,38,39,40,41,42,42,42,42,42,42,42,42,42,42,42,
0,15,16,1,2,5,6,7,10,11,12,3,8,13,17,4,9,14,14,14,14,14,14,14,14,14,14,14,27,45,63,18,21,22,23,24,25,26,27,28,29,30,31,32,33,34,35,36,37,38,39,40,41,42,42,42,42,42,42,42,42,42,42,42,
18,33,34,19,20,23,24,25,28,29,30,21,26,31,35,22,27,32,32,32,32,32,32,32,32,32,32,32,45,63,9,18,21,22,23,24,25,26,27,28,29,30,31,32,33,34,35,36,37,38,39,40,41,42,42,42,42,42,42,42,42,42,42,42,
18,33,34,19,20,23,24,25,28,29,30,21,26,31,35,22,27,32,32,32,32,32,32,32,32,32,32,32,45,63,9,18,21,22,23,24,25,26,27,28,29,30,31,32,33,34,35,36,37,38,39,40,41,42,42,42,42,42,42,42,42,42,42,42,
36,51,52,37,38,41,42,43,46,47,48,39,44,49,53,40,45,50,50,50,50,50,50,50,50,50,50,50,63,9,27,18,21,22,23,24,25,26,27,28,29,30,31,32,33,34,35,36,37,38,39,40,41,42,42,42,42,42,42,42,42,42,42,42,
36,51,52,37,38,41,42,43,46,47,48,39,44,49,53,40,45,50,50,50,50,50,50,50,50,50,50,50,63,9,27,18,21,22,23,24,25,26,27,28,29,30,31,32,33,34,35,36,37,38,39,40,41,42,42,42,42,42,42,42,42,42,42,42,
54,69,70,55,56,59,60,61,64,65,66,57,62,67,71,58,63,68,68,68,68,68,68,68,68,68,68,68,9,27,45,18,21,22,23,24,25,26,27,28,29,30,31,32,33,34,35,36,37,38,39,40,41,42,42,42,42,42,42,42,42,42,42,42,
54,69,70,55,56,59,60,61,64,65,66,57,62,67,71,58,63,68,68,68,68,68,68,68,68,68,68,68,9,27,45,18,21,22,23,24,25,26,27,28,29,30,31,32,33,34,35,36,37,38,39,40,41,42,42,42,42,42,42,42,42,42,42,42,
0,16,1,2,5,6,7,10,11,12,15,3,8,13,17,4,9,14,14,14,14,14,14,14,14,14,14,14,27,45,63,18,21,22,23,24,25,26,27,28,29,30,31,32,33,34,35,36,37,38,39,40,41,42,42,42,42,42,42,42,42,42,42,42,
0,16,1,2,5,6,7,10,11,12,15,3,8,13,17,4,9,14,14,14,14,14,14,14,14,14,14,14,27,45,63,18,21,22,23,24,25,26,27,28,29,30,31,32,33,34,35,36,37,38,39,40,41,42,42,42,42,42,42,42,42,42,42,42,
18,34,19,20,23,24,25,28,29,30,33,21,26,31,35,22,27,32,32,32,32,32,32,32,32,32,32,32,45,63,9,18,21,22,23,24,25,26,27,28,29,30,31,32,33,34,35,36,37,38,39,40,41,42,42,42,42,42,42,42,42,42,42,42,
18,34,19,20,23,24,25,28,29,30,33,21,26,31,35,22,27,32,32,32,32,32,32,32,32,32,32,32,45,63,9,18,21,22,23,24,25,26,27,28,29,30,31,32,33,34,35,36,37,38,39,40,41,42,42,42,42,42,42,42,42,42,42,42,
36,52,37,38,41,42,43,46,47,48,51,39,44,49,53,40,45,50,50,50,50,50,50,50,50,50,50,50,63,9,27,18,21,22,23,24,25,26,27,28,29,30,31,32,33,34,35,36,37,38,39,40,41,42,42,42,42,42,42,42,42,42,42,42,
36,52,37,38,41,42,43,46,47,48,51,39,44,49,53,40,45,50,50,50,50,50,50,50,50,50,50,50,63,9,27,18,21,22,23,24,25,26,27,28,29,30,31,32,33,34,35,36,37,38,39,40,41,42,42,42,42,42,42,42,42,42,42,42,
54,70,55,56,59,60,61,64,65,66,69,57,62,67,71,58,63,68,68,68,68,68,68,68,68,68,68,68,9,27,45,18,21,22,23,24,25,26,27,28,29,30,31,32,33,34,35,36,37,38,39,40,41,42,42,42,42,42,42,42,42,42,42,42,
54,70,55,56,59,60,61,64,65,66,69,57,62,67,71,58,63,68,68,68,68,68,68,68,68,68,68,68,9,27,45,18,21,22,23,24,25,26,27,28,29,30,31,32,33,34,35,36,37,38,39,40,41,42,42,42,42,42,42,42,42,42,42,42,
5,2,6,7,10,11,12,15,16,0,1,13,3,8,17,14,4,9,9,9,9,9,9,9,9,9,9,9,32,50,68,18,42,43,44,45,46,47,48,49,50,51,52,53,54,55,56,57,58,59,60,61,62,63,63,63,63,63,63,63,63,63,63,63,
5,2,6,7,10,11,12,15,16,0,1,13,3,8,17,14,4,9,9,9,9,9,9,9,9,9,9,9,32,50,68,18,42,43,44,45,46,47,48,49,50,51,52,53,54,55,56,57,58,59,60,61,62,63,63,63,63,63,63,63,63,63,63,63,
23,20,24,25,28,29,30,33,34,18,19,31,21,26,35,32,22,27,27,27,27,27,27,27,27,27,27,27,50,68,14,18,42,43,44,45,46,47,48,49,50,51,52,53,54,55,56,57,58,59,60,61,62,63,63,63,63,63,63,63,63,63,63,63,
23,20,24,25,28,29,30,33,34,18,19,31,21,26,35,32,22,27,27,27,27,27,27,27,27,27,27,27,50,68,14,18,42,43,44,45,46,47,48,49,50,51,52,53,54,55,56,57,58,59,60,61,62,63,63,63,63,63,63,63,63,63,63,63,
41,38,42,43,46,47,48,51,52,36,37,49,39,44,53,50,40,45,45,45,45,45,45,45,45,45,45,45,68,14,32,18,42,43,44,45,46,47,48,49,50,51,52,53,54,55,56,57,58,59,60,61,62,63,63,63,63,63,63,63,63,63,63,63,
41,38,42,43,46,47,48,51,52,36,37,49,39,44,53,50,40,45,45,45,45,45,45,45,45,45,45,45,68,14,32,18,42,43,44,45,46,47,48,49,50,51,52,53,54,55,56,57,58,59,60,61,62,63,63,63,63,63,63,63,63,63,63,63,
59,56,60,61,64,65,66,69,70,54,55,67,57,62,71,68,58,63,63,63,63,63,63,63,63,63,63,63,14,32,50,18,42,43,44,45,46,47,48,49,50,51,52,53,54,55,56,57,58,59,60,61,62,63,63,63,63,63,63,63,63,63,63,63,
59,56,60,61,64,65,66,69,70,54,55,67,57,62,71,68,58,63,63,63,63,63,63,63,63,63,63,63,14,32,50,18,42,43,44,45,46,47,48,49,50,51,52,53,54,55,56,57,58,59,60,61,62,63,63,63,63,63,63,63,63,63,63,63,
5,6,7,10,11,12,15,16,0,1,2,13,3,8,17,14,4,9,9,9,9,9,9,9,9,9,9,9,32,50,68,18,42,43,44,45,46,47,48,49,50,51,52,53,54,55,56,57,58,59,60,61,62,63,63,63,63,63,63,63,63,63,63,63,
5,6,7,10,11,12,15,16,0,1,2,13,3,8,17,14,4,9,9,9,9,9,9,9,9,9,9,9,32,50,68,18,42,43,44,45,46,47,48,49,50,51,52,53,54,55,56,57,58,59,60,61,62,63,63,63,63,63,63,63,63,63,63,63,
23,24,25,28,29,30,33,34,18,19,20,31,21,26,35,32,22,27,27,27,27,27,27,27,27,27,27,27,50,68,14,18,42,43,44,45,46,47,48,49,50,51,52,53,54,55,56,57,58,59,60,61,62,63,63,63,63,63,63,63,63,63,63,63,
23,24,25,28,29,30,33,34,18,19,20,31,21,26,35,32,22,27,27,27,27,27,27,27,27,27,27,27,50,68,14,18,42,43,44,45,46,47,48,49,50,51,52,53,54,55,56,57,58,59,60,61,62,63,63,63,63,63,63,63,63,63,63,63,
41,42,43,46,47,48,51,52,36,37,38,49,39,44,53,50,40,45,45,45,45,45,45,45,45,45,45,45,68,14,32,18,42,43,44,45,46,47,48,49,50,51,52,53,54,55,56,57,58,59,60,61,62,63,63,63,63,63,63,63,63,63,63,63,
41,42,43,46,47,48,51,52,36,37,38,49,39,44,53,50,40,45,45,45,45,45,45,45,45,45,45,45,68,14,32,18,42,43,44,45,46,47,48,49,50,51,52,53,54,55,56,57,58,59,60,61,62,63,63,63,63,63,63,63,63,63,63,63,
59,60,61,64,65,66,69,70,54,55,56,67,57,62,71,68,58,63,63,63,63,63,63,63,63,63,63,63,14,32,50,18,42,43,44,45,46,47,48,49,50,51,52,53,54,55,56,57,58,59,60,61,62,63,63,63,63,63,63,63,63,63,63,63,
59,60,61,64,65,66,69,70,54,55,56,67,57,62,71,68,58,63,63,63,63,63,63,63,63,63,63,63,14,32,50,18,42,43,44,45,46,47,48,49,50,51,52,53,54,55,56,57,58,59,60,61,62,63,63,63,63,63,63,63,63,63,63,63,
10,6,7,11,12,15,16,0,1,2,5,13,3,8,17,14,4,9,9,9,9,9,9,9,9,9,9,9,32,50,68,18,42,43,44,45,46,47,48,49,50,51,52,53,54,55,56,57,58,59,60,61,62,63,63,63,63,63,63,63,63,63,63,63,
10,6,7,11,12,15,16,0,1,2,5,13,3,8,17,14,4,9,9,9,9,9,9,9,9,9,9,9,32,50,68,18,42,43,44,45,46,47,48,49,50,51,52,53,54,55,56,57,58,59,60,61,62,63,63,63,63,63,63,63,63,63,63,63,
28,24,25,29,30,33,34,18,19,20,23,31,21,26,35,32,22,27,27,27,27,27,27,27,27,27,27,27,50,68,14,18,42,43,44,45,46,47,48,49,50,51,52,53,54,55,56,57,58,59,60,61,62,63,63,63,63,63,63,63,63,63,63,63,
28,24,25,29,30,33,34,18,19,20,23,31,21,26,35,32,22,27,27,27,27,27,27,27,27,27,27,27,50,68,14,18,42,43,44,45,46,47,48,49,50,51,52,53,54,55,56,57,58,59,60,61,62,63,63,63,63,63,63,63,63,63,63,63,
46,42,43,47,48,51,52,36,37,38,41,49,39,44,53,50,40,45,45,45,45,45,45,45,45,45,45,45,68,14,32,18,42,43,44,45,46,47,48,49,50,51,52,53,54,55,56,57,58,59,60,61,62,63,63,63,63,63,63,63,63,63,63,63,
46,42,43,47,48,51,52,36,37,38,41,49,39,44,53,50,40,45,45,45,45,45,45,45,45,45,45,45,68,14,32,18,42,43,44,45,46,47,48,49,50,51,52,53,54,55,56,57,58,59,60,61,62,63,63,63,63,63,63,63,63,63,63,63,
64,60,61,65,66,69,70,54,55,56,59,67,57,62,71,68,58,63,63,63,63,63,63,63,63,63,63,63,14,32,50,18,42,43,44,45,46,47,48,49,50,51,52,53,54,55,56,57,58,59,60,61,62,63,63,63,63,63,63,63,63,63,63,63,
64,60,61,65,66,69,70,54,55,56,59,67,57,62,71,68,58,63,63,63,63,63,63,63,63,63,63,63,14,32,50,18,42,43,44,45,46,47,48,49,50,51,52,53,54,55,56,57,58,59,60,61,62,63,63,63,63,63,63,63,63,63,63,63,
10,11,12,15,16,0,1,2,5,6,7,3,8,13,17,4,9,14,14,14,14,14,14,14,14,14,14,14,32,50,68,18,42,43,44,45,46,47,48,49,50,51,52,53,54,55,56,57,58,59,60,61,62,63,63,63,63,63,63,63,63,63,63,63,
10,11,12,15,16,0,1,2,5,6,7,3,8,13,17,4,9,14,14,14,14,14,14,14,14,14,14,14,32,50,68,18,42,43,44,45,46,47,48,49,50,51,52,53,54,55,56,57,58,59,60,61,62,63,63,63,63,63,63,63,63,63,63,63,
28,29,30,33,34,18,19,20,23,24,25,21,26,31,35,22,27,32,32,32,32,32,32,32,32,32,32,32,50,68,14,18,42,43,44,45,46,47,48,49,50,51,52,53,54,55,56,57,58,59,60,61,62,63,63,63,63,63,63,63,63,63,63,63,
28,29,30,33,34,18,19,20,23,24,25,21,26,31,35,22,27,32,32,32,32,32,32,32,32,32,32,32,50,68,14,18,42,43,44,45,46,47,48,49,50,51,52,53,54,55,56,57,58,59,60,61,62,63,63,63,63,63,63,63,63,63,63,63,
46,47,48,51,52,36,37,38,41,42,43,39,44,49,53,40,45,50,50,50,50,50,50,50,50,50,50,50,68,14,32,18,42,43,44,45,46,47,48,49,50,51,52,53,54,55,56,57,58,59,60,61,62,63,63,63,63,63,63,63,63,63,63,63,
46,47,48,51,52,36,37,38,41,42,43,39,44,49,53,40,45,50,50,50,50,50,50,50,50,50,50,50,68,14,32,18,42,43,44,45,46,47,48,49,50,51,52,53,54,55,56,57,58,59,60,61,62,63,63,63,63,63,63,63,63,63,63,63,
64,65,66,69,70,54,55,56,59,60,61,57,62,67,71,58,63,68,68,68,68,68,68,68,68,68,68,68,14,32,50,18,42,43,44,45,46,47,48,49,50,51,52,53,54,55,56,57,58,59,60,61,62,63,63,63,63,63,63,63,63,63,63,63,
64,65,66,69,70,54,55,56,59,60,61,57,62,67,71,58,63,68,68,68,68,68,68,68,68,68,68,68,14,32,50,18,42,43,44,45,46,47,48,49,50,51,52,53,54,55,56,57,58,59,60,61,62,63,63,63,63,63,63,63,63,63,63,63,
0,11,12,15,16,1,2,5,6,7,10,3,8,13,17,4,9,14,14,14,14,14,14,14,14,14,14,14,32,50,68,18,42,43,44,45,46,47,48,49,50,51,52,53,54,55,56,57,58,59,60,61,62,63,63,63,63,63,63,63,63,63,63,63,
0,11,12,15,16,1,2,5,6,7,10,3,8,13,17,4,9,14,14,14,14,14,14,14,14,14,14,14,32,50,68,18,42,43,44,45,46,47,48,49,50,51,52,53,54,55,56,57,58,59,60,61,62,63,63,63,63,63,63,63,63,63,63,63,
18,29,30,33,34,19,20,23,24,25,28,21,26,31,35,22,27,32,32,32,32,32,32,32,32,32,32,32,50,68,14,18,42,43,44,45,46,47,48,49,50,51,52,53,54,55,56,57,58,59,60,61,62,63,63,63,63,63,63,63,63,63,63,63,
18,29,30,33,34,19,20,23,24,25,28,21,26,31,35,22,27,32,32,32,32,32,32,32,32,32,32,32,50,68,14,18,42,43,44,45,46,47,48,49,50,51,52,53,54,55,56,57,58,59,60,61,62,63,63,63,63,63,63,63,63,63,63,63,
36,47,48,51,52,37,38,41,42,43,46,39,44,49,53,40,45,50,50,50,50,50,50,50,50,50,50,50,68,14,32,18,42,43,44,45,46,47,48,49,50,51,52,53,54,55,56,57,58,59,60,61,62,63,63,63,63,63,63,63,63,63,63,63,
36,47,48,51,52,37,38,41,42,43,46,39,44,49,53,40,45,50,50,50,50,50,50,50,50,50,50,50,68,14,32,18,42,43,44,45,46,47,48,49,50,51,52,53,54,55,56,57,58,59,60,61,62,63,63,63,63,63,63,63,63,63,63,63,
54,65,66,69,70,55,56,59,60,61,64,57,62,67,71,58,63,68,68,68,68,68,68,68,68,68,68,68,14,32,50,18,42,43,44,45,46,47,48,49,50,51,52,53,54,55,56,57,58,59,60,61,62,63,63,63,63,63,63,63,63,63,63,63,
54,65,66,69,70,55,56,59,60,61,64,57,62,67,71,58,63,68,68,68,68,68,68,68,68,68,68,68,14,32,50,18,42,43,44,45,46,47,48,49,50,51,52,53,54,55,56,57,58,59,60,61,62,63,63,63,63,63,63,63,63,63,63,63,
0,12,15,16,1,2,5,6,7,10,11,3,8,13,17,4,9,14,14,14,14,14,14,14,14,14,14,14,32,50,68,18,42,43,44,45,46,47,48,49,50,51,52,53,54,55,56,57,58,59,60,61,62,63,63,63,63,63,63,63,63,63,63,63,
0,12,15,16,1,2,5,6,7,10,11,3,8,13,17,4,9,14,14,14,14,14,14,14,14,14,14,14,32,50,68,18,42,43,44,45,46,47,48,49,50,51,52,53,54,55,56,57,58,59,60,61,62,63,63,63,63,63,63,63,63,63,63,63,
18,30,33,34,19,20,23,24,25,28,29,21,26,31,35,22,27,32,32,32,32,32,32,32,32,32,32,32,50,68,14,18,42,43,44,45,46,47,48,49,50,51,52,53,54,55,56,57,58,59,60,61,62,63,63,63,63,63,63,63,63,63,63,63,
18,30,33,34,19,20,23,24,25,28,29,21,26,31,35,22,27,32,32,32,32,32,32,32,32,32,32,32,50,68,14,18,42,43,44,45,46,47,48,49,50,51,52,53,54,55,56,57,58,59,60,61,62,63,63,63,63,63,63,63,63,63,63,63,
36,48,51,52,37,38,41,42,43,46,47,39,44,49,53,40,45,50,50,50,50,50,50,50,50,50,50,50,68,14,32,18,42,43,44,45,46,47,48,49,50,51,52,53,54,55,56,57,58,59,60,61,62,63,63,63,63,63,63,63,63,63,63,63,
36,48,51,52,37,38,41,42,43,46,47,39,44,49,53,40,45,50,50,50,50,50,50,50,50,50,50,50,68,14,32,18,42,43,44,45,46,47,48,49,50,51,52,53,54,55,56,57,58,59,60,61,62,63,63,63,63,63,63,63,63,63,63,63,
54,66,69,70,55,56,59,60,61,64,65,57,62,67,71,58,63,68,68,68,68,68,68,68,68,68,68,68,14,32,50,18,42,43,44,45,46,47,48,49,50,51,52,53,54,55,56,57,58,59,60,61,62,63,63,63,63,63,63,63,63,63,63,63,
54,66,69,70,55,56,59,60,61,64,65,57,62,67,71,58,63,68,68,68,68,68,68,68,68,68,68,68,14,32,50,18,42,43,44,45,46,47,48,49,50,51,52,53,54,55,56,57,58,59,60,61,62,63,63,63,63,63,63,63,63,63,63,63,
0,16,1,2,5,6,7,10,11,12,15,8,13,3,17,9,14,4,4,4,4,4,4,4,4,4,4,4,32,50,68,18,42,43,44,45,46,47,48,49,50,51,52,53,54,55,56,57,58,59,60,61,62,63,63,63,63,63,63,63,63,63,63,63,
0,16,1,2,5,6,7,10,11,12,15,8,13,3,17,9,14,4,4,4,4,4,4,4,4,4,4,4,32,50,68,18,42,43,44,45,46,47,48,49,50,51,52,53,54,55,56,57,58,59,60,61,62,63,63,63,63,63,63,63,63,63,63,63,
18,34,19,20,23,24,25,28,29,30,33,26,31,21,35,27,32,22,22,22,22,22,22,22,22,22,22,22,50,68,14,18,42,43,44,45,46,47,48,49,50,51,52,53,54,55,56,57,58,59,60,61,62,63,63,63,63,63,63,63,63,63,63,63,
18,34,19,20,23,24,25,28,29,30,33,26,31,21,35,27,32,22,22,22,22,22,22,22,22,22,22,22,50,68,14,18,42,43,44,45,46,47,48,49,50,51,52,53,54,55,56,57,58,59,60,61,62,63,63,63,63,63,63,63,63,63,63,63,
36,52,37,38,41,42,43,46,47,48,51,44,49,39,53,45,50,40,40,40,40,40,40,40,40,40,40,40,68,14,32,18,42,43,44,45,46,47,48,49,50,51,52,53,54,55,56,57,58,59,60,61,62,63,63,63,63,63,63,63,63,63,63,63,
36,52,37,38,41,42,43,46,47,48,51,44,49,39,53,45,50,40,40,40,40,40,40,40,40,40,40,40,68,14,32,18,42,43,44,45,46,47,48,49,50,51,52,53,54,55,56,57,58,59,60,61,62,63,63,63,63,63,63,63,63,63,63,63,
54,70,55,56,59,60,61,64,65,66,69,62,67,57,71,63,68,58,58,58,58,58,58,58,58,58,58,58,14,32,50,18,42,43,44,45,46,47,48,49,50,51,52,53,54,55,56,57,58,59,60,61,62,63,63,63,63,63,63,63,63,63,63,63,
54,70,55,56,59,60,61,64,65,66,69,62,67,57,71,63,68,58,58,58,58,58,58,58,58,58,58,58,14,32,50,18,42,43,44,45,46,47,48,49,50,51,52,53,54,55,56,57,58,59,60,61,62,63,63,63,63,63,63,63,63,63,63,63,
0,1,2,5,6,7,10,11,12,15,16,8,13,3,17,9,14,4,4,4,4,4,4,4,4,4,4,4,32,50,68,18,42,43,44,45,46,47,48,49,50,51,52,53,54,55,56,57,58,59,60,61,62,63,63,63,63,63,63,63,63,63,63,63,
0,1,2,5,6,7,10,11,12,15,16,8,13,3,17,9,14,4,4,4,4,4,4,4,4,4,4,4,32,50,68,18,42,43,44,45,46,47,48,49,50,51,52,53,54,55,56,57,58,59,60,61,62,63,63,63,63,63,63,63,63,63,63,63,
18,19,20,23,24,25,28,29,30,33,34,26,31,21,35,27,32,22,22,22,22,22,22,22,22,22,22,22,50,68,14,18,42,43,44,45,46,47,48,49,50,51,52,53,54,55,56,57,58,59,60,61,62,63,63,63,63,63,63,63,63,63,63,63,
18,19,20,23,24,25,28,29,30,33,34,26,31,21,35,27,32,22,22,22,22,22,22,22,22,22,22,22,50,68,14,18,42,43,44,45,46,47,48,49,50,51,52,53,54,55,56,57,58,59,60,61,62,63,63,63,63,63,63,63,63,63,63,63,
36,37,38,41,42,43,46,47,48,51,52,44,49,39,53,45,50,40,40,40,40,40,40,40,40,40,40,40,68,14,32,18,42,43,44,45,46,47,48,49,50,51,52,53,54,55,56,57,58,59,60,61,62,63,63,63,63,63,63,63,63,63,63,63,
36,37,38,41,42,43,46,47,48,51,52,44,49,39,53,45,50,40,40,40,40,40,40,40,40,40,40,40,68,14,32,18,42,43,44,45,46,47,48,49,50,51,52,53,54,55,56,57,58,59,60,61,62,63,63,63,63,63,63,63,63,63,63,63,
54,55,56,59,60,61,64,65,66,69,70,62,67,57,71,63,68,58,58,58,58,58,58,58,58,58,58,58,14,32,50,18,42,43,44,45,46,47,48,49,50,51,52,53,54,55,56,57,58,59,60,61,62,63,63,63,63,63,63,63,63,63,63,63,
54,55,56,59,60,61,64,65,66,69,70,62,67,57,71,63,68,58,58,58,58,58,58,58,58,58,58,58,14,32,50,18,42,43,44,45,46,47,48,49,50,51,52,53,54,55,56,57,58,59,60,61,62,63,63,63,63,63,63,63,63,63,63,63,
0,0,0,0,0,0,0,0,0,0,0,0,0,0,0,0,0,0,0,0,0,0,0,0,0,0,0,0,35,53,71,0,0,0,0,0,0,0,0,0,0,0,0,0,0,0,0,0,0,0,0,0,0,0,0,0,0,0,0,0,0,0,0,0,
0,0,0,0,0,0,0,0,0,0,0,0,0,0,0,0,0,0,0,0,0,0,0,0,0,0,0,0,35,53,71,0,0,0,0,0,0,0,0,0,0,0,0,0,0,0,0,0,0,0,0,0,0,0,0,0,0,0,0,0,0,0,0,0,
0,0,0,0,0,0,0,0,0,0,0,0,0,0,0,0,0,0,0,0,0,0,0,0,0,0,0,0,53,71,17,0,0,0,0,0,0,0,0,0,0,0,0,0,0,0,0,0,0,0,0,0,0,0,0,0,0,0,0,0,0,0,0,0,
0,0,0,0,0,0,0,0,0,0,0,0,0,0,0,0,0,0,0,0,0,0,0,0,0,0,0,0,53,71,17,0,0,0,0,0,0,0,0,0,0,0,0,0,0,0,0,0,0,0,0,0,0,0,0,0,0,0,0,0,0,0,0,0,
0,0,0,0,0,0,0,0,0,0,0,0,0,0,0,0,0,0,0,0,0,0,0,0,0,0,0,0,71,17,35,0,0,0,0,0,0,0,0,0,0,0,0,0,0,0,0,0,0,0,0,0,0,0,0,0,0,0,0,0,0,0,0,0,
0,0,0,0,0,0,0,0,0,0,0,0,0,0,0,0,0,0,0,0,0,0,0,0,0,0,0,0,71,17,35,0,0,0,0,0,0,0,0,0,0,0,0,0,0,0,0,0,0,0,0,0,0,0,0,0,0,0,0,0,0,0,0,0,
0,0,0,0,0,0,0,0,0,0,0,0,0,0,0,0,0,0,0,0,0,0,0,0,0,0,0,0,17,35,53,0,0,0,0,0,0,0,0,0,0,0,0,0,0,0,0,0,0,0,0,0,0,0,0,0,0,0,0,0,0,0,0,0,
0,0,0,0,0,0,0,0,0,0,0,0,0,0,0,0,0,0,0,0,0,0,0,0,0,0,0,0,17,35,53,0,0,0,0,0,0,0,0,0,0,0,0,0,0,0,0,0,0,0,0,0,0,0,0,0,0,0,0,0,0,0,0,0,
0,0,0,0,0,0,0,0,0,0,0,0,0,0,0,0,0,0,0,0,0,0,0,0,0,0,0,0,35,53,71,0,0,0,0,0,0,0,0,0,0,0,0,0,0,0,0,0,0,0,0,0,0,0,0,0,0,0,0,0,0,0,0,0,
0,0,0,0,0,0,0,0,0,0,0,0,0,0,0,0,0,0,0,0,0,0,0,0,0,0,0,0,35,53,71,0,0,0,0,0,0,0,0,0,0,0,0,0,0,0,0,0,0,0,0,0,0,0,0,0,0,0,0,0,0,0,0,0,
0,0,0,0,0,0,0,0,0,0,0,0,0,0,0,0,0,0,0,0,0,0,0,0,0,0,0,0,53,71,17,0,0,0,0,0,0,0,0,0,0,0,0,0,0,0,0,0,0,0,0,0,0,0,0,0,0,0,0,0,0,0,0,0,
0,0,0,0,0,0,0,0,0,0,0,0,0,0,0,0,0,0,0,0,0,0,0,0,0,0,0,0,53,71,17,0,0,0,0,0,0,0,0,0,0,0,0,0,0,0,0,0,0,0,0,0,0,0,0,0,0,0,0,0,0,0,0,0,
0,0,0,0,0,0,0,0,0,0,0,0,0,0,0,0,0,0,0,0,0,0,0,0,0,0,0,0,71,17,35,0,0,0,0,0,0,0,0,0,0,0,0,0,0,0,0,0,0,0,0,0,0,0,0,0,0,0,0,0,0,0,0,0,
0,0,0,0,0,0,0,0,0,0,0,0,0,0,0,0,0,0,0,0,0,0,0,0,0,0,0,0,71,17,35,0,0,0,0,0,0,0,0,0,0,0,0,0,0,0,0,0,0,0,0,0,0,0,0,0,0,0,0,0,0,0,0,0,
0,0,0,0,0,0,0,0,0,0,0,0,0,0,0,0,0,0,0,0,0,0,0,0,0,0,0,0,17,35,53,0,0,0,0,0,0,0,0,0,0,0,0,0,0,0,0,0,0,0,0,0,0,0,0,0,0,0,0,0,0,0,0,0,
0,0,0,0,0,0,0,0,0,0,0,0,0,0,0,0,0,0,0,0,0,0,0,0,0,0,0,0,17,35,53,0,0,0,0,0,0,0,0,0,0,0,0,0,0,0,0,0,0,0,0,0,0,0,0,0,0,0,0,0,0,0,0,0,
0,0,0,0,0,0,0,0,0,0,0,0,0,0,0,0,0,0,0,0,0,0,0,0,0,0,0,0,35,53,71,0,0,0,0,0,0,0,0,0,0,0,0,0,0,0,0,0,0,0,0,0,0,0,0,0,0,0,0,0,0,0,0,0,
0,0,0,0,0,0,0,0,0,0,0,0,0,0,0,0,0,0,0,0,0,0,0,0,0,0,0,0,35,53,71,0,0,0,0,0,0,0,0,0,0,0,0,0,0,0,0,0,0,0,0,0,0,0,0,0,0,0,0,0,0,0,0,0,
0,0,0,0,0,0,0,0,0,0,0,0,0,0,0,0,0,0,0,0,0,0,0,0,0,0,0,0,53,71,17,0,0,0,0,0,0,0,0,0,0,0,0,0,0,0,0,0,0,0,0,0,0,0,0,0,0,0,0,0,0,0,0,0,
0,0,0,0,0,0,0,0,0,0,0,0,0,0,0,0,0,0,0,0,0,0,0,0,0,0,0,0,53,71,17,0,0,0,0,0,0,0,0,0,0,0,0,0,0,0,0,0,0,0,0,0,0,0,0,0,0,0,0,0,0,0,0,0,
0,0,0,0,0,0,0,0,0,0,0,0,0,0,0,0,0,0,0,0,0,0,0,0,0,0,0,0,71,17,35,0,0,0,0,0,0,0,0,0,0,0,0,0,0,0,0,0,0,0,0,0,0,0,0,0,0,0,0,0,0,0,0,0,
0,0,0,0,0,0,0,0,0,0,0,0,0,0,0,0,0,0,0,0,0,0,0,0,0,0,0,0,71,17,35,0,0,0,0,0,0,0,0,0,0,0,0,0,0,0,0,0,0,0,0,0,0,0,0,0,0,0,0,0,0,0,0,0,
0,0,0,0,0,0,0,0,0,0,0,0,0,0,0,0,0,0,0,0,0,0,0,0,0,0,0,0,17,35,53,0,0,0,0,0,0,0,0,0,0,0,0,0,0,0,0,0,0,0,0,0,0,0,0,0,0,0,0,0,0,0,0,0,
0,0,0,0,0,0,0,0,0,0,0,0,0,0,0,0,0,0,0,0,0,0,0,0,0,0,0,0,17,35,53,0,0,0,0,0,0,0,0,0,0,0,0,0,0,0,0,0,0,0,0,0,0,0,0,0,0,0,0,0,0,0,0,0,
0,0,0,0,0,0,0,0,0,0,0,0,0,0,0,0,0,0,0,0,0,0,0,0,0,0,0,0,35,53,71,0,0,0,0,0,0,0,0,0,0,0,0,0,0,0,0,0,0,0,0,0,0,0,0,0,0,0,0,0,0,0,0,0,
0,0,0,0,0,0,0,0,0,0,0,0,0,0,0,0,0,0,0,0,0,0,0,0,0,0,0,0,35,53,71,0,0,0,0,0,0,0,0,0,0,0,0,0,0,0,0,0,0,0,0,0,0,0,0,0,0,0,0,0,0,0,0,0,
0,0,0,0,0,0,0,0,0,0,0,0,0,0,0,0,0,0,0,0,0,0,0,0,0,0,0,0,53,71,17,0,0,0,0,0,0,0,0,0,0,0,0,0,0,0,0,0,0,0,0,0,0,0,0,0,0,0,0,0,0,0,0,0,
0,0,0,0,0,0,0,0,0,0,0,0,0,0,0,0,0,0,0,0,0,0,0,0,0,0,0,0,53,71,17,0,0,0,0,0,0,0,0,0,0,0,0,0,0,0,0,0,0,0,0,0,0,0,0,0,0,0,0,0,0,0,0,0,
0,0,0,0,0,0,0,0,0,0,0,0,0,0,0,0,0,0,0,0,0,0,0,0,0,0,0,0,71,17,35,0,0,0,0,0,0,0,0,0,0,0,0,0,0,0,0,0,0,0,0,0,0,0,0,0,0,0,0,0,0,0,0,0,
0,0,0,0,0,0,0,0,0,0,0,0,0,0,0,0,0,0,0,0,0,0,0,0,0,0,0,0,71,17,35,0,0,0,0,0,0,0,0,0,0,0,0,0,0,0,0,0,0,0,0,0,0,0,0,0,0,0,0,0,0,0,0,0,
0,0,0,0,0,0,0,0,0,0,0,0,0,0,0,0,0,0,0,0,0,0,0,0,0,0,0,0,17,35,53,0,0,0,0,0,0,0,0,0,0,0,0,0,0,0,0,0,0,0,0,0,0,0,0,0,0,0,0,0,0,0,0,0,
0,0,0,0,0,0,0,0,0,0,0,0,0,0,0,0,0,0,0,0,0,0,0,0,0,0,0,0,17,35,53,0,0,0,0,0,0,0,0,0,0,0,0,0,0,0,0,0,0,0,0,0,0,0,0,0,0,0,0,0,0,0,0,0,
0,0,0,0,0,0,0,0,0,0,0,0,0,0,0,0,0,0,0,0,0,0,0,0,0,0,0,0,35,53,71,0,0,0,0,0,0,0,0,0,0,0,0,0,0,0,0,0,0,0,0,0,0,0,0,0,0,0,0,0,0,0,0,0,
0,0,0,0,0,0,0,0,0,0,0,0,0,0,0,0,0,0,0,0,0,0,0,0,0,0,0,0,35,53,71,0,0,0,0,0,0,0,0,0,0,0,0,0,0,0,0,0,0,0,0,0,0,0,0,0,0,0,0,0,0,0,0,0,
0,0,0,0,0,0,0,0,0,0,0,0,0,0,0,0,0,0,0,0,0,0,0,0,0,0,0,0,53,71,17,0,0,0,0,0,0,0,0,0,0,0,0,0,0,0,0,0,0,0,0,0,0,0,0,0,0,0,0,0,0,0,0,0,
0,0,0,0,0,0,0,0,0,0,0,0,0,0,0,0,0,0,0,0,0,0,0,0,0,0,0,0,53,71,17,0,0,0,0,0,0,0,0,0,0,0,0,0,0,0,0,0,0,0,0,0,0,0,0,0,0,0,0,0,0,0,0,0,
0,0,0,0,0,0,0,0,0,0,0,0,0,0,0,0,0,0,0,0,0,0,0,0,0,0,0,0,71,17,35,0,0,0,0,0,0,0,0,0,0,0,0,0,0,0,0,0,0,0,0,0,0,0,0,0,0,0,0,0,0,0,0,0,
0,0,0,0,0,0,0,0,0,0,0,0,0,0,0,0,0,0,0,0,0,0,0,0,0,0,0,0,71,17,35,0,0,0,0,0,0,0,0,0,0,0,0,0,0,0,0,0,0,0,0,0,0,0,0,0,0,0,0,0,0,0,0,0,
0,0,0,0,0,0,0,0,0,0,0,0,0,0,0,0,0,0,0,0,0,0,0,0,0,0,0,0,17,35,53,0,0,0,0,0,0,0,0,0,0,0,0,0,0,0,0,0,0,0,0,0,0,0,0,0,0,0,0,0,0,0,0,0,
0,0,0,0,0,0,0,0,0,0,0,0,0,0,0,0,0,0,0,0,0,0,0,0,0,0,0,0,17,35,53,0,0,0,0,0,0,0,0,0,0,0,0,0,0,0,0,0,0,0,0,0,0,0,0,0,0,0,0,0,0,0,0,0,
0,0,0,0,0,0,0,0,0,0,0,0,0,0,0,0,0,0,0,0,0,0,0,0,0,0,0,0,35,53,71,0,0,0,0,0,0,0,0,0,0,0,0,0,0,0,0,0,0,0,0,0,0,0,0,0,0,0,0,0,0,0,0,0,
0,0,0,0,0,0,0,0,0,0,0,0,0,0,0,0,0,0,0,0,0,0,0,0,0,0,0,0,35,53,71,0,0,0,0,0,0,0,0,0,0,0,0,0,0,0,0,0,0,0,0,0,0,0,0,0,0,0,0,0,0,0,0,0,
0,0,0,0,0,0,0,0,0,0,0,0,0,0,0,0,0,0,0,0,0,0,0,0,0,0,0,0,53,71,17,0,0,0,0,0,0,0,0,0,0,0,0,0,0,0,0,0,0,0,0,0,0,0,0,0,0,0,0,0,0,0,0,0,
0,0,0,0,0,0,0,0,0,0,0,0,0,0,0,0,0,0,0,0,0,0,0,0,0,0,0,0,53,71,17,0,0,0,0,0,0,0,0,0,0,0,0,0,0,0,0,0,0,0,0,0,0,0,0,0,0,0,0,0,0,0,0,0,
0,0,0,0,0,0,0,0,0,0,0,0,0,0,0,0,0,0,0,0,0,0,0,0,0,0,0,0,71,17,35,0,0,0,0,0,0,0,0,0,0,0,0,0,0,0,0,0,0,0,0,0,0,0,0,0,0,0,0,0,0,0,0,0,
0,0,0,0,0,0,0,0,0,0,0,0,0,0,0,0,0,0,0,0,0,0,0,0,0,0,0,0,71,17,35,0,0,0,0,0,0,0,0,0,0,0,0,0,0,0,0,0,0,0,0,0,0,0,0,0,0,0,0,0,0,0,0,0,
0,0,0,0,0,0,0,0,0,0,0,0,0,0,0,0,0,0,0,0,0,0,0,0,0,0,0,0,17,35,53,0,0,0,0,0,0,0,0,0,0,0,0,0,0,0,0,0,0,0,0,0,0,0,0,0,0,0,0,0,0,0,0,0,
0,0,0,0,0,0,0,0,0,0,0,0,0,0,0,0,0,0,0,0,0,0,0,0,0,0,0,0,17,35,53,0,0,0,0,0,0,0,0,0,0,0,0,0,0,0,0,0,0,0,0,0,0,0,0,0,0,0,0,0,0,0,0,0,
0,0,0,0,0,0,0,0,0,0,0,0,0,0,0,0,0,0,0,0,0,0,0,0,0,0,0,0,35,53,71,0,0,0,0,0,0,0,0,0,0,0,0,0,0,0,0,0,0,0,0,0,0,0,0,0,0,0,0,0,0,0,0,0,
0,0,0,0,0,0,0,0,0,0,0,0,0,0,0,0,0,0,0,0,0,0,0,0,0,0,0,0,35,53,71,0,0,0,0,0,0,0,0,0,0,0,0,0,0,0,0,0,0,0,0,0,0,0,0,0,0,0,0,0,0,0,0,0,
0,0,0,0,0,0,0,0,0,0,0,0,0,0,0,0,0,0,0,0,0,0,0,0,0,0,0,0,53,71,17,0,0,0,0,0,0,0,0,0,0,0,0,0,0,0,0,0,0,0,0,0,0,0,0,0,0,0,0,0,0,0,0,0,
0,0,0,0,0,0,0,0,0,0,0,0,0,0,0,0,0,0,0,0,0,0,0,0,0,0,0,0,53,71,17,0,0,0,0,0,0,0,0,0,0,0,0,0,0,0,0,0,0,0,0,0,0,0,0,0,0,0,0,0,0,0,0,0,
0,0,0,0,0,0,0,0,0,0,0,0,0,0,0,0,0,0,0,0,0,0,0,0,0,0,0,0,71,17,35,0,0,0,0,0,0,0,0,0,0,0,0,0,0,0,0,0,0,0,0,0,0,0,0,0,0,0,0,0,0,0,0,0,
0,0,0,0,0,0,0,0,0,0,0,0,0,0,0,0,0,0,0,0,0,0,0,0,0,0,0,0,71,17,35,0,0,0,0,0,0,0,0,0,0,0,0,0,0,0,0,0,0,0,0,0,0,0,0,0,0,0,0,0,0,0,0,0,
0,0,0,0,0,0,0,0,0,0,0,0,0,0,0,0,0,0,0,0,0,0,0,0,0,0,0,0,17,35,53,0,0,0,0,0,0,0,0,0,0,0,0,0,0,0,0,0,0,0,0,0,0,0,0,0,0,0,0,0,0,0,0,0,
0,0,0,0,0,0,0,0,0,0,0,0,0,0,0,0,0,0,0,0,0,0,0,0,0,0,0,0,17,35,53,0,0,0,0,0,0,0,0,0,0,0,0,0,0,0,0,0,0,0,0,0,0,0,0,0,0,0,0,0,0,0,0,0,
0,0,0,0,0,0,0,0,0,0,0,0,0,0,0,0,0,0,0,0,0,0,0,0,0,0,0,0,35,53,71,0,0,0,0,0,0,0,0,0,0,0,0,0,0,0,0,0,0,0,0,0,0,0,0,0,0,0,0,0,0,0,0,0,
0,0,0,0,0,0,0,0,0,0,0,0,0,0,0,0,0,0,0,0,0,0,0,0,0,0,0,0,35,53,71,0,0,0,0,0,0,0,0,0,0,0,0,0,0,0,0,0,0,0,0,0,0,0,0,0,0,0,0,0,0,0,0,0,
0,0,0,0,0,0,0,0,0,0,0,0,0,0,0,0,0,0,0,0,0,0,0,0,0,0,0,0,53,71,17,0,0,0,0,0,0,0,0,0,0,0,0,0,0,0,0,0,0,0,0,0,0,0,0,0,0,0,0,0,0,0,0,0,
0,0,0,0,0,0,0,0,0,0,0,0,0,0,0,0,0,0,0,0,0,0,0,0,0,0,0,0,53,71,17,0,0,0,0,0,0,0,0,0,0,0,0,0,0,0,0,0,0,0,0,0,0,0,0,0,0,0,0,0,0,0,0,0,
0,0,0,0,0,0,0,0,0,0,0,0,0,0,0,0,0,0,0,0,0,0,0,0,0,0,0,0,71,17,35,0,0,0,0,0,0,0,0,0,0,0,0,0,0,0,0,0,0,0,0,0,0,0,0,0,0,0,0,0,0,0,0,0,
0,0,0,0,0,0,0,0,0,0,0,0,0,0,0,0,0,0,0,0,0,0,0,0,0,0,0,0,71,17,35,0,0,0,0,0,0,0,0,0,0,0,0,0,0,0,0,0,0,0,0,0,0,0,0,0,0,0,0,0,0,0,0,0,
0,0,0,0,0,0,0,0,0,0,0,0,0,0,0,0,0,0,0,0,0,0,0,0,0,0,0,0,17,35,53,0,0,0,0,0,0,0,0,0,0,0,0,0,0,0,0,0,0,0,0,0,0,0,0,0,0,0,0,0,0,0,0,0,
0,0,0,0,0,0,0,0,0,0,0,0,0,0,0,0,0,0,0,0,0,0,0,0,0,0,0,0,17,35,53,0,0,0,0,0,0,0,0,0,0,0,0,0,0,0,0,0,0,0,0,0,0,0,0,0,0,0,0,0,0,0,0,0};
struct Args { const void* p[14]; };

__global__ __launch_bounds__(256, 1) void fused_glu_conv(Args a) {
  __shared__ __attribute__((aligned(16))) char smem[163840];
  unsigned wv = __builtin_amdgcn_readfirstlane(threadIdx.x >> 6);
  unsigned lb = (unsigned)(size_t)(__attribute__((address_space(3))) char*)smem;
  const unsigned char* tb = kTab;
  asm volatile(
"s_load_dwordx8 s[72:79], %0, 0\n"
"s_load_dwordx2 s[80:81], %0, 40\n"
"s_mov_b64 s[16:17], %0\n"
"s_mov_b32 s18, %1\n"
"s_mov_b32 s19, %2\n"
"v_mbcnt_lo_u32_b32 v0, -1, 0\n"
"v_mbcnt_hi_u32_b32 v0, -1, v0\n"
"s_getpc_b64 s[84:85]\n"
"Lpc%=:\n"
"s_and_b32 s85, s85, 0xffff\n"
"s_mov_b32 s86, Lexit%=-Lpc%=\n"
"s_mov_b32 s87, 0x20000\n"
"s_lshr_b32 s88, s18, 3\n"
"s_lshl_b32 s88, s88, 2\n"
"s_add_u32 s88, s88, s19\n"
"s_and_b32 s88, s88, 31\n"
"s_lshl_b32 s88, s88, 11\n"
"v_lshlrev_b32 v2, 7, v0\n"
"v_add_u32 v2, s88, v2\n"
"s_mov_b64 exec, 0xffff\n"
"buffer_load_dword v241, v2, s[84:87], 0 offen\n"
"s_mov_b64 exec, -1\n"
"s_and_b32 s84, s18, 7\n"
"s_lshr_b32 s85, s18, 3\n"
"s_lshr_b32 s34, s84, 1\n"
"s_and_b32 s84, s84, 1\n"
"s_and_b32 s35, s85, 7\n"
"s_lshr_b32 s67, s85, 3\n"
"s_lshl_b32 s66, s84, 3\n"
"s_add_u32 s66, s66, s35\n"
"s_mov_b64 s[82:83], %4\n"
"s_cmp_eq_u32 s67, 3\n"
"s_cbranch_scc1 Lpri%=\n"
"Lprb%=:\n"
"s_and_b32 s90, s19, 1\n"
"s_lshr_b32 s91, s19, 1\n"
"v_lshrrev_b32 v23, 3, v0\n"
"v_and_b32 v21, 7, v0\n"
"v_lshrrev_b32 v20, 4, v0\n"
"s_lshl_b32 s86, s90, 1\n"
"s_add_u32 s86, s86, s34\n"
"s_add_u32 s88, s86, 1\n"
"s_and_b32 s86, s86, 3\n"
"s_and_b32 s88, s88, 3\n"
"v_mul_u32_u24 v19, 1152, v23\n"
"v_xor_b32 v20, v20, v21\n"
"s_mul_i32 s86, s86, 36864\n"
"s_mul_i32 s88, s88, 36864\n"
"s_mul_i32 s85, s91, 18432\n"
"v_lshl_add_u32 v20, v20, 4, v19\n"
"s_add_u32 s85, s85, 1024\n"
"v_xor_b32 v22, 64, v20\n"
"s_add_u32 s88, s88, s85\n"
"s_add_u32 s86, s86, s85\n"
"v_add_u32 v22, 8192, v22\n"
"s_add_u32 s86, s86, 2048\n"
"v_add_u32 v56, s86, v20\n"
"v_add_u32 v57, s86, v22\n"
"v_add_u32 v58, s88, v20\n"
"v_add_u32 v59, s88, v22\n"
"s_mul_i32 s89, s66, 147456\n"
"s_lshl_b32 s87, s67, 1\n"
"s_add_u32 s87, s87, s35\n"
"s_and_b32 s87, s87, 7\n"
"s_lshl_b32 s88, s87, 7\n"
"s_add_u32 s52, s89, s88\n"
"s_mul_i32 s40, s87, 589824\n"
"s_lshl_b32 s61, s19, 12\n"
"s_add_u32 s62, s61, 98304\n"
"s_mov_b32 s22, 2362368\n"
"s_mov_b32 s23, 0x20000\n"
"s_add_u32 m0, s61, 147456\n"
"s_cmp_eq_u32 s67, 3\n"
"s_waitcnt lgkmcnt(0)\n"
"s_cbranch_scc1 Lskx%=\n"
"s_sub_u32 s20, s72, 3072\n"
"s_subb_u32 s21, s73, 0\n"
"s_and_b32 s21, s21, 0xffff\n"
"buffer_load_dwordx4 v56, s[20:23], s52 offen offset:0 lds\n"
"buffer_load_dwordx4 v57, s[20:23], s52 offen offset:1024 lds\n"
"buffer_load_dwordx4 v58, s[20:23], s52 offen offset:2048 lds\n"
"buffer_load_dwordx4 v59, s[20:23], s52 offen offset:3072 lds\n"
"Lskx%=:\n"
"s_sub_u32 s20, s72, 3072\n"
"s_subb_u32 s21, s73, 0\n"
"s_and_b32 s21, s21, 0xffff\n"
"s_mov_b32 s85, 0xf0a0500\n"
"s_mov_b32 s86, 0x3050505\n"
"s_lshl_b32 s87, s67, 3\n"
"s_lshr_b32 s85, s85, s87\n"
"s_and_b32 s85, s85, 0xff\n"
"s_lshr_b32 s86, s86, s87\n"
"s_and_b32 s64, s86, 0xff\n"
"s_mul_i32 s87, s34, 18\n"
"s_add_u32 s85, s85, s87\n"
"s_lshl_b32 s49, s85, 7\n"
"s_lshl_b32 s87, s19, 3\n"
"v_add_u32 v19, s87, v23\n"
"v_mul_u32_u24 v19, 9216, v19\n"
"v_bfe_u32 v20, v0, 4, 1\n"
"v_lshlrev_b32 v20, 1, v20\n"
"s_lshl_b32 s87, s90, 2\n"
"v_or_b32 v20, s87, v20\n"
"v_xor_b32 v20, v20, v21\n"
"v_lshl_add_u32 v10, v20, 4, v19\n"
"v_add_u32 v11, 292864, v10\n"
"s_mov_b32 s24, s74\n"
"s_and_b32 s25, s75, 0xffff\n"
"s_mov_b32 s26, 5308416\n"
"s_mov_b32 s27, 0x20000\n"
"s_sub_u32 s28, s76, 1024\n"
"s_subb_u32 s29, s77, 0\n"
"s_and_b32 s29, s29, 0xffff\n"
"s_mov_b32 s30, 5309440\n"
"s_mov_b32 s31, 0x20000\n"
"s_cmp_eq_u32 s67, 3\n"
"s_cbranch_scc1 Lsk0%=\n"
"s_add_u32 m0, s61, 0\n"
"s_add_u32 s51, s40, s49\n"
"buffer_load_dwordx4 v10, s[24:27], s51 offen lds\n"
"buffer_load_dwordx4 v10, s[28:31], s51 offen offset:1024 lds\n"
"buffer_load_dwordx4 v11, s[24:27], s51 offen offset:2048 lds\n"
"Lsk0%=:\n"
"s_mov_b32 s36, s78\n"
"s_and_b32 s37, s79, 0xffff\n"
"s_mov_b32 s38, 33098880\n"
"s_mov_b32 s39, 0x20000\n"
"s_lshl_b32 s84, s18, 6\n"
"v_add_u32 v60, s84, v0\n"
"s_mov_b32 s84, 33065984\n"
"buffer_load_dwordx2 v[242:243], off, s[36:39], s84 sc1\n"
"global_load_ubyte v60, v60, s[82:83]\n"
"s_cmp_eq_u32 s67, 3\n"
"s_cbranch_scc1 Lsk1%=\n"
"buffer_load_dwordx4 v11, s[28:31], s51 offen offset:3072 lds\n"
"Lsk1%=:\n"
"s_lshl_b32 s87, s67, 1\n"
"s_add_u32 s87, s87, s35\n"
"s_add_u32 s87, s87, 1\n"
"s_and_b32 s87, s87, 7\n"
"s_mul_i32 s41, s87, 589824\n"
"s_lshl_b32 s88, s87, 7\n"
"s_add_u32 s53, s89, s88\n"
"s_add_u32 s87, s87, 1\n"
"s_and_b32 s87, s87, 7\n"
"s_mul_i32 s42, s87, 589824\n"
"s_lshl_b32 s88, s87, 7\n"
"s_add_u32 s54, s89, s88\n"
"s_add_u32 s87, s87, 1\n"
"s_and_b32 s87, s87, 7\n"
"s_mul_i32 s43, s87, 589824\n"
"s_lshl_b32 s88, s87, 7\n"
"s_add_u32 s55, s89, s88\n"
"s_add_u32 s87, s87, 1\n"
"s_and_b32 s87, s87, 7\n"
"s_mul_i32 s44, s87, 589824\n"
"s_lshl_b32 s88, s87, 7\n"
"s_add_u32 s56, s89, s88\n"
"s_add_u32 s87, s87, 1\n"
"s_and_b32 s87, s87, 7\n"
"s_mul_i32 s45, s87, 589824\n"
"s_lshl_b32 s88, s87, 7\n"
"s_add_u32 s57, s89, s88\n"
"s_add_u32 s87, s87, 1\n"
"s_and_b32 s87, s87, 7\n"
"s_mul_i32 s46, s87, 589824\n"
"s_lshl_b32 s88, s87, 7\n"
"s_add_u32 s58, s89, s88\n"
"s_add_u32 s87, s87, 1\n"
"s_and_b32 s87, s87, 7\n"
"s_mul_i32 s47, s87, 589824\n"
"s_lshl_b32 s88, s87, 7\n"
"s_add_u32 s59, s89, s88\n"
"s_mov_b32 s87, 8\n"
"s_mul_i32 s48, s87, 589824\n"
"s_lshl_b32 s88, s87, 7\n"
"s_add_u32 s60, s89, s88\n"
"s_sub_u32 s72, s80, s89\n"
"s_subb_u32 s73, s81, 0\n"
"s_and_b32 s73, s73, 0xffff\n"
"s_add_u32 s74, s89, 1152\n"
"s_mov_b32 s75, 0x20000\n"
"v_lshrrev_b32 v19, 4, v0\n"
"v_lshlrev_b32 v15, 4, v19\n"
"s_mul_i32 s78, s66, 640\n"
"s_add_u32 s78, s78, 33030144\n"
"s_lshl_b32 s76, s18, 7\n"
"s_add_u32 s76, s76, 33066112\n"
"s_add_u32 s50, s49, 128\n"
"s_add_u32 s68, s49, 256\n"
"s_mov_b32 s65, 0xbfb8aa3b\n"
"v_bfe_u32 v20, v0, 2, 2\n"
"v_lshlrev_b32 v21, 12, v19\n"
"v_lshl_add_u32 v21, v20, 7, v21\n"
"v_and_b32 v22, 1, v0\n"
"v_lshl_add_u32 v21, v22, 3, v21\n"
"v_and_b32 v22, 2, v20\n"
"v_and_b32 v23, 1, v19\n"
"v_lshl_add_u32 v22, v23, 2, v22\n"
"v_bfe_u32 v23, v0, 1, 1\n"
"s_lshl_b32 s87, s91, 2\n"
"v_or_b32 v23, s87, v23\n"
"v_xor_b32 v23, v23, v22\n"
"v_lshl_add_u32 v4, v23, 4, v21\n"
"v_xor_b32 v7, 32, v4\n"
"v_add_u32 v5, 65536, v4\n"
"v_add_u32 v6, 131072, v4\n"
"v_add_u32 v8, 65536, v7\n"
"v_add_u32 v9, 131072, v7\n"
"v_and_b32 v20, 15, v0\n"
"v_lshrrev_b32 v21, 1, v20\n"
"v_xor_b32 v21, v21, v19\n"
"s_lshl_b32 s87, s90, 13\n"
"s_add_u32 s87, s87, 98304\n"
"v_lshlrev_b32 v20, 7, v20\n"
"v_add_u32 v20, s87, v20\n"
"v_lshl_add_u32 v12, v21, 4, v20\n"
"v_xor_b32 v13, 64, v12\n"
"v_lshlrev_b32 v19, 4, v0\n"
"s_add_u32 s87, s34, 0\n"
"s_and_b32 s87, s87, 3\n"
"s_lshl_b32 s87, s87, 2\n"
"s_add_u32 s87, s87, s91\n"
"s_lshl_b32 s88, s90, 1\n"
"s_add_u32 s87, s87, s88\n"
"s_lshl_b32 s87, s87, 10\n"
"v_add_u32 v24, s87, v19\n"
"s_add_u32 s87, s34, 1\n"
"s_and_b32 s87, s87, 3\n"
"s_lshl_b32 s87, s87, 2\n"
"s_add_u32 s87, s87, s91\n"
"s_lshl_b32 s88, s90, 1\n"
"s_add_u32 s87, s87, s88\n"
"s_lshl_b32 s87, s87, 10\n"
"v_add_u32 v25, s87, v19\n"
"s_add_u32 s87, s34, 2\n"
"s_and_b32 s87, s87, 3\n"
"s_lshl_b32 s87, s87, 2\n"
"s_add_u32 s87, s87, s91\n"
"s_lshl_b32 s88, s90, 1\n"
"s_add_u32 s87, s87, s88\n"
"s_lshl_b32 s87, s87, 10\n"
"v_add_u32 v26, s87, v19\n"
"s_add_u32 s87, s34, 3\n"
"s_and_b32 s87, s87, 3\n"
"s_lshl_b32 s87, s87, 2\n"
"s_add_u32 s87, s87, s91\n"
"s_lshl_b32 s88, s90, 1\n"
"s_add_u32 s87, s87, s88\n"
"s_lshl_b32 s87, s87, 10\n"
"v_add_u32 v27, s87, v19\n"
"v_cmp_eq_u32 vcc, 0, v0\n"
"s_cmp_eq_u32 s19, 0\n"
"s_cselect_b64 vcc, vcc, 0\n"
"v_mov_b32 v19, 0x7fff0000\n"
"v_mov_b32 v20, 0\n"
"v_cndmask_b32 v61, v19, v20, vcc\n"
"s_cmp_eq_u32 s67, 3\n"
"s_cbranch_scc1 Lea_c%=\n"
"s_lshl_b32 s80, s19, 12\n"
"v_lshlrev_b32 v19, 4, v0\n"
"v_add_u32 v50, s80, v19\n"
"s_add_u32 s80, s34, 0\n"
"s_and_b32 s80, s80, 3\n"
"s_lshl_b32 s80, s80, 1\n"
"s_add_u32 s80, s80, s90\n"
"s_lshl_b32 s80, s80, 11\n"
"s_add_u32 s80, s80, 73728\n"
"v_add_u32 v40, s80, v19\n"
"s_add_u32 s80, s34, 1\n"
"s_and_b32 s80, s80, 3\n"
"s_lshl_b32 s80, s80, 1\n"
"s_add_u32 s80, s80, s90\n"
"s_lshl_b32 s80, s80, 11\n"
"s_add_u32 s80, s80, 73728\n"
"v_add_u32 v41, s80, v19\n"
"s_add_u32 s80, s34, 2\n"
"s_and_b32 s80, s80, 3\n"
"s_lshl_b32 s80, s80, 1\n"
"s_add_u32 s80, s80, s90\n"
"s_lshl_b32 s80, s80, 11\n"
"s_add_u32 s80, s80, 73728\n"
"v_add_u32 v42, s80, v19\n"
"s_add_u32 s80, s34, 3\n"
"s_and_b32 s80, s80, 3\n"
"s_lshl_b32 s80, s80, 1\n"
"s_add_u32 s80, s80, s90\n"
"s_lshl_b32 s80, s80, 11\n"
"s_add_u32 s80, s80, 73728\n"
"v_add_u32 v43, s80, v19\n"
"v_add_u32 v19, 0, v0\n"
"v_mul_u32_u24 v20, 2731, v19\n"
"v_lshrrev_b32 v20, 16, v20\n"
"v_mul_u32_u24 v21, 24, v20\n"
"v_sub_u32 v21, v19, v21\n"
"v_and_b32 v22, 6, v20\n"
"v_xor_b32 v21, v21, v22\n"
"v_mul_u32_u24 v20, 1152, v20\n"
"v_lshl_add_u32 v20, v21, 4, v20\n"
"s_mul_i32 s80, s19, 18432\n"
"s_add_u32 s80, s80, 2048\n"
"v_add_u32 v44, s80, v20\n"
"v_add_u32 v47, 9216, v44\n"
"v_add_u32 v19, 64, v0\n"
"v_mul_u32_u24 v20, 2731, v19\n"
"v_lshrrev_b32 v20, 16, v20\n"
"v_mul_u32_u24 v21, 24, v20\n"
"v_sub_u32 v21, v19, v21\n"
"v_and_b32 v22, 6, v20\n"
"v_xor_b32 v21, v21, v22\n"
"v_mul_u32_u24 v20, 1152, v20\n"
"v_lshl_add_u32 v20, v21, 4, v20\n"
"s_mul_i32 s80, s19, 18432\n"
"s_add_u32 s80, s80, 1024\n"
"v_add_u32 v45, s80, v20\n"
"v_add_u32 v48, 9216, v45\n"
"v_add_u32 v19, 128, v0\n"
"v_mul_u32_u24 v20, 2731, v19\n"
"v_lshrrev_b32 v20, 16, v20\n"
"v_mul_u32_u24 v21, 24, v20\n"
"v_sub_u32 v21, v19, v21\n"
"v_and_b32 v22, 6, v20\n"
"v_xor_b32 v21, v21, v22\n"
"v_mul_u32_u24 v20, 1152, v20\n"
"v_lshl_add_u32 v20, v21, 4, v20\n"
"s_mul_i32 s80, s19, 18432\n"
"s_add_u32 s80, s80, 0\n"
"v_add_u32 v46, s80, v20\n"
"v_add_u32 v49, 9216, v46\n"
"v_lshrrev_b32 v19, 2, v0\n"
"v_bfe_u32 v20, v19, 1, 2\n"
"v_mul_u32_u24 v19, 384, v19\n"
"v_and_b32 v21, 3, v0\n"
"v_lshl_add_u32 v19, v21, 3, v19\n"
"v_add_u32 v19, 0, v19\n"
"s_mul_i32 s80, s91, 6\n"
"s_add_u32 s80, s80, 0\n"
"v_xor_b32 v21, s80, v20\n"
"v_lshl_add_u32 v32, v21, 5, v19\n"
"v_add_u32 v36, 49152, v32\n"
"s_mul_i32 s80, s91, 6\n"
"s_add_u32 s80, s80, 1\n"
"v_xor_b32 v21, s80, v20\n"
"v_lshl_add_u32 v33, v21, 5, v19\n"
"v_add_u32 v37, 49152, v33\n"
"s_mul_i32 s80, s91, 6\n"
"s_add_u32 s80, s80, 2\n"
"v_xor_b32 v21, s80, v20\n"
"v_lshl_add_u32 v34, v21, 5, v19\n"
"v_add_u32 v38, 49152, v34\n"
"s_mul_i32 s80, s91, 6\n"
"s_add_u32 s80, s80, 3\n"
"v_xor_b32 v21, s80, v20\n"
"v_lshl_add_u32 v35, v21, 5, v19\n"
"v_add_u32 v39, 49152, v35\n"
"s_and_b32 s80, s18, 7\n"
"s_mul_i32 s80, s80, 24\n"
"s_lshr_b32 s81, s18, 3\n"
"s_add_u32 s80, s80, s81\n"
"s_lshl_b32 s80, s80, 2\n"
"s_add_u32 s80, s80, s19\n"
"s_mul_i32 s80, s80, 6912\n"
"v_lshlrev_b32 v18, 7, v0\n"
"v_add_u32 v18, s80, v18\n"
"v_cmp_gt_u32 vcc, 18, v0\n"
"v_mov_b32 v19, 0x7fff0000\n"
"s_nop 0\n"
"v_cndmask_b32 v18, v19, v18, vcc\n"
"s_branch Lea_e%=\n"
"Lea_c%=:\n"
"v_lshlrev_b32 v19, 4, v0\n"
"s_lshl_b32 s80, s90, 6\n"
"s_add_u32 s80, s80, 18874368\n"
"v_lshrrev_b32 v20, 2, v0\n"
"v_lshlrev_b32 v20, 7, v20\n"
"v_and_b32 v21, 3, v0\n"
"v_lshl_add_u32 v20, v21, 4, v20\n"
"v_add_u32 v43, s80, v20\n"
"s_lshl_b32 s80, s90, 3\n"
"s_add_u32 s81, s80, 4\n"
"v_add_u32 v42, s81, v19\n"
"s_sub_u32 s80, s80, 60\n"
"v_lshlrev_b32 v44, 2, v0\n"
"v_add_u32 v44, s80, v44\n"
"s_lshl_b32 s80, s19, 13\n"
"s_add_u32 s80, s80, 107520\n"
"v_add_u32 v46, s80, v19\n"
"s_lshl_b32 s80, s91, 14\n"
"s_lshl_b32 s81, s90, 12\n"
"s_add_u32 s80, s80, s81\n"
"s_add_u32 s80, s80, 107520\n"
"v_add_u32 v47, s80, v19\n"
"v_lshlrev_b32 v53, 3, v0\n"
"v_add_u32 v53, 33064960, v53\n"
"v_and_b32 v20, 3, v0\n"
"v_mul_u32_u24 v20, 4160, v20\n"
"v_bfe_u32 v21, v0, 2, 2\n"
"v_lshrrev_b32 v22, 4, v0\n"
"v_sub_u32 v21, v21, v22\n"
"v_lshl_add_u32 v20, v21, 4, v20\n"
"s_mul_i32 s81, s91, 26880\n"
"s_lshl_b32 s80, s90, 9\n"
"s_add_u32 s80, s80, s81\n"
"s_add_u32 s80, s80, 48\n"
"v_add_u32 v32, s80, v20\n"
"s_add_u32 s81, s81, 17664\n"
"v_and_b32 v20, 15, v0\n"
"s_lshl_b32 s80, s90, 3\n"
"s_add_u32 s80, s80, 0\n"
"v_subrev_u32 v21, s80, v20\n"
"v_lshrrev_b32 v33, 1, v21\n"
"v_and_b32 v33, 2, v33\n"
"v_xor_b32 v33, v33, v22\n"
"v_lshlrev_b32 v33, 4, v33\n"
"v_lshl_add_u32 v33, v21, 6, v33\n"
"v_add_u32 v33, s81, v33\n"
"s_lshl_b32 s80, s90, 3\n"
"s_add_u32 s80, s80, 1\n"
"v_subrev_u32 v21, s80, v20\n"
"v_lshrrev_b32 v34, 1, v21\n"
"v_and_b32 v34, 2, v34\n"
"v_xor_b32 v34, v34, v22\n"
"v_lshlrev_b32 v34, 4, v34\n"
"v_lshl_add_u32 v34, v21, 6, v34\n"
"v_add_u32 v34, s81, v34\n"
"s_lshl_b32 s80, s90, 3\n"
"s_add_u32 s80, s80, 2\n"
"v_subrev_u32 v21, s80, v20\n"
"v_lshrrev_b32 v35, 1, v21\n"
"v_and_b32 v35, 2, v35\n"
"v_xor_b32 v35, v35, v22\n"
"v_lshlrev_b32 v35, 4, v35\n"
"v_lshl_add_u32 v35, v21, 6, v35\n"
"v_add_u32 v35, s81, v35\n"
"s_lshl_b32 s80, s90, 3\n"
"s_add_u32 s80, s80, 3\n"
"v_subrev_u32 v21, s80, v20\n"
"v_lshrrev_b32 v36, 1, v21\n"
"v_and_b32 v36, 2, v36\n"
"v_xor_b32 v36, v36, v22\n"
"v_lshlrev_b32 v36, 4, v36\n"
"v_lshl_add_u32 v36, v21, 6, v36\n"
"v_add_u32 v36, s81, v36\n"
"s_lshl_b32 s80, s90, 3\n"
"s_add_u32 s80, s80, 4\n"
"v_subrev_u32 v21, s80, v20\n"
"v_lshrrev_b32 v37, 1, v21\n"
"v_and_b32 v37, 2, v37\n"
"v_xor_b32 v37, v37, v22\n"
"v_lshlrev_b32 v37, 4, v37\n"
"v_lshl_add_u32 v37, v21, 6, v37\n"
"v_add_u32 v37, s81, v37\n"
"s_lshl_b32 s80, s90, 3\n"
"s_add_u32 s80, s80, 5\n"
"v_subrev_u32 v21, s80, v20\n"
"v_lshrrev_b32 v38, 1, v21\n"
"v_and_b32 v38, 2, v38\n"
"v_xor_b32 v38, v38, v22\n"
"v_lshlrev_b32 v38, 4, v38\n"
"v_lshl_add_u32 v38, v21, 6, v38\n"
"v_add_u32 v38, s81, v38\n"
"s_lshl_b32 s80, s90, 3\n"
"s_add_u32 s80, s80, 6\n"
"v_subrev_u32 v21, s80, v20\n"
"v_lshrrev_b32 v39, 1, v21\n"
"v_and_b32 v39, 2, v39\n"
"v_xor_b32 v39, v39, v22\n"
"v_lshlrev_b32 v39, 4, v39\n"
"v_lshl_add_u32 v39, v21, 6, v39\n"
"v_add_u32 v39, s81, v39\n"
"s_lshl_b32 s80, s90, 3\n"
"s_add_u32 s80, s80, 7\n"
"v_subrev_u32 v21, s80, v20\n"
"v_lshrrev_b32 v40, 1, v21\n"
"v_and_b32 v40, 2, v40\n"
"v_xor_b32 v40, v40, v22\n"
"v_lshlrev_b32 v40, 4, v40\n"
"v_lshl_add_u32 v40, v21, 6, v40\n"
"v_add_u32 v40, s81, v40\n"
"s_lshl_b32 s80, s90, 5\n"
"v_add_u32 v21, s80, v20\n"
"v_lshrrev_b32 v41, 1, v21\n"
"v_and_b32 v41, 2, v41\n"
"v_xor_b32 v41, v41, v22\n"
"v_lshlrev_b32 v41, 4, v41\n"
"v_lshl_add_u32 v41, v21, 6, v41\n"
"v_add_u32 v41, s81, v41\n"
"v_lshlrev_b32 v21, 2, v22\n"
"v_add_u32 v21, 3, v21\n"
"v_sub_u32 v21, v21, v20\n"
"v_cmp_eq_u32 vcc, 0, v21\n"
"v_mov_b32 v49, 0xffff\n"
"s_nop 0\n"
"v_cndmask_b32 v49, 0, v49, vcc\n"
"v_cmp_eq_u32 vcc, 1, v21\n"
"v_mov_b32 v50, 0xffff\n"
"s_nop 0\n"
"v_cndmask_b32 v50, 0, v50, vcc\n"
"v_cmp_eq_u32 vcc, 2, v21\n"
"v_mov_b32 v51, 0xffff\n"
"s_nop 0\n"
"v_cndmask_b32 v51, 0, v51, vcc\n"
"v_cmp_eq_u32 vcc, 3, v21\n"
"v_mov_b32 v52, 0xffff\n"
"s_nop 0\n"
"v_cndmask_b32 v52, 0, v52, vcc\n"
"s_lshl_b32 s80, s90, 5\n"
"v_add_u32 v45, s80, v20\n"
"v_and_b32 v21, 1, v45\n"
"v_lshrrev_b32 v45, 1, v45\n"
"v_mul_u32_u24 v45, 73728, v45\n"
"v_lshl_add_u32 v45, v21, 6, v45\n"
"v_lshl_add_u32 v45, v22, 4, v45\n"
"s_lshr_b32 s80, s34, 1\n"
"s_lshl_b32 s80, s80, 4\n"
"s_add_u32 s80, s80, s66\n"
"s_lshl_b32 s80, s80, 1\n"
"s_and_b32 s81, s34, 1\n"
"s_add_u32 s80, s80, s81\n"
"s_lshr_b32 s81, s80, 1\n"
"s_mul_i32 s81, s81, 18\n"
"s_bitcmp1_b32 s80, 0\n"
"s_cselect_b32 s80, 9, 0\n"
"s_add_u32 s80, s80, s81\n"
"s_lshl_b32 s80, s80, 12\n"
"s_mul_i32 s81, s19, 10240\n"
"s_add_u32 s80, s80, s81\n"
"v_lshlrev_b32 v18, 7, v0\n"
"v_add_u32 v18, s80, v18\n"
"v_cmp_gt_u32 vcc, 40, v0\n"
"v_mov_b32 v19, 0x7fff0000\n"
"s_nop 0\n"
"v_cndmask_b32 v18, v19, v18, vcc\n"
"Lea_e%=:\n"
"s_and_b32 s87, s19, 1\n"
"s_lshl_b32 s88, s34, 1\n"
"s_add_u32 s87, s87, s88\n"
"s_mul_i32 s87, s87, 18432\n"
"s_lshr_b32 s88, s19, 1\n"
"s_mul_i32 s88, s88, 192\n"
"s_add_u32 s87, s87, s88\n"
"s_min_u32 s88, s67, 2\n"
"s_mul_i32 s88, s88, 384\n"
"s_add_u32 s87, s87, s88\n"
"s_add_u32 s87, s87, s89\n"
"v_add_u32 v20, 0, v0\n"
"v_mul_u32_u24 v23, 5462, v20\n"
"v_lshrrev_b32 v23, 16, v23\n"
"v_mul_u32_u24 v23, 960, v23\n"
"v_lshl_add_u32 v20, v20, 4, v23\n"
"v_add_u32 v20, s87, v20\n"
"v_add_u32 v20, 3072, v20\n"
"v_add_u32 v21, 64, v0\n"
"v_mul_u32_u24 v23, 5462, v21\n"
"v_lshrrev_b32 v23, 16, v23\n"
"v_mul_u32_u24 v23, 960, v23\n"
"v_lshl_add_u32 v21, v21, 4, v23\n"
"v_add_u32 v21, s87, v21\n"
"v_add_u32 v21, 2048, v21\n"
"v_add_u32 v22, 128, v0\n"
"v_mul_u32_u24 v23, 5462, v22\n"
"v_lshrrev_b32 v23, 16, v23\n"
"v_mul_u32_u24 v23, 960, v23\n"
"v_lshl_add_u32 v22, v22, 4, v23\n"
"v_add_u32 v22, s87, v22\n"
"v_add_u32 v22, 1024, v22\n"
"s_cmp_eq_u32 s67, 3\n"
"s_cbranch_scc1 Lprep%=\n"
"s_add_u32 m0, s62, 32768\n"
"s_nop 0\n"
"buffer_load_dwordx4 v56, s[20:23], s53 offen offset:0 lds\n"
"buffer_load_dwordx4 v57, s[20:23], s53 offen offset:1024 lds\n"
"buffer_load_dwordx4 v58, s[20:23], s53 offen offset:2048 lds\n"
"buffer_load_dwordx4 v59, s[20:23], s53 offen offset:3072 lds\n"
"s_add_u32 m0, s61, 16384\n"
"s_add_u32 s51, s41, s49\n"
"buffer_load_dwordx4 v10, s[24:27], s51 offen lds\n"
"buffer_load_dwordx4 v10, s[28:31], s51 offen offset:1024 lds\n"
"buffer_load_dwordx4 v11, s[24:27], s51 offen offset:2048 lds\n"
"buffer_load_dwordx4 v11, s[28:31], s51 offen offset:3072 lds\n"
"buffer_load_dwordx4 a[108:111], v15, s[72:75], s52 offen offset:0\n"
"buffer_load_dwordx4 a[124:127], v15, s[72:75], s52 offen offset:64\n"
"buffer_load_dwordx4 a[140:143], v15, s[72:75], s53 offen offset:0\n"
"buffer_load_dwordx4 a[156:159], v15, s[72:75], s53 offen offset:64\n"
"buffer_load_dwordx4 a[172:175], v15, s[72:75], s54 offen offset:0\n"
"buffer_load_dwordx4 a[188:191], v15, s[72:75], s54 offen offset:64\n"
"buffer_load_dwordx4 a[204:207], v15, s[72:75], s55 offen offset:0\n"
"buffer_load_dwordx4 a[220:223], v15, s[72:75], s55 offen offset:64\n"
"buffer_load_dwordx4 a[236:239], v15, s[72:75], s56 offen offset:0\n"
"buffer_load_dwordx4 a[252:255], v15, s[72:75], s56 offen offset:64\n"
"buffer_load_dwordx4 v[236:239], v15, s[72:75], s57 offen offset:0\n"
"buffer_load_dwordx4 v[252:255], v15, s[72:75], s57 offen offset:64\n"
"buffer_load_dwordx4 v[160:163], v15, s[72:75], s58 offen offset:0\n"
"buffer_load_dwordx4 v[164:167], v15, s[72:75], s58 offen offset:64\n"
"buffer_load_dwordx4 v[168:171], v15, s[72:75], s59 offen offset:0\n"
"buffer_load_dwordx4 v[172:175], v15, s[72:75], s59 offen offset:64\n"
"buffer_load_dwordx4 v[176:179], v15, s[72:75], s60 offen offset:0\n"
"buffer_load_dwordx4 v[180:183], v15, s[72:75], s60 offen offset:64\n"
"Lp1go%=:\n"
"s_mov_b32 s72, s24\n"
"s_mov_b32 s88, s28\n"
"s_mov_b32 s73, s25\n"
"s_mov_b32 s89, s29\n"
"s_mov_b32 s74, s26\n"
"s_mov_b32 s90, s30\n"
"s_mov_b32 s75, s27\n"
"s_mov_b32 s91, s31\n"
"s_cmp_eq_u32 s67, 3\n"
"s_cselect_b32 s80, 88, 32\n"
"s_cselect_b32 s81, 96, 48\n"
"s_load_dwordx2 s[84:85], s[16:17], s80\n"
"s_load_dwordx2 s[86:87], s[16:17], s81\n"
"s_waitcnt vmcnt(12)\n"
"s_barrier\n"
"ds_read_b128 a[0:3], v12 offset:49152\n"
"ds_read_b128 a[4:7], v12 offset:51200\n"
"ds_read_b128 a[8:11], v12 offset:53248\n"
"ds_read_b128 a[12:15], v12 offset:55296\n"
"ds_read_b128 a[16:19], v13 offset:49152\n"
"ds_read_b128 a[20:23], v13 offset:51200\n"
"ds_read_b128 a[24:27], v13 offset:53248\n"
"ds_read_b128 a[28:31], v13 offset:55296\n"
"ds_read_b64_tr_b16 v[64:65], v4 offset:0\n"
"ds_read_b64_tr_b16 v[66:67], v4 offset:512\n"
"ds_read_b64_tr_b16 v[80:81], v4 offset:2048\n"
"ds_read_b64_tr_b16 v[82:83], v4 offset:2560\n"
"ds_read_b64_tr_b16 v[68:69], v4 offset:1024\n"
"ds_read_b64_tr_b16 v[70:71], v4 offset:1536\n"
"ds_read_b64_tr_b16 v[84:85], v4 offset:3072\n"
"ds_read_b64_tr_b16 v[86:87], v4 offset:3584\n"
"ds_read_b64_tr_b16 v[72:73], v7 offset:0\n"
"ds_read_b64_tr_b16 v[74:75], v7 offset:512\n"
"ds_read_b64_tr_b16 v[88:89], v7 offset:2048\n"
"ds_read_b64_tr_b16 v[90:91], v7 offset:2560\n"
"v_readfirstlane_b32 s92, v242\n"
"v_readfirstlane_b32 s93, v243\n"
"s_nop 0\n"
"s_mul_i32 s92, s92, 0x2c9277b5\n"
"s_add_u32 s92, s92, 0xac564b05\n"
"s_mul_i32 s93, s93, 0x01c8e815\n"
"s_add_u32 s93, s93, 0x3c6ef35f\n"
"v_mov_b32 v62, s92\n"
"v_mov_b32 v63, s93\n"
"s_cmp_eq_u32 s67, 3\n"
"s_cbranch_scc1 Lad%=\n"
"v_readlane_b32 s79, v60, 31\n"
"v_readlane_b32 s80, v60, 27\n"
"s_nop 1\n"
"v_writelane_b32 v60, s80, 31\n"
"s_lshl_b32 s80, s66, 14\n"
"v_lshlrev_b32 v51, 18, v60\n"
"v_add_u32 v51, s80, v51\n"
"s_mul_i32 s80, s67, 384\n"
"v_mul_u32_u24 v52, 73728, v60\n"
"v_add_u32 v52, s80, v52\n"
"s_mul_i32 s80, s66, 640\n"
"s_add_u32 s80, s80, 33030144\n"
"v_lshlrev_b32 v53, 3, v60\n"
"v_add_u32 v53, s80, v53\n"
"v_lshlrev_b32 v19, 3, v60\n"
"v_add_u32 v19, 33065472, v19\n"
"v_cmp_lt_u32 vcc, 31, v0\n"
"s_nop 1\n"
"v_cndmask_b32 v53, v53, v19, vcc\n"
"Lad%=:\n"
"s_cmp_eq_u32 s19, 0\n"
"s_cselect_b64 vcc, -1, 0\n"
"v_mov_b32 v19, 0x7fff0000\n"
"v_cndmask_b32 v53, v19, v53, vcc\n"
"s_waitcnt lgkmcnt(0)\n"
"v_mfma_f32_16x16x32_bf16 v[96:99], v[64:67], a[0:3], 0\n"
"ds_read_b64_tr_b16 v[76:77], v7 offset:1024\n"
"s_add_u32 m0, s62, 16384\n"
"v_mfma_f32_16x16x32_bf16 v[96:99], v[80:83], a[16:19], v[96:99]\n"
"buffer_load_dwordx4 v56, s[20:23], s54 offen offset:0 lds\n"
"v_mfma_f32_16x16x32_bf16 v[100:103], v[80:83], a[20:23], 0\n"
"ds_read_b64_tr_b16 v[78:79], v7 offset:1536\n"
"buffer_load_dwordx4 v57, s[20:23], s54 offen offset:1024 lds\n"
"v_mfma_f32_16x16x32_bf16 v[100:103], v[64:67], a[4:7], v[100:103]\n"
"v_mfma_f32_16x16x32_bf16 v[104:107], v[64:67], a[8:11], 0\n"
"ds_read_b64_tr_b16 v[92:93], v7 offset:3072\n"
"buffer_load_dwordx4 v58, s[20:23], s54 offen offset:2048 lds\n"
"v_mfma_f32_16x16x32_bf16 v[104:107], v[80:83], a[24:27], v[104:107]\n"
"buffer_load_dwordx4 v59, s[20:23], s54 offen offset:3072 lds\n"
"v_mfma_f32_16x16x32_bf16 v[108:111], v[80:83], a[28:31], 0\n"
"v_mfma_f32_16x16x32_bf16 v[108:111], v[64:67], a[12:15], v[108:111]\n"
"ds_read_b64_tr_b16 v[94:95], v7 offset:3584\n"
"s_waitcnt lgkmcnt(4)\n"
"s_waitcnt vmcnt(4)\n"
"s_barrier\n"
"v_mfma_f32_16x16x32_bf16 v[124:127], v[68:71], a[12:15], 0\n"
"ds_read_b64_tr_b16 v[64:65], v4 offset:16384\n"
"ds_read_b128 a[32:35], v12 offset:32768\n"
"s_add_u32 m0, s61, 32768\n"
"s_add_u32 s51, s42, s49\n"
"v_mfma_f32_16x16x32_bf16 v[124:127], v[84:87], a[28:31], v[124:127]\n"
"buffer_load_dwordx4 v10, s[24:27], s51 offen lds\n"
"v_mfma_f32_16x16x32_bf16 v[120:123], v[84:87], a[24:27], 0\n"
"ds_read_b64_tr_b16 v[66:67], v4 offset:16896\n"
"ds_read_b128 a[36:39], v12 offset:34816\n"
"v_mfma_f32_16x16x32_bf16 v[120:123], v[68:71], a[8:11], v[120:123]\n"
"buffer_load_dwordx4 v10, s[28:31], s51 offen offset:1024 lds\n"
"v_mfma_f32_16x16x32_bf16 v[116:119], v[68:71], a[4:7], 0\n"
"ds_read_b64_tr_b16 v[80:81], v4 offset:18432\n"
"ds_read_b128 a[40:43], v12 offset:36864\n"
"v_mfma_f32_16x16x32_bf16 v[116:119], v[84:87], a[20:23], v[116:119]\n"
"buffer_load_dwordx4 v11, s[24:27], s51 offen offset:2048 lds\n"
"v_mfma_f32_16x16x32_bf16 v[112:115], v[84:87], a[16:19], 0\n"
"ds_read_b128 a[44:47], v12 offset:38912\n"
"v_mfma_f32_16x16x32_bf16 v[112:115], v[68:71], a[0:3], v[112:115]\n"
"ds_read_b64_tr_b16 v[82:83], v4 offset:18944\n"
"s_waitcnt lgkmcnt(12)\n"
"v_mfma_f32_16x16x32_bf16 v[128:131], v[72:75], a[0:3], 0\n"
"ds_read_b64_tr_b16 v[68:69], v4 offset:17408\n"
"ds_read_b128 a[48:51], v13 offset:32768\n"
"v_mfma_f32_16x16x32_bf16 v[128:131], v[88:91], a[16:19], v[128:131]\n"
"buffer_load_dwordx4 v11, s[28:31], s51 offen offset:3072 lds\n"
"v_mfma_f32_16x16x32_bf16 v[132:135], v[88:91], a[20:23], 0\n"
"ds_read_b64_tr_b16 v[70:71], v4 offset:17920\n"
"ds_read_b128 a[52:55], v13 offset:34816\n"
"s_add_u32 m0, s62, 0\n"
"v_mfma_f32_16x16x32_bf16 v[132:135], v[72:75], a[4:7], v[132:135]\n"
"buffer_load_dwordx4 v56, s[20:23], s55 offen offset:0 lds\n"
"v_mfma_f32_16x16x32_bf16 v[136:139], v[72:75], a[8:11], 0\n"
"ds_read_b64_tr_b16 v[84:85], v4 offset:19456\n"
"ds_read_b128 a[56:59], v13 offset:36864\n"
"v_mfma_f32_16x16x32_bf16 v[136:139], v[88:91], a[24:27], v[136:139]\n"
"buffer_load_dwordx4 v57, s[20:23], s55 offen offset:1024 lds\n"
"v_mfma_f32_16x16x32_bf16 v[140:143], v[88:91], a[28:31], 0\n"
"ds_read_b128 a[60:63], v13 offset:38912\n"
"v_mfma_f32_16x16x32_bf16 v[140:143], v[72:75], a[12:15], v[140:143]\n"
"ds_read_b64_tr_b16 v[86:87], v4 offset:19968\n"
"s_waitcnt lgkmcnt(15)\n"
"v_mfma_f32_16x16x32_bf16 v[156:159], v[76:79], a[12:15], 0\n"
"ds_read_b64_tr_b16 v[72:73], v7 offset:16384\n"
"v_mfma_f32_16x16x32_bf16 v[156:159], v[92:95], a[28:31], v[156:159]\n"
"buffer_load_dwordx4 v58, s[20:23], s55 offen offset:2048 lds\n"
"v_mfma_f32_16x16x32_bf16 v[152:155], v[92:95], a[24:27], 0\n"
"ds_read_b64_tr_b16 v[74:75], v7 offset:16896\n"
"v_mfma_f32_16x16x32_bf16 v[152:155], v[76:79], a[8:11], v[152:155]\n"
"buffer_load_dwordx4 v59, s[20:23], s55 offen offset:3072 lds\n"
"v_mfma_f32_16x16x32_bf16 v[148:151], v[76:79], a[4:7], 0\n"
"ds_read_b64_tr_b16 v[88:89], v7 offset:18432\n"
"v_mfma_f32_16x16x32_bf16 v[148:151], v[92:95], a[20:23], v[148:151]\n"
"s_add_u32 m0, s61, 49152\n"
"s_add_u32 s51, s43, s49\n"
"v_mfma_f32_16x16x32_bf16 v[144:147], v[92:95], a[16:19], 0\n"
"buffer_load_dwordx4 v10, s[24:27], s51 offen lds\n"
"v_mfma_f32_16x16x32_bf16 v[144:147], v[76:79], a[0:3], v[144:147]\n"
"ds_read_b64_tr_b16 v[90:91], v7 offset:18944\n"
"s_waitcnt lgkmcnt(5)\n"
"v_mfma_f32_16x16x32_bf16 v[96:99], v[64:67], a[32:35], v[96:99]\n"
"ds_read_b64_tr_b16 v[76:77], v7 offset:17408\n"
"v_mfma_f32_16x16x32_bf16 v[96:99], v[80:83], a[48:51], v[96:99]\n"
"buffer_load_dwordx4 v10, s[28:31], s51 offen offset:1024 lds\n"
"v_mfma_f32_16x16x32_bf16 v[100:103], v[80:83], a[52:55], v[100:103]\n"
"ds_read_b64_tr_b16 v[78:79], v7 offset:17920\n"
"v_mfma_f32_16x16x32_bf16 v[100:103], v[64:67], a[36:39], v[100:103]\n"
"buffer_load_dwordx4 v11, s[24:27], s51 offen offset:2048 lds\n"
"v_mfma_f32_16x16x32_bf16 v[104:107], v[64:67], a[40:43], v[104:107]\n"
"ds_read_b64_tr_b16 v[92:93], v7 offset:19456\n"
"v_mfma_f32_16x16x32_bf16 v[104:107], v[80:83], a[56:59], v[104:107]\n"
"v_mfma_f32_16x16x32_bf16 v[108:111], v[80:83], a[60:63], v[108:111]\n"
"buffer_load_dwordx4 v11, s[28:31], s51 offen offset:3072 lds\n"
"v_mfma_f32_16x16x32_bf16 v[108:111], v[64:67], a[44:47], v[108:111]\n"
"ds_read_b64_tr_b16 v[94:95], v7 offset:19968\n"
"s_waitcnt lgkmcnt(8)\n"
"s_waitcnt vmcnt(8)\n"
"s_barrier\n"
"v_mfma_f32_16x16x32_bf16 v[124:127], v[68:71], a[44:47], v[124:127]\n"
"ds_read_b64_tr_b16 v[64:65], v4 offset:32768\n"
"ds_read_b128 a[64:67], v12 offset:16384\n"
"s_add_u32 m0, s62, 49152\n"
"v_mfma_f32_16x16x32_bf16 v[124:127], v[84:87], a[60:63], v[124:127]\n"
"buffer_load_dwordx4 v56, s[20:23], s56 offen offset:0 lds\n"
"v_mfma_f32_16x16x32_bf16 v[120:123], v[84:87], a[56:59], v[120:123]\n"
"ds_read_b64_tr_b16 v[66:67], v4 offset:33280\n"
"ds_read_b128 a[68:71], v12 offset:18432\n"
"v_mfma_f32_16x16x32_bf16 v[120:123], v[68:71], a[40:43], v[120:123]\n"
"v_mfma_f32_16x16x32_bf16 v[116:119], v[68:71], a[36:39], v[116:119]\n"
"ds_read_b64_tr_b16 v[80:81], v4 offset:34816\n"
"ds_read_b128 a[72:75], v12 offset:20480\n"
"v_mfma_f32_16x16x32_bf16 v[116:119], v[84:87], a[52:55], v[116:119]\n"
"buffer_load_dwordx4 v57, s[20:23], s56 offen offset:1024 lds\n"
"v_mfma_f32_16x16x32_bf16 v[112:115], v[84:87], a[48:51], v[112:115]\n"
"ds_read_b128 a[76:79], v12 offset:22528\n"
"v_mfma_f32_16x16x32_bf16 v[112:115], v[68:71], a[32:35], v[112:115]\n"
"ds_read_b64_tr_b16 v[82:83], v4 offset:35328\n"
"s_waitcnt lgkmcnt(12)\n"
"v_mfma_f32_16x16x32_bf16 v[128:131], v[72:75], a[32:35], v[128:131]\n"
"ds_read_b64_tr_b16 v[68:69], v4 offset:33792\n"
"ds_read_b128 a[80:83], v13 offset:16384\n"
"v_mfma_f32_16x16x32_bf16 v[128:131], v[88:91], a[48:51], v[128:131]\n"
"buffer_load_dwordx4 v58, s[20:23], s56 offen offset:2048 lds\n"
"v_mfma_f32_16x16x32_bf16 v[132:135], v[88:91], a[52:55], v[132:135]\n"
"ds_read_b64_tr_b16 v[70:71], v4 offset:34304\n"
"ds_read_b128 a[84:87], v13 offset:18432\n"
"v_mfma_f32_16x16x32_bf16 v[132:135], v[72:75], a[36:39], v[132:135]\n"
"v_mfma_f32_16x16x32_bf16 v[136:139], v[72:75], a[40:43], v[136:139]\n"
"ds_read_b64_tr_b16 v[84:85], v4 offset:35840\n"
"ds_read_b128 a[88:91], v13 offset:20480\n"
"v_mfma_f32_16x16x32_bf16 v[136:139], v[88:91], a[56:59], v[136:139]\n"
"buffer_load_dwordx4 v59, s[20:23], s56 offen offset:3072 lds\n"
"v_mfma_f32_16x16x32_bf16 v[140:143], v[88:91], a[60:63], v[140:143]\n"
"ds_read_b128 a[92:95], v13 offset:22528\n"
"s_add_u32 m0, s61, 65536\n"
"v_mfma_f32_16x16x32_bf16 v[140:143], v[72:75], a[44:47], v[140:143]\n"
"ds_read_b64_tr_b16 v[86:87], v4 offset:36352\n"
"s_waitcnt lgkmcnt(15)\n"
"v_mfma_f32_16x16x32_bf16 v[156:159], v[76:79], a[44:47], v[156:159]\n"
"ds_read_b64_tr_b16 v[72:73], v7 offset:32768\n"
"s_add_u32 s51, s44, s49\n"
"v_mfma_f32_16x16x32_bf16 v[156:159], v[92:95], a[60:63], v[156:159]\n"
"buffer_load_dwordx4 v10, s[24:27], s51 offen lds\n"
"v_mfma_f32_16x16x32_bf16 v[152:155], v[92:95], a[56:59], v[152:155]\n"
"ds_read_b64_tr_b16 v[74:75], v7 offset:33280\n"
"v_mfma_f32_16x16x32_bf16 v[152:155], v[76:79], a[40:43], v[152:155]\n"
"v_mfma_f32_16x16x32_bf16 v[148:151], v[76:79], a[36:39], v[148:151]\n"
"ds_read_b64_tr_b16 v[88:89], v7 offset:34816\n"
"v_mfma_f32_16x16x32_bf16 v[148:151], v[92:95], a[52:55], v[148:151]\n"
"buffer_load_dwordx4 v10, s[28:31], s51 offen offset:1024 lds\n"
"v_mfma_f32_16x16x32_bf16 v[144:147], v[92:95], a[48:51], v[144:147]\n"
"v_mfma_f32_16x16x32_bf16 v[144:147], v[76:79], a[32:35], v[144:147]\n"
"ds_read_b64_tr_b16 v[90:91], v7 offset:35328\n"
"s_waitcnt lgkmcnt(5)\n"
"v_mfma_f32_16x16x32_bf16 v[96:99], v[64:67], a[64:67], v[96:99]\n"
"ds_read_b64_tr_b16 v[76:77], v7 offset:33792\n"
"v_mfma_f32_16x16x32_bf16 v[96:99], v[80:83], a[80:83], v[96:99]\n"
"buffer_load_dwordx4 v11, s[24:27], s51 offen offset:2048 lds\n"
"v_mfma_f32_16x16x32_bf16 v[100:103], v[80:83], a[84:87], v[100:103]\n"
"ds_read_b64_tr_b16 v[78:79], v7 offset:34304\n"
"v_mfma_f32_16x16x32_bf16 v[100:103], v[64:67], a[68:71], v[100:103]\n"
"v_mfma_f32_16x16x32_bf16 v[28:31], a[108:111], a[0:3], 0\n"
"v_mfma_f32_16x16x32_bf16 v[28:31], a[124:127], a[16:19], v[28:31]\n"
"v_mfma_f32_16x16x32_bf16 v[104:107], v[64:67], a[72:75], v[104:107]\n"
"ds_read_b64_tr_b16 v[92:93], v7 offset:35840\n"
"v_mfma_f32_16x16x32_bf16 v[104:107], v[80:83], a[88:91], v[104:107]\n"
"buffer_load_dwordx4 v11, s[28:31], s51 offen offset:3072 lds\n"
"v_mfma_f32_16x16x32_bf16 v[108:111], v[80:83], a[92:95], v[108:111]\n"
"v_mfma_f32_16x16x32_bf16 v[108:111], v[64:67], a[76:79], v[108:111]\n"
"ds_read_b64_tr_b16 v[94:95], v7 offset:36352\n"
"s_waitcnt lgkmcnt(8)\n"
"s_waitcnt vmcnt(8)\n"
"s_barrier\n"
"v_mfma_f32_16x16x32_bf16 v[124:127], v[68:71], a[76:79], v[124:127]\n"
"ds_read_b64_tr_b16 v[64:65], v4 offset:49152\n"
"ds_read_b128 a[96:99], v12 offset:0\n"
"s_add_u32 m0, s62, 32768\n"
"v_mfma_f32_16x16x32_bf16 v[124:127], v[84:87], a[92:95], v[124:127]\n"
"buffer_load_dwordx4 v56, s[20:23], s57 offen offset:0 lds\n"
"v_mfma_f32_16x16x32_bf16 v[120:123], v[84:87], a[88:91], v[120:123]\n"
"ds_read_b64_tr_b16 v[66:67], v4 offset:49664\n"
"ds_read_b128 a[100:103], v12 offset:2048\n"
"v_mfma_f32_16x16x32_bf16 v[120:123], v[68:71], a[72:75], v[120:123]\n"
"v_mfma_f32_16x16x32_bf16 v[116:119], v[68:71], a[68:71], v[116:119]\n"
"ds_read_b64_tr_b16 v[80:81], v4 offset:51200\n"
"ds_read_b128 a[104:107], v12 offset:4096\n"
"v_mfma_f32_16x16x32_bf16 v[116:119], v[84:87], a[84:87], v[116:119]\n"
"buffer_load_dwordx4 v57, s[20:23], s57 offen offset:1024 lds\n"
"v_mfma_f32_16x16x32_bf16 v[112:115], v[84:87], a[80:83], v[112:115]\n"
"ds_read_b128 a[108:111], v12 offset:6144\n"
"v_mfma_f32_16x16x32_bf16 v[112:115], v[68:71], a[64:67], v[112:115]\n"
"ds_read_b64_tr_b16 v[82:83], v4 offset:51712\n"
"s_waitcnt lgkmcnt(12)\n"
"v_mfma_f32_16x16x32_bf16 v[128:131], v[72:75], a[64:67], v[128:131]\n"
"ds_read_b64_tr_b16 v[68:69], v4 offset:50176\n"
"ds_read_b128 a[112:115], v13 offset:0\n"
"v_mfma_f32_16x16x32_bf16 v[128:131], v[88:91], a[80:83], v[128:131]\n"
"buffer_load_dwordx4 v58, s[20:23], s57 offen offset:2048 lds\n"
"v_mfma_f32_16x16x32_bf16 v[132:135], v[88:91], a[84:87], v[132:135]\n"
"ds_read_b64_tr_b16 v[70:71], v4 offset:50688\n"
"ds_read_b128 a[116:119], v13 offset:2048\n"
"v_mfma_f32_16x16x32_bf16 v[132:135], v[72:75], a[68:71], v[132:135]\n"
"v_mfma_f32_16x16x32_bf16 v[136:139], v[72:75], a[72:75], v[136:139]\n"
"ds_read_b64_tr_b16 v[84:85], v4 offset:52224\n"
"ds_read_b128 a[120:123], v13 offset:4096\n"
"v_mfma_f32_16x16x32_bf16 v[136:139], v[88:91], a[88:91], v[136:139]\n"
"buffer_load_dwordx4 v59, s[20:23], s57 offen offset:3072 lds\n"
"v_mfma_f32_16x16x32_bf16 v[140:143], v[88:91], a[92:95], v[140:143]\n"
"ds_read_b128 a[124:127], v13 offset:6144\n"
"s_add_u32 m0, s61, 81920\n"
"v_mfma_f32_16x16x32_bf16 v[140:143], v[72:75], a[76:79], v[140:143]\n"
"ds_read_b64_tr_b16 v[86:87], v4 offset:52736\n"
"s_waitcnt lgkmcnt(15)\n"
"v_mfma_f32_16x16x32_bf16 v[156:159], v[76:79], a[76:79], v[156:159]\n"
"ds_read_b64_tr_b16 v[72:73], v7 offset:49152\n"
"s_add_u32 s51, s45, s49\n"
"v_mfma_f32_16x16x32_bf16 v[156:159], v[92:95], a[92:95], v[156:159]\n"
"buffer_load_dwordx4 v10, s[24:27], s51 offen lds\n"
"v_mfma_f32_16x16x32_bf16 v[152:155], v[92:95], a[88:91], v[152:155]\n"
"ds_read_b64_tr_b16 v[74:75], v7 offset:49664\n"
"v_mfma_f32_16x16x32_bf16 v[152:155], v[76:79], a[72:75], v[152:155]\n"
"v_mfma_f32_16x16x32_bf16 v[148:151], v[76:79], a[68:71], v[148:151]\n"
"ds_read_b64_tr_b16 v[88:89], v7 offset:51200\n"
"v_mfma_f32_16x16x32_bf16 v[148:151], v[92:95], a[84:87], v[148:151]\n"
"buffer_load_dwordx4 v10, s[28:31], s51 offen offset:1024 lds\n"
"v_mfma_f32_16x16x32_bf16 v[144:147], v[92:95], a[80:83], v[144:147]\n"
"v_mfma_f32_16x16x32_bf16 v[144:147], v[76:79], a[64:67], v[144:147]\n"
"ds_read_b64_tr_b16 v[90:91], v7 offset:51712\n"
"s_waitcnt lgkmcnt(5)\n"
"v_mfma_f32_16x16x32_bf16 v[96:99], v[64:67], a[96:99], v[96:99]\n"
"ds_read_b64_tr_b16 v[76:77], v7 offset:50176\n"
"v_mfma_f32_16x16x32_bf16 v[96:99], v[80:83], a[112:115], v[96:99]\n"
"buffer_load_dwordx4 v11, s[24:27], s51 offen offset:2048 lds\n"
"v_mfma_f32_16x16x32_bf16 v[100:103], v[80:83], a[116:119], v[100:103]\n"
"ds_read_b64_tr_b16 v[78:79], v7 offset:50688\n"
"v_mfma_f32_16x16x32_bf16 v[100:103], v[64:67], a[100:103], v[100:103]\n"
"v_mfma_f32_16x16x32_bf16 v[28:31], a[140:143], a[32:35], v[28:31]\n"
"v_mfma_f32_16x16x32_bf16 v[28:31], a[156:159], a[48:51], v[28:31]\n"
"v_mfma_f32_16x16x32_bf16 v[104:107], v[64:67], a[104:107], v[104:107]\n"
"ds_read_b64_tr_b16 v[92:93], v7 offset:52224\n"
"v_mfma_f32_16x16x32_bf16 v[104:107], v[80:83], a[120:123], v[104:107]\n"
"buffer_load_dwordx4 v11, s[28:31], s51 offen offset:3072 lds\n"
"v_mfma_f32_16x16x32_bf16 v[108:111], v[80:83], a[124:127], v[108:111]\n"
"v_mfma_f32_16x16x32_bf16 v[108:111], v[64:67], a[108:111], v[108:111]\n"
"ds_read_b64_tr_b16 v[94:95], v7 offset:52736\n"
"s_waitcnt lgkmcnt(8)\n"
"s_waitcnt vmcnt(8)\n"
"s_barrier\n"
"v_mfma_f32_16x16x32_bf16 v[124:127], v[68:71], a[108:111], v[124:127]\n"
"ds_read_b64_tr_b16 v[64:65], v5 offset:0\n"
"ds_read_b128 a[128:131], v12 offset:49152\n"
"s_add_u32 m0, s62, 16384\n"
"v_mfma_f32_16x16x32_bf16 v[124:127], v[84:87], a[124:127], v[124:127]\n"
"buffer_load_dwordx4 v56, s[20:23], s58 offen offset:0 lds\n"
"v_mfma_f32_16x16x32_bf16 v[120:123], v[84:87], a[120:123], v[120:123]\n"
"ds_read_b64_tr_b16 v[66:67], v5 offset:512\n"
"ds_read_b128 a[132:135], v12 offset:51200\n"
"v_mfma_f32_16x16x32_bf16 v[120:123], v[68:71], a[104:107], v[120:123]\n"
"v_mfma_f32_16x16x32_bf16 v[116:119], v[68:71], a[100:103], v[116:119]\n"
"ds_read_b64_tr_b16 v[80:81], v5 offset:2048\n"
"ds_read_b128 a[136:139], v12 offset:53248\n"
"v_mfma_f32_16x16x32_bf16 v[116:119], v[84:87], a[116:119], v[116:119]\n"
"buffer_load_dwordx4 v57, s[20:23], s58 offen offset:1024 lds\n"
"v_mfma_f32_16x16x32_bf16 v[112:115], v[84:87], a[112:115], v[112:115]\n"
"ds_read_b128 a[140:143], v12 offset:55296\n"
"v_mfma_f32_16x16x32_bf16 v[112:115], v[68:71], a[96:99], v[112:115]\n"
"ds_read_b64_tr_b16 v[82:83], v5 offset:2560\n"
"s_waitcnt lgkmcnt(12)\n"
"v_mfma_f32_16x16x32_bf16 v[128:131], v[72:75], a[96:99], v[128:131]\n"
"ds_read_b64_tr_b16 v[68:69], v5 offset:1024\n"
"ds_read_b128 a[144:147], v13 offset:49152\n"
"v_mfma_f32_16x16x32_bf16 v[128:131], v[88:91], a[112:115], v[128:131]\n"
"buffer_load_dwordx4 v58, s[20:23], s58 offen offset:2048 lds\n"
"v_mfma_f32_16x16x32_bf16 v[132:135], v[88:91], a[116:119], v[132:135]\n"
"ds_read_b64_tr_b16 v[70:71], v5 offset:1536\n"
"ds_read_b128 a[148:151], v13 offset:51200\n"
"v_mfma_f32_16x16x32_bf16 v[132:135], v[72:75], a[100:103], v[132:135]\n"
"v_mfma_f32_16x16x32_bf16 v[136:139], v[72:75], a[104:107], v[136:139]\n"
"ds_read_b64_tr_b16 v[84:85], v5 offset:3072\n"
"ds_read_b128 a[152:155], v13 offset:53248\n"
"v_mfma_f32_16x16x32_bf16 v[136:139], v[88:91], a[120:123], v[136:139]\n"
"buffer_load_dwordx4 v59, s[20:23], s58 offen offset:3072 lds\n"
"v_mfma_f32_16x16x32_bf16 v[140:143], v[88:91], a[124:127], v[140:143]\n"
"ds_read_b128 a[156:159], v13 offset:55296\n"
"s_add_u32 m0, s61, 0\n"
"v_mfma_f32_16x16x32_bf16 v[140:143], v[72:75], a[108:111], v[140:143]\n"
"ds_read_b64_tr_b16 v[86:87], v5 offset:3584\n"
"s_waitcnt lgkmcnt(15)\n"
"v_mfma_f32_16x16x32_bf16 v[156:159], v[76:79], a[108:111], v[156:159]\n"
"ds_read_b64_tr_b16 v[72:73], v8 offset:0\n"
"s_add_u32 s51, s46, s49\n"
"v_mfma_f32_16x16x32_bf16 v[156:159], v[92:95], a[124:127], v[156:159]\n"
"buffer_load_dwordx4 v10, s[24:27], s51 offen lds\n"
"v_mfma_f32_16x16x32_bf16 v[152:155], v[92:95], a[120:123], v[152:155]\n"
"ds_read_b64_tr_b16 v[74:75], v8 offset:512\n"
"v_mfma_f32_16x16x32_bf16 v[152:155], v[76:79], a[104:107], v[152:155]\n"
"v_mfma_f32_16x16x32_bf16 v[148:151], v[76:79], a[100:103], v[148:151]\n"
"ds_read_b64_tr_b16 v[88:89], v8 offset:2048\n"
"v_mfma_f32_16x16x32_bf16 v[148:151], v[92:95], a[116:119], v[148:151]\n"
"buffer_load_dwordx4 v10, s[28:31], s51 offen offset:1024 lds\n"
"v_mfma_f32_16x16x32_bf16 v[144:147], v[92:95], a[112:115], v[144:147]\n"
"v_mfma_f32_16x16x32_bf16 v[144:147], v[76:79], a[96:99], v[144:147]\n"
"ds_read_b64_tr_b16 v[90:91], v8 offset:2560\n"
"s_waitcnt lgkmcnt(5)\n"
"v_mfma_f32_16x16x32_bf16 v[96:99], v[64:67], a[128:131], v[96:99]\n"
"ds_read_b64_tr_b16 v[76:77], v8 offset:1024\n"
"v_mfma_f32_16x16x32_bf16 v[96:99], v[80:83], a[144:147], v[96:99]\n"
"buffer_load_dwordx4 v11, s[24:27], s51 offen offset:2048 lds\n"
"v_mfma_f32_16x16x32_bf16 v[100:103], v[80:83], a[148:151], v[100:103]\n"
"ds_read_b64_tr_b16 v[78:79], v8 offset:1536\n"
"v_mfma_f32_16x16x32_bf16 v[100:103], v[64:67], a[132:135], v[100:103]\n"
"v_mfma_f32_16x16x32_bf16 v[28:31], a[172:175], a[64:67], v[28:31]\n"
"v_mfma_f32_16x16x32_bf16 v[28:31], a[188:191], a[80:83], v[28:31]\n"
"v_mfma_f32_16x16x32_bf16 v[104:107], v[64:67], a[136:139], v[104:107]\n"
"ds_read_b64_tr_b16 v[92:93], v8 offset:3072\n"
"v_mfma_f32_16x16x32_bf16 v[104:107], v[80:83], a[152:155], v[104:107]\n"
"buffer_load_dwordx4 v11, s[28:31], s51 offen offset:3072 lds\n"
"v_mfma_f32_16x16x32_bf16 v[108:111], v[80:83], a[156:159], v[108:111]\n"
"v_mfma_f32_16x16x32_bf16 v[108:111], v[64:67], a[140:143], v[108:111]\n"
"ds_read_b64_tr_b16 v[94:95], v8 offset:3584\n"
"s_waitcnt lgkmcnt(8)\n"
"s_waitcnt vmcnt(8)\n"
"s_barrier\n"
"v_mfma_f32_16x16x32_bf16 v[124:127], v[68:71], a[140:143], v[124:127]\n"
"ds_read_b64_tr_b16 v[64:65], v5 offset:16384\n"
"ds_read_b128 a[160:163], v12 offset:32768\n"
"s_add_u32 m0, s62, 0\n"
"v_mfma_f32_16x16x32_bf16 v[124:127], v[84:87], a[156:159], v[124:127]\n"
"buffer_load_dwordx4 v56, s[20:23], s59 offen offset:0 lds\n"
"v_mfma_f32_16x16x32_bf16 v[120:123], v[84:87], a[152:155], v[120:123]\n"
"ds_read_b64_tr_b16 v[66:67], v5 offset:16896\n"
"ds_read_b128 a[164:167], v12 offset:34816\n"
"v_mfma_f32_16x16x32_bf16 v[120:123], v[68:71], a[136:139], v[120:123]\n"
"v_mfma_f32_16x16x32_bf16 v[116:119], v[68:71], a[132:135], v[116:119]\n"
"ds_read_b64_tr_b16 v[80:81], v5 offset:18432\n"
"ds_read_b128 a[168:171], v12 offset:36864\n"
"v_mfma_f32_16x16x32_bf16 v[116:119], v[84:87], a[148:151], v[116:119]\n"
"buffer_load_dwordx4 v57, s[20:23], s59 offen offset:1024 lds\n"
"v_mfma_f32_16x16x32_bf16 v[112:115], v[84:87], a[144:147], v[112:115]\n"
"ds_read_b128 a[172:175], v12 offset:38912\n"
"v_mfma_f32_16x16x32_bf16 v[112:115], v[68:71], a[128:131], v[112:115]\n"
"ds_read_b64_tr_b16 v[82:83], v5 offset:18944\n"
"s_waitcnt lgkmcnt(12)\n"
"v_mfma_f32_16x16x32_bf16 v[128:131], v[72:75], a[128:131], v[128:131]\n"
"ds_read_b64_tr_b16 v[68:69], v5 offset:17408\n"
"ds_read_b128 a[176:179], v13 offset:32768\n"
"v_mfma_f32_16x16x32_bf16 v[128:131], v[88:91], a[144:147], v[128:131]\n"
"buffer_load_dwordx4 v58, s[20:23], s59 offen offset:2048 lds\n"
"v_mfma_f32_16x16x32_bf16 v[132:135], v[88:91], a[148:151], v[132:135]\n"
"ds_read_b64_tr_b16 v[70:71], v5 offset:17920\n"
"ds_read_b128 a[180:183], v13 offset:34816\n"
"v_mfma_f32_16x16x32_bf16 v[132:135], v[72:75], a[132:135], v[132:135]\n"
"v_mfma_f32_16x16x32_bf16 v[136:139], v[72:75], a[136:139], v[136:139]\n"
"ds_read_b64_tr_b16 v[84:85], v5 offset:19456\n"
"ds_read_b128 a[184:187], v13 offset:36864\n"
"v_mfma_f32_16x16x32_bf16 v[136:139], v[88:91], a[152:155], v[136:139]\n"
"buffer_load_dwordx4 v59, s[20:23], s59 offen offset:3072 lds\n"
"v_mfma_f32_16x16x32_bf16 v[140:143], v[88:91], a[156:159], v[140:143]\n"
"ds_read_b128 a[188:191], v13 offset:38912\n"
"s_add_u32 m0, s61, 16384\n"
"v_mfma_f32_16x16x32_bf16 v[140:143], v[72:75], a[140:143], v[140:143]\n"
"ds_read_b64_tr_b16 v[86:87], v5 offset:19968\n"
"s_waitcnt lgkmcnt(15)\n"
"v_mfma_f32_16x16x32_bf16 v[156:159], v[76:79], a[140:143], v[156:159]\n"
"ds_read_b64_tr_b16 v[72:73], v8 offset:16384\n"
"s_add_u32 s51, s47, s49\n"
"v_mfma_f32_16x16x32_bf16 v[156:159], v[92:95], a[156:159], v[156:159]\n"
"buffer_load_dwordx4 v10, s[24:27], s51 offen lds\n"
"v_mfma_f32_16x16x32_bf16 v[152:155], v[92:95], a[152:155], v[152:155]\n"
"ds_read_b64_tr_b16 v[74:75], v8 offset:16896\n"
"v_mfma_f32_16x16x32_bf16 v[152:155], v[76:79], a[136:139], v[152:155]\n"
"v_mfma_f32_16x16x32_bf16 v[148:151], v[76:79], a[132:135], v[148:151]\n"
"ds_read_b64_tr_b16 v[88:89], v8 offset:18432\n"
"v_mfma_f32_16x16x32_bf16 v[148:151], v[92:95], a[148:151], v[148:151]\n"
"buffer_load_dwordx4 v10, s[28:31], s51 offen offset:1024 lds\n"
"v_mfma_f32_16x16x32_bf16 v[144:147], v[92:95], a[144:147], v[144:147]\n"
"v_mfma_f32_16x16x32_bf16 v[144:147], v[76:79], a[128:131], v[144:147]\n"
"ds_read_b64_tr_b16 v[90:91], v8 offset:18944\n"
"s_waitcnt lgkmcnt(5)\n"
"v_mfma_f32_16x16x32_bf16 v[96:99], v[64:67], a[160:163], v[96:99]\n"
"ds_read_b64_tr_b16 v[76:77], v8 offset:17408\n"
"v_mfma_f32_16x16x32_bf16 v[96:99], v[80:83], a[176:179], v[96:99]\n"
"buffer_load_dwordx4 v11, s[24:27], s51 offen offset:2048 lds\n"
"v_mfma_f32_16x16x32_bf16 v[100:103], v[80:83], a[180:183], v[100:103]\n"
"ds_read_b64_tr_b16 v[78:79], v8 offset:17920\n"
"v_mfma_f32_16x16x32_bf16 v[100:103], v[64:67], a[164:167], v[100:103]\n"
"v_mfma_f32_16x16x32_bf16 v[28:31], a[204:207], a[96:99], v[28:31]\n"
"v_mfma_f32_16x16x32_bf16 v[28:31], a[220:223], a[112:115], v[28:31]\n"
"v_mfma_f32_16x16x32_bf16 v[104:107], v[64:67], a[168:171], v[104:107]\n"
"ds_read_b64_tr_b16 v[92:93], v8 offset:19456\n"
"v_mfma_f32_16x16x32_bf16 v[104:107], v[80:83], a[184:187], v[104:107]\n"
"buffer_load_dwordx4 v11, s[28:31], s51 offen offset:3072 lds\n"
"v_mfma_f32_16x16x32_bf16 v[108:111], v[80:83], a[188:191], v[108:111]\n"
"v_mfma_f32_16x16x32_bf16 v[108:111], v[64:67], a[172:175], v[108:111]\n"
"ds_read_b64_tr_b16 v[94:95], v8 offset:19968\n"
"s_waitcnt lgkmcnt(8)\n"
"s_waitcnt vmcnt(8)\n"
"s_barrier\n"
"v_mfma_f32_16x16x32_bf16 v[124:127], v[68:71], a[172:175], v[124:127]\n"
"ds_read_b64_tr_b16 v[64:65], v4 offset:0\n"
"ds_read_b128 a[192:195], v12 offset:16384\n"
"s_add_u32 m0, s62, 49152\n"
"v_mfma_f32_16x16x32_bf16 v[124:127], v[84:87], a[188:191], v[124:127]\n"
"buffer_load_dwordx4 v56, s[20:23], s60 offen offset:0 lds\n"
"v_mfma_f32_16x16x32_bf16 v[120:123], v[84:87], a[184:187], v[120:123]\n"
"ds_read_b64_tr_b16 v[66:67], v4 offset:512\n"
"ds_read_b128 a[196:199], v12 offset:18432\n"
"v_mfma_f32_16x16x32_bf16 v[120:123], v[68:71], a[168:171], v[120:123]\n"
"buffer_load_dwordx4 v57, s[20:23], s60 offen offset:1024 lds\n"
"v_mfma_f32_16x16x32_bf16 v[116:119], v[68:71], a[164:167], v[116:119]\n"
"ds_read_b64_tr_b16 v[80:81], v4 offset:2048\n"
"ds_read_b128 a[200:203], v12 offset:20480\n"
"v_mfma_f32_16x16x32_bf16 v[116:119], v[84:87], a[180:183], v[116:119]\n"
"buffer_load_dwordx4 v58, s[20:23], s60 offen offset:2048 lds\n"
"v_mfma_f32_16x16x32_bf16 v[112:115], v[84:87], a[176:179], v[112:115]\n"
"ds_read_b128 a[204:207], v12 offset:22528\n"
"v_mfma_f32_16x16x32_bf16 v[112:115], v[68:71], a[160:163], v[112:115]\n"
"ds_read_b64_tr_b16 v[82:83], v4 offset:2560\n"
"s_waitcnt lgkmcnt(12)\n"
"v_mfma_f32_16x16x32_bf16 v[128:131], v[72:75], a[160:163], v[128:131]\n"
"ds_read_b64_tr_b16 v[68:69], v4 offset:1024\n"
"ds_read_b128 a[208:211], v13 offset:16384\n"
"v_mfma_f32_16x16x32_bf16 v[128:131], v[88:91], a[176:179], v[128:131]\n"
"buffer_load_dwordx4 v59, s[20:23], s60 offen offset:3072 lds\n"
"v_mfma_f32_16x16x32_bf16 v[132:135], v[88:91], a[180:183], v[132:135]\n"
"ds_read_b64_tr_b16 v[70:71], v4 offset:1536\n"
"ds_read_b128 a[212:215], v13 offset:18432\n"
"s_add_u32 m0, s61, 32768\n"
"s_add_u32 s51, s48, s49\n"
"v_mfma_f32_16x16x32_bf16 v[132:135], v[72:75], a[164:167], v[132:135]\n"
"buffer_load_dwordx4 v10, s[24:27], s51 offen lds\n"
"v_mfma_f32_16x16x32_bf16 v[136:139], v[72:75], a[168:171], v[136:139]\n"
"ds_read_b64_tr_b16 v[84:85], v4 offset:3072\n"
"ds_read_b128 a[216:219], v13 offset:20480\n"
"v_mfma_f32_16x16x32_bf16 v[136:139], v[88:91], a[184:187], v[136:139]\n"
"buffer_load_dwordx4 v10, s[28:31], s51 offen offset:1024 lds\n"
"v_mfma_f32_16x16x32_bf16 v[140:143], v[88:91], a[188:191], v[140:143]\n"
"ds_read_b128 a[220:223], v13 offset:22528\n"
"v_mfma_f32_16x16x32_bf16 v[140:143], v[72:75], a[172:175], v[140:143]\n"
"ds_read_b64_tr_b16 v[86:87], v4 offset:3584\n"
"s_waitcnt lgkmcnt(15)\n"
"v_mfma_f32_16x16x32_bf16 v[156:159], v[76:79], a[172:175], v[156:159]\n"
"ds_read_b64_tr_b16 v[72:73], v7 offset:0\n"
"v_mfma_f32_16x16x32_bf16 v[156:159], v[92:95], a[188:191], v[156:159]\n"
"buffer_load_dwordx4 v11, s[24:27], s51 offen offset:2048 lds\n"
"v_mfma_f32_16x16x32_bf16 v[152:155], v[92:95], a[184:187], v[152:155]\n"
"ds_read_b64_tr_b16 v[74:75], v7 offset:512\n"
"v_mfma_f32_16x16x32_bf16 v[152:155], v[76:79], a[168:171], v[152:155]\n"
"buffer_load_dwordx4 v11, s[28:31], s51 offen offset:3072 lds\n"
"v_mfma_f32_16x16x32_bf16 v[148:151], v[76:79], a[164:167], v[148:151]\n"
"ds_read_b64_tr_b16 v[88:89], v7 offset:2048\n"
"v_mfma_f32_16x16x32_bf16 v[148:151], v[92:95], a[180:183], v[148:151]\n"
"s_add_u32 m0, s61, 49152\n"
"s_add_u32 s51, s40, s50\n"
"v_mfma_f32_16x16x32_bf16 v[144:147], v[92:95], a[176:179], v[144:147]\n"
"buffer_load_dwordx4 v10, s[72:75], s51 offen lds\n"
"v_mfma_f32_16x16x32_bf16 v[144:147], v[76:79], a[160:163], v[144:147]\n"
"ds_read_b64_tr_b16 v[90:91], v7 offset:2560\n"
"s_waitcnt lgkmcnt(5)\n"
"v_mfma_f32_16x16x32_bf16 v[96:99], v[64:67], a[192:195], v[96:99]\n"
"ds_read_b64_tr_b16 v[76:77], v7 offset:1024\n"
"v_mfma_f32_16x16x32_bf16 v[96:99], v[80:83], a[208:211], v[96:99]\n"
"buffer_load_dwordx4 v10, s[88:91], s51 offen offset:1024 lds\n"
"v_mfma_f32_16x16x32_bf16 v[100:103], v[80:83], a[212:215], v[100:103]\n"
"ds_read_b64_tr_b16 v[78:79], v7 offset:1536\n"
"v_mfma_f32_16x16x32_bf16 v[100:103], v[64:67], a[196:199], v[100:103]\n"
"v_mfma_f32_16x16x32_bf16 v[28:31], a[236:239], a[128:131], v[28:31]\n"
"v_mfma_f32_16x16x32_bf16 v[28:31], a[252:255], a[144:147], v[28:31]\n"
"v_mfma_f32_16x16x32_bf16 v[104:107], v[64:67], a[200:203], v[104:107]\n"
"ds_read_b64_tr_b16 v[92:93], v7 offset:3072\n"
"v_mfma_f32_16x16x32_bf16 v[104:107], v[80:83], a[216:219], v[104:107]\n"
"buffer_load_dwordx4 v11, s[72:75], s51 offen offset:2048 lds\n"
"v_mfma_f32_16x16x32_bf16 v[108:111], v[80:83], a[220:223], v[108:111]\n"
"buffer_load_dwordx4 v11, s[88:91], s51 offen offset:3072 lds\n"
"v_mfma_f32_16x16x32_bf16 v[108:111], v[64:67], a[204:207], v[108:111]\n"
"ds_read_b64_tr_b16 v[94:95], v7 offset:3584\n"
"s_waitcnt lgkmcnt(8)\n"
"s_waitcnt vmcnt(12)\n"
"s_barrier\n"
"v_mfma_f32_16x16x32_bf16 v[124:127], v[68:71], a[204:207], v[124:127]\n"
"ds_read_b64_tr_b16 v[64:65], v4 offset:16384\n"
"ds_read_b128 a[224:227], v12 offset:0\n"
"s_add_u32 m0, s61, 65536\n"
"s_add_u32 s51, s41, s50\n"
"v_mfma_f32_16x16x32_bf16 v[124:127], v[84:87], a[220:223], v[124:127]\n"
"buffer_load_dwordx4 v10, s[72:75], s51 offen lds\n"
"v_mfma_f32_16x16x32_bf16 v[120:123], v[84:87], a[216:219], v[120:123]\n"
"ds_read_b64_tr_b16 v[66:67], v4 offset:16896\n"
"ds_read_b128 a[228:231], v12 offset:2048\n"
"v_mfma_f32_16x16x32_bf16 v[120:123], v[68:71], a[200:203], v[120:123]\n"
"v_mfma_f32_16x16x32_bf16 v[116:119], v[68:71], a[196:199], v[116:119]\n"
"ds_read_b64_tr_b16 v[80:81], v4 offset:18432\n"
"ds_read_b128 a[232:235], v12 offset:4096\n"
"v_mfma_f32_16x16x32_bf16 v[116:119], v[84:87], a[212:215], v[116:119]\n"
"buffer_load_dwordx4 v10, s[88:91], s51 offen offset:1024 lds\n"
"v_mfma_f32_16x16x32_bf16 v[112:115], v[84:87], a[208:211], v[112:115]\n"
"ds_read_b128 a[236:239], v12 offset:6144\n"
"v_mfma_f32_16x16x32_bf16 v[112:115], v[68:71], a[192:195], v[112:115]\n"
"ds_read_b64_tr_b16 v[82:83], v4 offset:18944\n"
"s_waitcnt lgkmcnt(12)\n"
"v_mfma_f32_16x16x32_bf16 v[128:131], v[72:75], a[192:195], v[128:131]\n"
"ds_read_b64_tr_b16 v[68:69], v4 offset:17408\n"
"ds_read_b128 a[240:243], v13 offset:0\n"
"v_mfma_f32_16x16x32_bf16 v[128:131], v[88:91], a[208:211], v[128:131]\n"
"buffer_load_dwordx4 v11, s[72:75], s51 offen offset:2048 lds\n"
"v_mfma_f32_16x16x32_bf16 v[132:135], v[88:91], a[212:215], v[132:135]\n"
"ds_read_b64_tr_b16 v[70:71], v4 offset:17920\n"
"ds_read_b128 a[244:247], v13 offset:2048\n"
"v_mfma_f32_16x16x32_bf16 v[132:135], v[72:75], a[196:199], v[132:135]\n"
"v_mfma_f32_16x16x32_bf16 v[136:139], v[72:75], a[200:203], v[136:139]\n"
"ds_read_b64_tr_b16 v[84:85], v4 offset:19456\n"
"ds_read_b128 a[248:251], v13 offset:4096\n"
"v_mfma_f32_16x16x32_bf16 v[136:139], v[88:91], a[216:219], v[136:139]\n"
"buffer_load_dwordx4 v11, s[88:91], s51 offen offset:3072 lds\n"
"v_mfma_f32_16x16x32_bf16 v[140:143], v[88:91], a[220:223], v[140:143]\n"
"ds_read_b128 a[252:255], v13 offset:6144\n"
"s_add_u32 m0, s61, 81920\n"
"v_mfma_f32_16x16x32_bf16 v[140:143], v[72:75], a[204:207], v[140:143]\n"
"ds_read_b64_tr_b16 v[86:87], v4 offset:19968\n"
"s_waitcnt lgkmcnt(15)\n"
"v_mfma_f32_16x16x32_bf16 v[156:159], v[76:79], a[204:207], v[156:159]\n"
"ds_read_b64_tr_b16 v[72:73], v7 offset:16384\n"
"s_add_u32 s51, s42, s50\n"
"v_mfma_f32_16x16x32_bf16 v[156:159], v[92:95], a[220:223], v[156:159]\n"
"buffer_load_dwordx4 v10, s[72:75], s51 offen lds\n"
"v_mfma_f32_16x16x32_bf16 v[152:155], v[92:95], a[216:219], v[152:155]\n"
"ds_read_b64_tr_b16 v[74:75], v7 offset:16896\n"
"v_mfma_f32_16x16x32_bf16 v[152:155], v[76:79], a[200:203], v[152:155]\n"
"v_mfma_f32_16x16x32_bf16 v[148:151], v[76:79], a[196:199], v[148:151]\n"
"ds_read_b64_tr_b16 v[88:89], v7 offset:18432\n"
"v_mfma_f32_16x16x32_bf16 v[148:151], v[92:95], a[212:215], v[148:151]\n"
"buffer_load_dwordx4 v10, s[88:91], s51 offen offset:1024 lds\n"
"v_mfma_f32_16x16x32_bf16 v[144:147], v[92:95], a[208:211], v[144:147]\n"
"v_mfma_f32_16x16x32_bf16 v[144:147], v[76:79], a[192:195], v[144:147]\n"
"ds_read_b64_tr_b16 v[90:91], v7 offset:18944\n"
"s_waitcnt lgkmcnt(5)\n"
"v_mfma_f32_16x16x32_bf16 v[96:99], v[64:67], a[224:227], v[96:99]\n"
"ds_read_b64_tr_b16 v[76:77], v7 offset:17408\n"
"v_mfma_f32_16x16x32_bf16 v[96:99], v[80:83], a[240:243], v[96:99]\n"
"buffer_load_dwordx4 v11, s[72:75], s51 offen offset:2048 lds\n"
"v_mfma_f32_16x16x32_bf16 v[100:103], v[80:83], a[244:247], v[100:103]\n"
"ds_read_b64_tr_b16 v[78:79], v7 offset:17920\n"
"v_mfma_f32_16x16x32_bf16 v[100:103], v[64:67], a[228:231], v[100:103]\n"
"v_mfma_f32_16x16x32_bf16 v[28:31], v[236:239], a[160:163], v[28:31]\n"
"v_mfma_f32_16x16x32_bf16 v[28:31], v[252:255], a[176:179], v[28:31]\n"
"v_mfma_f32_16x16x32_bf16 v[104:107], v[64:67], a[232:235], v[104:107]\n"
"ds_read_b64_tr_b16 v[92:93], v7 offset:19456\n"
"v_mfma_f32_16x16x32_bf16 v[104:107], v[80:83], a[248:251], v[104:107]\n"
"buffer_load_dwordx4 v11, s[88:91], s51 offen offset:3072 lds\n"
"v_mfma_f32_16x16x32_bf16 v[108:111], v[80:83], a[252:255], v[108:111]\n"
"v_mfma_f32_16x16x32_bf16 v[108:111], v[64:67], a[236:239], v[108:111]\n"
"ds_read_b64_tr_b16 v[94:95], v7 offset:19968\n"
"s_waitcnt lgkmcnt(8)\n"
"s_waitcnt vmcnt(12)\n"
"s_barrier\n"
"v_mfma_f32_16x16x32_bf16 v[124:127], v[68:71], a[236:239], v[124:127]\n"
"ds_read_b64_tr_b16 v[64:65], v4 offset:32768\n"
"ds_read_b128 v[224:227], v12 offset:49152\n"
"s_add_u32 m0, s61, 114688\n"
"v_mfma_f32_16x16x32_bf16 v[124:127], v[84:87], a[252:255], v[124:127]\n"
"s_add_u32 s51, s44, s50\n"
"v_mfma_f32_16x16x32_bf16 v[120:123], v[84:87], a[248:251], v[120:123]\n"
"ds_read_b64_tr_b16 v[66:67], v4 offset:33280\n"
"ds_read_b128 v[228:231], v12 offset:51200\n"
"v_mfma_f32_16x16x32_bf16 v[120:123], v[68:71], a[232:235], v[120:123]\n"
"buffer_load_dwordx4 v10, s[72:75], s51 offen lds\n"
"v_mfma_f32_16x16x32_bf16 v[116:119], v[68:71], a[228:231], v[116:119]\n"
"ds_read_b64_tr_b16 v[80:81], v4 offset:34816\n"
"ds_read_b128 v[232:235], v12 offset:53248\n"
"v_mfma_f32_16x16x32_bf16 v[116:119], v[84:87], a[244:247], v[116:119]\n"
"buffer_load_dwordx4 v10, s[88:91], s51 offen offset:1024 lds\n"
"v_mfma_f32_16x16x32_bf16 v[112:115], v[84:87], a[240:243], v[112:115]\n"
"ds_read_b128 v[236:239], v12 offset:55296\n"
"buffer_load_dwordx4 v11, s[72:75], s51 offen offset:2048 lds\n"
"v_mfma_f32_16x16x32_bf16 v[112:115], v[68:71], a[224:227], v[112:115]\n"
"ds_read_b64_tr_b16 v[82:83], v4 offset:35328\n"
"s_waitcnt lgkmcnt(12)\n"
"v_mfma_f32_16x16x32_bf16 v[128:131], v[72:75], a[224:227], v[128:131]\n"
"ds_read_b64_tr_b16 v[68:69], v4 offset:33792\n"
"ds_read_b128 v[240:243], v13 offset:49152\n"
"buffer_load_dwordx4 v11, s[88:91], s51 offen offset:3072 lds\n"
"v_mfma_f32_16x16x32_bf16 v[128:131], v[88:91], a[240:243], v[128:131]\n"
"v_mfma_f32_16x16x32_bf16 v[132:135], v[88:91], a[244:247], v[132:135]\n"
"ds_read_b64_tr_b16 v[70:71], v4 offset:34304\n"
"ds_read_b128 v[244:247], v13 offset:51200\n"
"s_add_u32 m0, s61, 98304\n"
"v_mfma_f32_16x16x32_bf16 v[132:135], v[72:75], a[228:231], v[132:135]\n"
"v_mfma_f32_16x16x32_bf16 v[28:31], v[160:163], a[192:195], v[28:31]\n"
"v_mfma_f32_16x16x32_bf16 v[28:31], v[164:167], a[208:211], v[28:31]\n"
"v_mfma_f32_16x16x32_bf16 v[136:139], v[72:75], a[232:235], v[136:139]\n"
"ds_read_b64_tr_b16 v[84:85], v4 offset:35840\n"
"ds_read_b128 v[248:251], v13 offset:53248\n"
"s_add_u32 s51, s43, s50\n"
"v_mfma_f32_16x16x32_bf16 v[136:139], v[88:91], a[248:251], v[136:139]\n"
"buffer_load_dwordx4 v10, s[72:75], s51 offen lds\n"
"v_mfma_f32_16x16x32_bf16 v[140:143], v[88:91], a[252:255], v[140:143]\n"
"ds_read_b128 v[252:255], v13 offset:55296\n"
"v_mfma_f32_16x16x32_bf16 v[140:143], v[72:75], a[236:239], v[140:143]\n"
"ds_read_b64_tr_b16 v[86:87], v4 offset:36352\n"
"buffer_load_dwordx4 v10, s[88:91], s51 offen offset:1024 lds\n"
"s_waitcnt lgkmcnt(15)\n"
"v_mfma_f32_16x16x32_bf16 v[156:159], v[76:79], a[236:239], v[156:159]\n"
"ds_read_b64_tr_b16 v[72:73], v7 offset:32768\n"
"v_mfma_f32_16x16x32_bf16 v[156:159], v[92:95], a[252:255], v[156:159]\n"
"buffer_load_dwordx4 v11, s[72:75], s51 offen offset:2048 lds\n"
"v_mfma_f32_16x16x32_bf16 v[152:155], v[92:95], a[248:251], v[152:155]\n"
"ds_read_b64_tr_b16 v[74:75], v7 offset:33280\n"
"buffer_load_dwordx4 v11, s[88:91], s51 offen offset:3072 lds\n"
"v_mfma_f32_16x16x32_bf16 v[152:155], v[76:79], a[232:235], v[152:155]\n"
"v_mfma_f32_16x16x32_bf16 v[148:151], v[76:79], a[228:231], v[148:151]\n"
"ds_read_b64_tr_b16 v[88:89], v7 offset:34816\n"
"s_add_u32 m0, s61, 131072\n"
"v_mfma_f32_16x16x32_bf16 v[148:151], v[92:95], a[244:247], v[148:151]\n"
"v_mfma_f32_16x16x32_bf16 v[144:147], v[92:95], a[240:243], v[144:147]\n"
"s_add_u32 s51, s45, s50\n"
"v_mfma_f32_16x16x32_bf16 v[144:147], v[76:79], a[224:227], v[144:147]\n"
"ds_read_b64_tr_b16 v[90:91], v7 offset:35328\n"
"s_waitcnt lgkmcnt(5)\n"
"v_mfma_f32_16x16x32_bf16 v[96:99], v[64:67], v[224:227], v[96:99]\n"
"ds_read_b64_tr_b16 v[76:77], v7 offset:33792\n"
"buffer_load_dwordx4 v10, s[72:75], s51 offen lds\n"
"v_mfma_f32_16x16x32_bf16 v[96:99], v[80:83], v[240:243], v[96:99]\n"
"buffer_load_dwordx4 v10, s[88:91], s51 offen offset:1024 lds\n"
"v_mfma_f32_16x16x32_bf16 v[100:103], v[80:83], v[244:247], v[100:103]\n"
"ds_read_b64_tr_b16 v[78:79], v7 offset:34304\n"
"v_mfma_f32_16x16x32_bf16 v[100:103], v[64:67], v[228:231], v[100:103]\n"
"v_mfma_f32_16x16x32_bf16 v[28:31], v[168:171], a[224:227], v[28:31]\n"
"v_mfma_f32_16x16x32_bf16 v[28:31], v[172:175], a[240:243], v[28:31]\n"
"buffer_load_dwordx4 v11, s[72:75], s51 offen offset:2048 lds\n"
"v_mfma_f32_16x16x32_bf16 v[104:107], v[64:67], v[232:235], v[104:107]\n"
"ds_read_b64_tr_b16 v[92:93], v7 offset:35840\n"
"v_mfma_f32_16x16x32_bf16 v[104:107], v[80:83], v[248:251], v[104:107]\n"
"buffer_load_dwordx4 v11, s[88:91], s51 offen offset:3072 lds\n"
"v_mfma_f32_16x16x32_bf16 v[108:111], v[80:83], v[252:255], v[108:111]\n"
"v_mfma_f32_16x16x32_bf16 v[108:111], v[64:67], v[236:239], v[108:111]\n"
"ds_read_b64_tr_b16 v[94:95], v7 offset:36352\n"
"s_waitcnt lgkmcnt(8)\n"
"s_waitcnt vmcnt(12)\n"
"s_barrier\n"
"v_mfma_f32_16x16x32_bf16 v[124:127], v[68:71], v[236:239], v[124:127]\n"
"ds_read_b64_tr_b16 v[64:65], v4 offset:49152\n"
"v_mfma_f32_16x16x32_bf16 v[124:127], v[84:87], v[252:255], v[124:127]\n"
"s_mul_i32 s69, s19, 3072\n"
"v_mfma_f32_16x16x32_bf16 v[120:123], v[84:87], v[248:251], v[120:123]\n"
"ds_read_b64_tr_b16 v[66:67], v4 offset:49664\n"
"s_add_u32 m0, s69, 147456\n"
"v_mfma_f32_16x16x32_bf16 v[120:123], v[68:71], v[232:235], v[120:123]\n"
"buffer_load_dwordx4 v20, s[20:23], 0 offen offset:0 lds\n"
"v_mfma_f32_16x16x32_bf16 v[116:119], v[68:71], v[228:231], v[116:119]\n"
"ds_read_b64_tr_b16 v[80:81], v4 offset:51200\n"
"v_mfma_f32_16x16x32_bf16 v[116:119], v[84:87], v[244:247], v[116:119]\n"
"v_mfma_f32_16x16x32_bf16 v[112:115], v[84:87], v[240:243], v[112:115]\n"
"v_mfma_f32_16x16x32_bf16 v[112:115], v[68:71], v[224:227], v[112:115]\n"
"ds_read_b64_tr_b16 v[82:83], v4 offset:51712\n"
"s_waitcnt lgkmcnt(8)\n"
"v_mfma_f32_16x16x32_bf16 v[128:131], v[72:75], v[224:227], v[128:131]\n"
"ds_read_b64_tr_b16 v[68:69], v4 offset:50176\n"
"v_mfma_f32_16x16x32_bf16 v[128:131], v[88:91], v[240:243], v[128:131]\n"
"v_mfma_f32_16x16x32_bf16 v[132:135], v[88:91], v[244:247], v[132:135]\n"
"ds_read_b64_tr_b16 v[70:71], v4 offset:50688\n"
"v_mfma_f32_16x16x32_bf16 v[132:135], v[72:75], v[228:231], v[132:135]\n"
"v_mfma_f32_16x16x32_bf16 v[28:31], v[176:179], v[224:227], v[28:31]\n"
"v_mfma_f32_16x16x32_bf16 v[28:31], v[180:183], v[240:243], v[28:31]\n"
"v_mfma_f32_16x16x32_bf16 v[136:139], v[72:75], v[232:235], v[136:139]\n"
"ds_read_b64_tr_b16 v[84:85], v4 offset:52224\n"
"v_mfma_f32_16x16x32_bf16 v[136:139], v[88:91], v[248:251], v[136:139]\n"
"buffer_load_dwordx4 v21, s[20:23], 0 offen offset:1024 lds\n"
"v_mfma_f32_16x16x32_bf16 v[140:143], v[88:91], v[252:255], v[140:143]\n"
"v_mfma_f32_16x16x32_bf16 v[140:143], v[72:75], v[236:239], v[140:143]\n"
"ds_read_b64_tr_b16 v[86:87], v4 offset:52736\n"
"s_waitcnt lgkmcnt(8)\n"
"v_mfma_f32_16x16x32_bf16 v[156:159], v[76:79], v[236:239], v[156:159]\n"
"ds_read_b64_tr_b16 v[72:73], v7 offset:49152\n"
"v_mfma_f32_16x16x32_bf16 v[156:159], v[92:95], v[252:255], v[156:159]\n"
"v_mfma_f32_16x16x32_bf16 v[152:155], v[92:95], v[248:251], v[152:155]\n"
"ds_read_b64_tr_b16 v[74:75], v7 offset:49664\n"
"v_mfma_f32_16x16x32_bf16 v[152:155], v[76:79], v[232:235], v[152:155]\n"
"buffer_load_dwordx4 v22, s[20:23], 0 offen offset:2048 lds\n"
"v_mfma_f32_16x16x32_bf16 v[148:151], v[76:79], v[228:231], v[148:151]\n"
"ds_read_b64_tr_b16 v[88:89], v7 offset:51200\n"
"v_mfma_f32_16x16x32_bf16 v[148:151], v[92:95], v[244:247], v[148:151]\n"
"v_mfma_f32_16x16x32_bf16 v[144:147], v[92:95], v[240:243], v[144:147]\n"
"v_mfma_f32_16x16x32_bf16 v[144:147], v[76:79], v[224:227], v[144:147]\n"
"ds_read_b64_tr_b16 v[90:91], v7 offset:51712\n"
"s_waitcnt lgkmcnt(8)\n"
"s_mov_b32 s77, s76\n"
"s_lshr_b32 s76, s49, 4\n"
"s_add_u32 s76, s76, s78\n"
"s_lshl_b32 s63, s49, 11\n"
"s_lshl_b32 s69, s66, 14\n"
"s_add_u32 s63, s63, s69\n"
"s_mov_b32 s49, s50\n"
"s_add_u32 s50, s50, 128\n"
"s_add_u32 s68, s50, 128\n"
"s_sub_u32 s64, s64, 1\n"
"s_cmp_gt_u32 s64, 1\n"
"s_cselect_b32 s74, s74, 0\n"
"s_cselect_b32 s90, s90, 0\n"
"s_cselect_b32 s62, s61, 0x1c000\n"
"s_mov_b32 s56, s84\n"
"s_and_b32 s57, s85, 0xffff\n"
"s_mov_b32 s59, 0x20000\n"
"s_cmp_eq_u32 s67, 3\n"
"s_mov_b32 s58, 5308416\n"
"s_cselect_b32 s58, 2359296, s58\n"
"s_movk_i32 s60, 2304\n"
"s_cselect_b32 s60, 5120, s60\n"
"s_mov_b32 s71, 0\n"
"LpB%=:\n"
"v_mfma_f32_16x16x32_bf16 v[160:163], v[64:67], a[0:3], 0\n"
"ds_read_b64_tr_b16 v[76:77], v7 offset:50176\n"
"v_mfma_f32_16x16x32_bf16 v[160:163], v[80:83], a[16:19], v[160:163]\n"
"v_mul_f32 v96, s65, v96\n"
"v_mul_f32 v97, s65, v97\n"
"v_mfma_f32_16x16x32_bf16 v[164:167], v[80:83], a[20:23], 0\n"
"ds_read_b64_tr_b16 v[78:79], v7 offset:50688\n"
"s_add_u32 m0, s61, 0\n"
"v_mfma_f32_16x16x32_bf16 v[164:167], v[64:67], a[4:7], v[164:167]\n"
"v_exp_f32 v96, v96\n"
"v_mfma_f32_16x16x32_bf16 v[168:171], v[64:67], a[8:11], 0\n"
"ds_read_b64_tr_b16 v[92:93], v7 offset:52224\n"
"s_add_u32 s51, s46, s49\n"
"v_mfma_f32_16x16x32_bf16 v[168:171], v[80:83], a[24:27], v[168:171]\n"
"buffer_load_dwordx4 v10, s[24:27], s51 offen lds\n"
"v_mfma_f32_16x16x32_bf16 v[172:175], v[80:83], a[28:31], 0\n"
"v_exp_f32 v97, v97\n"
"v_mfma_f32_16x16x32_bf16 v[172:175], v[64:67], a[12:15], v[172:175]\n"
"ds_read_b64_tr_b16 v[94:95], v7 offset:52736\n"
"s_waitcnt lgkmcnt(8)\n"
"v_mfma_f32_16x16x32_bf16 v[188:191], v[68:71], a[12:15], 0\n"
"ds_read_b64_tr_b16 v[64:65], v5 offset:0\n"
"v_mul_f32 v98, s65, v98\n"
"v_mfma_f32_16x16x32_bf16 v[188:191], v[84:87], a[28:31], v[188:191]\n"
"v_exp_f32 v98, v98\n"
"v_mfma_f32_16x16x32_bf16 v[184:187], v[84:87], a[24:27], 0\n"
"ds_read_b64_tr_b16 v[66:67], v5 offset:512\n"
"v_mul_f32 v99, s65, v99\n"
"v_mfma_f32_16x16x32_bf16 v[184:187], v[68:71], a[8:11], v[184:187]\n"
"v_exp_f32 v99, v99\n"
"v_mfma_f32_16x16x32_bf16 v[180:183], v[68:71], a[4:7], 0\n"
"ds_read_b64_tr_b16 v[80:81], v5 offset:2048\n"
"v_add_f32 v96, 1.0, v96\n"
"v_mfma_f32_16x16x32_bf16 v[180:183], v[84:87], a[20:23], v[180:183]\n"
"buffer_load_dwordx4 v10, s[28:31], s51 offen offset:1024 lds\n"
"v_mfma_f32_16x16x32_bf16 v[176:179], v[84:87], a[16:19], 0\n"
"v_rcp_f32 v96, v96\n"
"v_mfma_f32_16x16x32_bf16 v[176:179], v[68:71], a[0:3], v[176:179]\n"
"ds_read_b64_tr_b16 v[82:83], v5 offset:2560\n"
"s_waitcnt lgkmcnt(8)\n"
"v_mfma_f32_16x16x32_bf16 v[192:195], v[72:75], a[0:3], 0\n"
"ds_read_b64_tr_b16 v[68:69], v5 offset:1024\n"
"v_add_f32 v97, 1.0, v97\n"
"v_mfma_f32_16x16x32_bf16 v[192:195], v[88:91], a[16:19], v[192:195]\n"
"v_rcp_f32 v97, v97\n"
"v_mfma_f32_16x16x32_bf16 v[196:199], v[88:91], a[20:23], 0\n"
"ds_read_b64_tr_b16 v[70:71], v5 offset:1536\n"
"v_add_f32 v98, 1.0, v98\n"
"v_mfma_f32_16x16x32_bf16 v[196:199], v[72:75], a[4:7], v[196:199]\n"
"v_rcp_f32 v98, v98\n"
"v_mfma_f32_16x16x32_bf16 v[200:203], v[72:75], a[8:11], 0\n"
"ds_read_b64_tr_b16 v[84:85], v5 offset:3072\n"
"v_add_f32 v99, 1.0, v99\n"
"v_mfma_f32_16x16x32_bf16 v[200:203], v[88:91], a[24:27], v[200:203]\n"
"buffer_load_dwordx4 v11, s[24:27], s51 offen offset:2048 lds\n"
"v_mfma_f32_16x16x32_bf16 v[204:207], v[88:91], a[28:31], 0\n"
"v_rcp_f32 v99, v99\n"
"v_mfma_f32_16x16x32_bf16 v[204:207], v[72:75], a[12:15], v[204:207]\n"
"ds_read_b64_tr_b16 v[86:87], v5 offset:3584\n"
"s_waitcnt lgkmcnt(8)\n"
"v_mfma_f32_16x16x32_bf16 v[220:223], v[76:79], a[12:15], 0\n"
"ds_read_b64_tr_b16 v[72:73], v8 offset:0\n"
"v_mul_f32 v96, v112, v96\n"
"v_mfma_f32_16x16x32_bf16 v[220:223], v[92:95], a[28:31], v[220:223]\n"
"v_mul_f32 v97, v113, v97\n"
"v_mul_f32 v98, v114, v98\n"
"v_mfma_f32_16x16x32_bf16 v[216:219], v[92:95], a[24:27], 0\n"
"ds_read_b64_tr_b16 v[74:75], v8 offset:512\n"
"v_mul_f32 v99, v115, v99\n"
"v_mfma_f32_16x16x32_bf16 v[216:219], v[76:79], a[8:11], v[216:219]\n"
"v_cvt_pk_bf16_f32 v112, v96, v97\n"
"v_cvt_pk_bf16_f32 v113, v98, v99\n"
"v_mfma_f32_16x16x32_bf16 v[212:215], v[76:79], a[4:7], 0\n"
"ds_read_b64_tr_b16 v[88:89], v8 offset:2048\n"
"v_mul_f32 v128, s65, v128\n"
"v_mfma_f32_16x16x32_bf16 v[212:215], v[92:95], a[20:23], v[212:215]\n"
"buffer_load_dwordx4 v11, s[28:31], s51 offen offset:3072 lds\n"
"v_mfma_f32_16x16x32_bf16 v[208:211], v[92:95], a[16:19], 0\n"
"v_exp_f32 v128, v128\n"
"v_mfma_f32_16x16x32_bf16 v[208:211], v[76:79], a[0:3], v[208:211]\n"
"ds_read_b64_tr_b16 v[90:91], v8 offset:2560\n"
"s_waitcnt lgkmcnt(8)\n"
"v_mfma_f32_16x16x32_bf16 v[160:163], v[64:67], a[32:35], v[160:163]\n"
"ds_read_b64_tr_b16 v[76:77], v8 offset:1024\n"
"v_mul_f32 v129, s65, v129\n"
"v_mfma_f32_16x16x32_bf16 v[160:163], v[80:83], a[48:51], v[160:163]\n"
"v_exp_f32 v129, v129\n"
"v_mfma_f32_16x16x32_bf16 v[164:167], v[80:83], a[52:55], v[164:167]\n"
"ds_read_b64_tr_b16 v[78:79], v8 offset:1536\n"
"s_add_u32 m0, s61, 16384\n"
"v_mfma_f32_16x16x32_bf16 v[164:167], v[64:67], a[36:39], v[164:167]\n"
"v_mul_f32 v130, s65, v130\n"
"v_mul_f32 v131, s65, v131\n"
"v_mfma_f32_16x16x32_bf16 v[168:171], v[64:67], a[40:43], v[168:171]\n"
"ds_read_b64_tr_b16 v[92:93], v8 offset:3072\n"
"s_add_u32 s51, s47, s49\n"
"v_mfma_f32_16x16x32_bf16 v[168:171], v[80:83], a[56:59], v[168:171]\n"
"buffer_load_dwordx4 v10, s[24:27], s51 offen lds\n"
"v_mfma_f32_16x16x32_bf16 v[172:175], v[80:83], a[60:63], v[172:175]\n"
"v_exp_f32 v130, v130\n"
"v_mfma_f32_16x16x32_bf16 v[172:175], v[64:67], a[44:47], v[172:175]\n"
"ds_read_b64_tr_b16 v[94:95], v8 offset:3584\n"
"s_waitcnt lgkmcnt(8)\n"
"v_mfma_f32_16x16x32_bf16 v[188:191], v[68:71], a[44:47], v[188:191]\n"
"ds_read_b64_tr_b16 v[64:65], v5 offset:16384\n"
"v_add_f32 v128, 1.0, v128\n"
"v_mfma_f32_16x16x32_bf16 v[188:191], v[84:87], a[60:63], v[188:191]\n"
"v_exp_f32 v131, v131\n"
"v_mfma_f32_16x16x32_bf16 v[184:187], v[84:87], a[56:59], v[184:187]\n"
"ds_read_b64_tr_b16 v[66:67], v5 offset:16896\n"
"v_add_f32 v129, 1.0, v129\n"
"v_mfma_f32_16x16x32_bf16 v[184:187], v[68:71], a[40:43], v[184:187]\n"
"v_rcp_f32 v128, v128\n"
"v_mfma_f32_16x16x32_bf16 v[180:183], v[68:71], a[36:39], v[180:183]\n"
"ds_read_b64_tr_b16 v[80:81], v5 offset:18432\n"
"v_add_f32 v130, 1.0, v130\n"
"v_mfma_f32_16x16x32_bf16 v[180:183], v[84:87], a[52:55], v[180:183]\n"
"buffer_load_dwordx4 v10, s[28:31], s51 offen offset:1024 lds\n"
"v_mfma_f32_16x16x32_bf16 v[176:179], v[84:87], a[48:51], v[176:179]\n"
"v_rcp_f32 v129, v129\n"
"v_mfma_f32_16x16x32_bf16 v[176:179], v[68:71], a[32:35], v[176:179]\n"
"ds_read_b64_tr_b16 v[82:83], v5 offset:18944\n"
"s_waitcnt lgkmcnt(8)\n"
"v_mfma_f32_16x16x32_bf16 v[192:195], v[72:75], a[32:35], v[192:195]\n"
"ds_read_b64_tr_b16 v[68:69], v5 offset:17408\n"
"v_add_f32 v131, 1.0, v131\n"
"v_mfma_f32_16x16x32_bf16 v[192:195], v[88:91], a[48:51], v[192:195]\n"
"v_rcp_f32 v130, v130\n"
"v_mfma_f32_16x16x32_bf16 v[196:199], v[88:91], a[52:55], v[196:199]\n"
"ds_read_b64_tr_b16 v[70:71], v5 offset:17920\n"
"v_mul_f32 v128, v144, v128\n"
"v_mfma_f32_16x16x32_bf16 v[196:199], v[72:75], a[36:39], v[196:199]\n"
"v_rcp_f32 v131, v131\n"
"v_mfma_f32_16x16x32_bf16 v[200:203], v[72:75], a[40:43], v[200:203]\n"
"ds_read_b64_tr_b16 v[84:85], v5 offset:19456\n"
"v_mul_f32 v129, v145, v129\n"
"v_mfma_f32_16x16x32_bf16 v[200:203], v[88:91], a[56:59], v[200:203]\n"
"buffer_load_dwordx4 v11, s[24:27], s51 offen offset:2048 lds\n"
"v_mfma_f32_16x16x32_bf16 v[204:207], v[88:91], a[60:63], v[204:207]\n"
"v_mul_f32 v130, v146, v130\n"
"v_mul_f32 v131, v147, v131\n"
"v_mfma_f32_16x16x32_bf16 v[204:207], v[72:75], a[44:47], v[204:207]\n"
"ds_read_b64_tr_b16 v[86:87], v5 offset:19968\n"
"s_waitcnt lgkmcnt(8)\n"
"v_mfma_f32_16x16x32_bf16 v[220:223], v[76:79], a[44:47], v[220:223]\n"
"ds_read_b64_tr_b16 v[72:73], v8 offset:16384\n"
"v_cvt_pk_bf16_f32 v114, v128, v129\n"
"v_mfma_f32_16x16x32_bf16 v[220:223], v[92:95], a[60:63], v[220:223]\n"
"v_cvt_pk_bf16_f32 v115, v130, v131\n"
"v_mul_f32 v100, s65, v100\n"
"v_mfma_f32_16x16x32_bf16 v[216:219], v[92:95], a[56:59], v[216:219]\n"
"ds_read_b64_tr_b16 v[74:75], v8 offset:16896\n"
"v_mul_f32 v101, s65, v101\n"
"v_mfma_f32_16x16x32_bf16 v[216:219], v[76:79], a[40:43], v[216:219]\n"
"buffer_store_dwordx4 v[112:115], v24, s[36:39], s63 offen sc1\n"
"v_mfma_f32_16x16x32_bf16 v[212:215], v[76:79], a[36:39], v[212:215]\n"
"ds_read_b64_tr_b16 v[88:89], v8 offset:18432\n"
"v_mul_f32 v102, s65, v102\n"
"v_mfma_f32_16x16x32_bf16 v[212:215], v[92:95], a[52:55], v[212:215]\n"
"buffer_load_dwordx4 v11, s[28:31], s51 offen offset:3072 lds\n"
"v_mfma_f32_16x16x32_bf16 v[208:211], v[92:95], a[48:51], v[208:211]\n"
"v_exp_f32 v100, v100\n"
"v_mfma_f32_16x16x32_bf16 v[208:211], v[76:79], a[32:35], v[208:211]\n"
"ds_read_b64_tr_b16 v[90:91], v8 offset:18944\n"
"s_waitcnt lgkmcnt(8)\n"
"v_mfma_f32_16x16x32_bf16 v[160:163], v[64:67], a[64:67], v[160:163]\n"
"ds_read_b64_tr_b16 v[76:77], v8 offset:17408\n"
"v_mul_f32 v103, s65, v103\n"
"v_mfma_f32_16x16x32_bf16 v[160:163], v[80:83], a[80:83], v[160:163]\n"
"v_exp_f32 v101, v101\n"
"v_mfma_f32_16x16x32_bf16 v[164:167], v[80:83], a[84:87], v[164:167]\n"
"ds_read_b64_tr_b16 v[78:79], v8 offset:17920\n"
"v_add_f32 v100, 1.0, v100\n"
"v_mfma_f32_16x16x32_bf16 v[164:167], v[64:67], a[68:71], v[164:167]\n"
"v_exp_f32 v102, v102\n"
"v_mfma_f32_16x16x32_bf16 v[168:171], v[64:67], a[72:75], v[168:171]\n"
"ds_read_b64_tr_b16 v[92:93], v8 offset:19456\n"
"v_add_f32 v101, 1.0, v101\n"
"v_mfma_f32_16x16x32_bf16 v[168:171], v[80:83], a[88:91], v[168:171]\n"
"v_exp_f32 v103, v103\n"
"v_mfma_f32_16x16x32_bf16 v[172:175], v[80:83], a[92:95], v[172:175]\n"
"v_rcp_f32 v100, v100\n"
"v_mfma_f32_16x16x32_bf16 v[172:175], v[64:67], a[76:79], v[172:175]\n"
"ds_read_b64_tr_b16 v[94:95], v8 offset:19968\n"
"s_waitcnt lgkmcnt(8)\n"
"s_waitcnt vmcnt(9)\n"
"s_barrier\n"
"v_mfma_f32_16x16x32_bf16 v[188:191], v[68:71], a[76:79], v[188:191]\n"
"ds_read_b64_tr_b16 v[64:65], v5 offset:32768\n"
"s_add_u32 m0, s61, 32768\n"
"v_mfma_f32_16x16x32_bf16 v[188:191], v[84:87], a[92:95], v[188:191]\n"
"v_rcp_f32 v101, v101\n"
"v_mfma_f32_16x16x32_bf16 v[184:187], v[84:87], a[88:91], v[184:187]\n"
"ds_read_b64_tr_b16 v[66:67], v5 offset:33280\n"
"s_add_u32 s51, s48, s49\n"
"v_mfma_f32_16x16x32_bf16 v[184:187], v[68:71], a[72:75], v[184:187]\n"
"buffer_load_dwordx4 v10, s[24:27], s51 offen lds\n"
"v_mfma_f32_16x16x32_bf16 v[180:183], v[68:71], a[68:71], v[180:183]\n"
"ds_read_b64_tr_b16 v[80:81], v5 offset:34816\n"
"v_add_f32 v102, 1.0, v102\n"
"v_mfma_f32_16x16x32_bf16 v[180:183], v[84:87], a[84:87], v[180:183]\n"
"v_rcp_f32 v102, v102\n"
"v_mfma_f32_16x16x32_bf16 v[176:179], v[84:87], a[80:83], v[176:179]\n"
"v_add_f32 v103, 1.0, v103\n"
"v_mul_f32 v100, v116, v100\n"
"v_mfma_f32_16x16x32_bf16 v[176:179], v[68:71], a[64:67], v[176:179]\n"
"ds_read_b64_tr_b16 v[82:83], v5 offset:35328\n"
"s_waitcnt lgkmcnt(8)\n"
"v_mfma_f32_16x16x32_bf16 v[192:195], v[72:75], a[64:67], v[192:195]\n"
"ds_read_b64_tr_b16 v[68:69], v5 offset:33792\n"
"v_mul_f32 v101, v117, v101\n"
"v_mfma_f32_16x16x32_bf16 v[192:195], v[88:91], a[80:83], v[192:195]\n"
"v_rcp_f32 v103, v103\n"
"v_mfma_f32_16x16x32_bf16 v[196:199], v[88:91], a[84:87], v[196:199]\n"
"ds_read_b64_tr_b16 v[70:71], v5 offset:34304\n"
"v_mul_f32 v102, v118, v102\n"
"v_mfma_f32_16x16x32_bf16 v[196:199], v[72:75], a[68:71], v[196:199]\n"
"buffer_load_dwordx4 v10, s[28:31], s51 offen offset:1024 lds\n"
"v_mfma_f32_16x16x32_bf16 v[200:203], v[72:75], a[72:75], v[200:203]\n"
"ds_read_b64_tr_b16 v[84:85], v5 offset:35840\n"
"v_mul_f32 v103, v119, v103\n"
"v_mfma_f32_16x16x32_bf16 v[200:203], v[88:91], a[88:91], v[200:203]\n"
"v_cvt_pk_bf16_f32 v116, v100, v101\n"
"v_cvt_pk_bf16_f32 v117, v102, v103\n"
"v_mfma_f32_16x16x32_bf16 v[204:207], v[88:91], a[92:95], v[204:207]\n"
"buffer_load_dwordx2 v[54:55], v53, s[36:39], 0 offen sc1\n"
"v_mfma_f32_16x16x32_bf16 v[204:207], v[72:75], a[76:79], v[204:207]\n"
"ds_read_b64_tr_b16 v[86:87], v5 offset:36352\n"
"s_waitcnt lgkmcnt(8)\n"
"v_mfma_f32_16x16x32_bf16 v[220:223], v[76:79], a[76:79], v[220:223]\n"
"ds_read_b64_tr_b16 v[72:73], v8 offset:32768\n"
"v_mul_f32 v132, s65, v132\n"
"v_mfma_f32_16x16x32_bf16 v[220:223], v[92:95], a[92:95], v[220:223]\n"
"v_exp_f32 v132, v132\n"
"v_mfma_f32_16x16x32_bf16 v[216:219], v[92:95], a[88:91], v[216:219]\n"
"ds_read_b64_tr_b16 v[74:75], v8 offset:33280\n"
"v_mul_f32 v133, s65, v133\n"
"v_mfma_f32_16x16x32_bf16 v[216:219], v[76:79], a[72:75], v[216:219]\n"
"buffer_load_dwordx4 v11, s[24:27], s51 offen offset:2048 lds\n"
"v_mfma_f32_16x16x32_bf16 v[212:215], v[76:79], a[68:71], v[212:215]\n"
"ds_read_b64_tr_b16 v[88:89], v8 offset:34816\n"
"v_mul_f32 v134, s65, v134\n"
"v_mfma_f32_16x16x32_bf16 v[212:215], v[92:95], a[84:87], v[212:215]\n"
"v_exp_f32 v133, v133\n"
"v_mfma_f32_16x16x32_bf16 v[208:211], v[92:95], a[80:83], v[208:211]\n"
"buffer_load_dwordx4 v11, s[28:31], s51 offen offset:3072 lds\n"
"v_mfma_f32_16x16x32_bf16 v[208:211], v[76:79], a[64:67], v[208:211]\n"
"ds_read_b64_tr_b16 v[90:91], v8 offset:35328\n"
"s_waitcnt lgkmcnt(8)\n"
"v_mfma_f32_16x16x32_bf16 v[160:163], v[64:67], a[96:99], v[160:163]\n"
"ds_read_b64_tr_b16 v[76:77], v8 offset:33792\n"
"s_add_u32 m0, s62, 49152\n"
"v_mfma_f32_16x16x32_bf16 v[160:163], v[80:83], a[112:115], v[160:163]\n"
"v_exp_f32 v134, v134\n"
"v_mfma_f32_16x16x32_bf16 v[164:167], v[80:83], a[116:119], v[164:167]\n"
"ds_read_b64_tr_b16 v[78:79], v8 offset:34304\n"
"s_add_u32 s51, s40, s50\n"
"v_mfma_f32_16x16x32_bf16 v[164:167], v[64:67], a[100:103], v[164:167]\n"
"buffer_load_dwordx4 v10, s[72:75], s51 offen lds\n"
"v_mfma_f32_16x16x32_bf16 v[168:171], v[64:67], a[104:107], v[168:171]\n"
"ds_read_b64_tr_b16 v[92:93], v8 offset:35840\n"
"v_mul_f32 v135, s65, v135\n"
"v_mfma_f32_16x16x32_bf16 v[168:171], v[80:83], a[120:123], v[168:171]\n"
"v_exp_f32 v135, v135\n"
"v_mfma_f32_16x16x32_bf16 v[172:175], v[80:83], a[124:127], v[172:175]\n"
"buffer_load_dwordx4 v10, s[88:91], s51 offen offset:1024 lds\n"
"v_mfma_f32_16x16x32_bf16 v[172:175], v[64:67], a[108:111], v[172:175]\n"
"ds_read_b64_tr_b16 v[94:95], v8 offset:36352\n"
"s_waitcnt lgkmcnt(8)\n"
"v_mfma_f32_16x16x32_bf16 v[188:191], v[68:71], a[108:111], v[188:191]\n"
"ds_read_b64_tr_b16 v[64:65], v5 offset:49152\n"
"v_add_f32 v132, 1.0, v132\n"
"v_mfma_f32_16x16x32_bf16 v[188:191], v[84:87], a[124:127], v[188:191]\n"
"v_rcp_f32 v132, v132\n"
"v_mfma_f32_16x16x32_bf16 v[184:187], v[84:87], a[120:123], v[184:187]\n"
"ds_read_b64_tr_b16 v[66:67], v5 offset:49664\n"
"v_add_f32 v133, 1.0, v133\n"
"v_mfma_f32_16x16x32_bf16 v[184:187], v[68:71], a[104:107], v[184:187]\n"
"buffer_load_dwordx4 v11, s[72:75], s51 offen offset:2048 lds\n"
"v_mfma_f32_16x16x32_bf16 v[180:183], v[68:71], a[100:103], v[180:183]\n"
"ds_read_b64_tr_b16 v[80:81], v5 offset:51200\n"
"v_add_f32 v134, 1.0, v134\n"
"v_mfma_f32_16x16x32_bf16 v[180:183], v[84:87], a[116:119], v[180:183]\n"
"v_rcp_f32 v133, v133\n"
"v_mfma_f32_16x16x32_bf16 v[176:179], v[84:87], a[112:115], v[176:179]\n"
"buffer_load_dwordx4 v11, s[88:91], s51 offen offset:3072 lds\n"
"v_mfma_f32_16x16x32_bf16 v[176:179], v[68:71], a[96:99], v[176:179]\n"
"ds_read_b64_tr_b16 v[82:83], v5 offset:51712\n"
"s_waitcnt lgkmcnt(8)\n"
"v_mfma_f32_16x16x32_bf16 v[192:195], v[72:75], a[96:99], v[192:195]\n"
"ds_read_b64_tr_b16 v[68:69], v5 offset:50176\n"
"v_add_f32 v135, 1.0, v135\n"
"v_mfma_f32_16x16x32_bf16 v[192:195], v[88:91], a[112:115], v[192:195]\n"
"v_rcp_f32 v134, v134\n"
"v_mfma_f32_16x16x32_bf16 v[196:199], v[88:91], a[116:119], v[196:199]\n"
"ds_read_b64_tr_b16 v[70:71], v5 offset:50688\n"
"v_mul_f32 v132, v148, v132\n"
"v_mfma_f32_16x16x32_bf16 v[196:199], v[72:75], a[100:103], v[196:199]\n"
"buffer_load_dword v15, v18, s[56:59], s71 offen\n"
"v_mfma_f32_16x16x32_bf16 v[200:203], v[72:75], a[104:107], v[200:203]\n"
"ds_read_b64_tr_b16 v[84:85], v5 offset:52224\n"
"v_mul_f32 v133, v149, v133\n"
"v_mfma_f32_16x16x32_bf16 v[200:203], v[88:91], a[120:123], v[200:203]\n"
"v_rcp_f32 v135, v135\n"
"v_mfma_f32_16x16x32_bf16 v[204:207], v[88:91], a[124:127], v[204:207]\n"
"s_add_u32 s71, s71, s60\n"
"v_mul_f32 v134, v150, v134\n"
"v_mfma_f32_16x16x32_bf16 v[204:207], v[72:75], a[108:111], v[204:207]\n"
"ds_read_b64_tr_b16 v[86:87], v5 offset:52736\n"
"s_waitcnt lgkmcnt(8)\n"
"v_mfma_f32_16x16x32_bf16 v[220:223], v[76:79], a[108:111], v[220:223]\n"
"ds_read_b64_tr_b16 v[72:73], v8 offset:49152\n"
"v_mul_f32 v135, v151, v135\n"
"v_mfma_f32_16x16x32_bf16 v[220:223], v[92:95], a[124:127], v[220:223]\n"
"v_cvt_pk_bf16_f32 v118, v132, v133\n"
"v_cvt_pk_bf16_f32 v119, v134, v135\n"
"v_mfma_f32_16x16x32_bf16 v[216:219], v[92:95], a[120:123], v[216:219]\n"
"ds_read_b64_tr_b16 v[74:75], v8 offset:49664\n"
"v_mul_f32 v104, s65, v104\n"
"v_mfma_f32_16x16x32_bf16 v[216:219], v[76:79], a[104:107], v[216:219]\n"
"buffer_store_dwordx4 v[116:119], v25, s[36:39], s63 offen sc1\n"
"v_mfma_f32_16x16x32_bf16 v[212:215], v[76:79], a[100:103], v[212:215]\n"
"ds_read_b64_tr_b16 v[88:89], v8 offset:51200\n"
"v_mul_f32 v105, s65, v105\n"
"v_mfma_f32_16x16x32_bf16 v[212:215], v[92:95], a[116:119], v[212:215]\n"
"v_exp_f32 v104, v104\n"
"v_mfma_f32_16x16x32_bf16 v[208:211], v[92:95], a[112:115], v[208:211]\n"
"v_exp_f32 v105, v105\n"
"v_mfma_f32_16x16x32_bf16 v[208:211], v[76:79], a[96:99], v[208:211]\n"
"ds_read_b64_tr_b16 v[90:91], v8 offset:51712\n"
"s_waitcnt lgkmcnt(8)\n"
"v_mfma_f32_16x16x32_bf16 v[160:163], v[64:67], a[128:131], v[160:163]\n"
"ds_read_b64_tr_b16 v[76:77], v8 offset:50176\n"
"v_mul_f32 v106, s65, v106\n"
"v_mfma_f32_16x16x32_bf16 v[160:163], v[80:83], a[144:147], v[160:163]\n"
"v_exp_f32 v106, v106\n"
"v_mfma_f32_16x16x32_bf16 v[164:167], v[80:83], a[148:151], v[164:167]\n"
"ds_read_b64_tr_b16 v[78:79], v8 offset:50688\n"
"v_mul_f32 v107, s65, v107\n"
"v_mfma_f32_16x16x32_bf16 v[164:167], v[64:67], a[132:135], v[164:167]\n"
"v_exp_f32 v107, v107\n"
"v_mfma_f32_16x16x32_bf16 v[168:171], v[64:67], a[136:139], v[168:171]\n"
"ds_read_b64_tr_b16 v[92:93], v8 offset:52224\n"
"v_add_f32 v104, 1.0, v104\n"
"v_mfma_f32_16x16x32_bf16 v[168:171], v[80:83], a[152:155], v[168:171]\n"
"v_rcp_f32 v104, v104\n"
"v_mfma_f32_16x16x32_bf16 v[172:175], v[80:83], a[156:159], v[172:175]\n"
"v_add_f32 v105, 1.0, v105\n"
"v_add_f32 v106, 1.0, v106\n"
"v_mfma_f32_16x16x32_bf16 v[172:175], v[64:67], a[140:143], v[172:175]\n"
"ds_read_b64_tr_b16 v[94:95], v8 offset:52736\n"
"s_waitcnt lgkmcnt(8)\n"
"v_mfma_f32_16x16x32_bf16 v[188:191], v[68:71], a[140:143], v[188:191]\n"
"ds_read_b64_tr_b16 v[64:65], v6 offset:0\n"
"v_add_f32 v107, 1.0, v107\n"
"v_mfma_f32_16x16x32_bf16 v[188:191], v[84:87], a[156:159], v[188:191]\n"
"v_rcp_f32 v105, v105\n"
"v_mfma_f32_16x16x32_bf16 v[184:187], v[84:87], a[152:155], v[184:187]\n"
"ds_read_b64_tr_b16 v[66:67], v6 offset:512\n"
"v_mul_f32 v104, v120, v104\n"
"v_mfma_f32_16x16x32_bf16 v[184:187], v[68:71], a[136:139], v[184:187]\n"
"v_rcp_f32 v106, v106\n"
"v_mfma_f32_16x16x32_bf16 v[180:183], v[68:71], a[132:135], v[180:183]\n"
"ds_read_b64_tr_b16 v[80:81], v6 offset:2048\n"
"v_mul_f32 v105, v121, v105\n"
"v_mfma_f32_16x16x32_bf16 v[180:183], v[84:87], a[148:151], v[180:183]\n"
"v_rcp_f32 v107, v107\n"
"v_mfma_f32_16x16x32_bf16 v[176:179], v[84:87], a[144:147], v[176:179]\n"
"v_mul_f32 v106, v122, v106\n"
"v_mul_f32 v107, v123, v107\n"
"v_mfma_f32_16x16x32_bf16 v[176:179], v[68:71], a[128:131], v[176:179]\n"
"ds_read_b64_tr_b16 v[82:83], v6 offset:2560\n"
"s_waitcnt lgkmcnt(8)\n"
"v_mfma_f32_16x16x32_bf16 v[192:195], v[72:75], a[128:131], v[192:195]\n"
"ds_read_b64_tr_b16 v[68:69], v6 offset:1024\n"
"v_cvt_pk_bf16_f32 v120, v104, v105\n"
"v_mfma_f32_16x16x32_bf16 v[192:195], v[88:91], a[144:147], v[192:195]\n"
"v_cvt_pk_bf16_f32 v121, v106, v107\n"
"v_mul_f32 v136, s65, v136\n"
"v_mfma_f32_16x16x32_bf16 v[196:199], v[88:91], a[148:151], v[196:199]\n"
"ds_read_b64_tr_b16 v[70:71], v6 offset:1536\n"
"s_waitcnt vmcnt(8)\n"
"v_mfma_f32_16x16x32_bf16 v[196:199], v[72:75], a[132:135], v[196:199]\n"
"v_exp_f32 v136, v136\n"
"v_mfma_f32_16x16x32_bf16 v[200:203], v[72:75], a[136:139], v[200:203]\n"
"ds_read_b64_tr_b16 v[84:85], v6 offset:3072\n"
"s_cmp_eq_u32 s64, 1\n"
"v_mfma_f32_16x16x32_bf16 v[200:203], v[88:91], a[152:155], v[200:203]\n"
"s_cbranch_scc0 Lis9B%=\n"
"s_cmp_eq_u32 s67, 3\n"
"s_cbranch_scc1 Lic9B%=\n"
"s_cmp_eq_u32 s19, 0\n"
"s_cselect_b64 s[82:83], s[92:93], 0\n"
"s_mov_b32 s44, 0\n"
"v_cmp_eq_u64 vcc, s[82:83], v[54:55]\n"
"s_nop 0\n"
"s_bitcmp1_b32 vcc_lo, 0\n"
"s_cbranch_scc0 LkB_slow%=\n"
"LkB_ok%=:\n"
"s_branch Lis9B%=\n"
"Lic9B%=:\n"
"s_cmp_eq_u32 s19, 0\n"
"s_cselect_b64 s[82:83], s[92:93], 0\n"
"s_mov_b32 s44, 0\n"
"v_cmp_eq_u64 vcc, s[82:83], v[54:55]\n"
"s_nop 0\n"
"s_cmp_eq_u64 vcc, exec\n"
"s_cbranch_scc0 LxB_slow%=\n"
"LxB_ok%=:\n"
"Lis9B%=:\n"
"v_mul_f32 v137, s65, v137\n"
"v_mfma_f32_16x16x32_bf16 v[204:207], v[88:91], a[156:159], v[204:207]\n"
"v_exp_f32 v137, v137\n"
"v_mfma_f32_16x16x32_bf16 v[204:207], v[72:75], a[140:143], v[204:207]\n"
"ds_read_b64_tr_b16 v[86:87], v6 offset:3584\n"
"s_waitcnt lgkmcnt(8)\n"
"v_mfma_f32_16x16x32_bf16 v[220:223], v[76:79], a[140:143], v[220:223]\n"
"ds_read_b64_tr_b16 v[72:73], v9 offset:0\n"
"v_mul_f32 v138, s65, v138\n"
"v_mfma_f32_16x16x32_bf16 v[220:223], v[92:95], a[156:159], v[220:223]\n"
"v_exp_f32 v138, v138\n"
"v_mfma_f32_16x16x32_bf16 v[216:219], v[92:95], a[152:155], v[216:219]\n"
"ds_read_b64_tr_b16 v[74:75], v9 offset:512\n"
"v_mul_f32 v139, s65, v139\n"
"v_mfma_f32_16x16x32_bf16 v[216:219], v[76:79], a[136:139], v[216:219]\n"
"v_exp_f32 v139, v139\n"
"v_mfma_f32_16x16x32_bf16 v[212:215], v[76:79], a[132:135], v[212:215]\n"
"ds_read_b64_tr_b16 v[88:89], v9 offset:2048\n"
"v_add_f32 v136, 1.0, v136\n"
"v_mfma_f32_16x16x32_bf16 v[212:215], v[92:95], a[148:151], v[212:215]\n"
"v_rcp_f32 v136, v136\n"
"v_mfma_f32_16x16x32_bf16 v[208:211], v[92:95], a[144:147], v[208:211]\n"
"v_add_f32 v137, 1.0, v137\n"
"v_add_f32 v138, 1.0, v138\n"
"v_mfma_f32_16x16x32_bf16 v[208:211], v[76:79], a[128:131], v[208:211]\n"
"ds_read_b64_tr_b16 v[90:91], v9 offset:2560\n"
"s_waitcnt lgkmcnt(8)\n"
"v_mfma_f32_16x16x32_bf16 v[160:163], v[64:67], a[160:163], v[160:163]\n"
"ds_read_b64_tr_b16 v[76:77], v9 offset:1024\n"
"v_add_f32 v139, 1.0, v139\n"
"v_mfma_f32_16x16x32_bf16 v[160:163], v[80:83], a[176:179], v[160:163]\n"
"v_rcp_f32 v137, v137\n"
"v_mfma_f32_16x16x32_bf16 v[164:167], v[80:83], a[180:183], v[164:167]\n"
"ds_read_b64_tr_b16 v[78:79], v9 offset:1536\n"
"v_mul_f32 v136, v152, v136\n"
"v_mfma_f32_16x16x32_bf16 v[164:167], v[64:67], a[164:167], v[164:167]\n"
"v_rcp_f32 v138, v138\n"
"v_mfma_f32_16x16x32_bf16 v[168:171], v[64:67], a[168:171], v[168:171]\n"
"ds_read_b64_tr_b16 v[92:93], v9 offset:3072\n"
"v_mul_f32 v137, v153, v137\n"
"v_mfma_f32_16x16x32_bf16 v[168:171], v[80:83], a[184:187], v[168:171]\n"
"v_rcp_f32 v139, v139\n"
"v_mfma_f32_16x16x32_bf16 v[172:175], v[80:83], a[188:191], v[172:175]\n"
"v_mul_f32 v138, v154, v138\n"
"v_mul_f32 v139, v155, v139\n"
"v_mfma_f32_16x16x32_bf16 v[172:175], v[64:67], a[172:175], v[172:175]\n"
"ds_read_b64_tr_b16 v[94:95], v9 offset:3584\n"
"s_waitcnt lgkmcnt(8)\n"
"s_waitcnt vmcnt(6)\n"
"s_barrier\n"
"v_mfma_f32_16x16x32_bf16 v[188:191], v[68:71], a[172:175], v[188:191]\n"
"ds_read_b64_tr_b16 v[64:65], v4 offset:0\n"
"s_add_u32 m0, s62, 65536\n"
"v_mfma_f32_16x16x32_bf16 v[188:191], v[84:87], a[188:191], v[188:191]\n"
"v_cvt_pk_bf16_f32 v122, v136, v137\n"
"v_cvt_pk_bf16_f32 v123, v138, v139\n"
"v_mfma_f32_16x16x32_bf16 v[184:187], v[84:87], a[184:187], v[184:187]\n"
"ds_read_b64_tr_b16 v[66:67], v4 offset:512\n"
"s_add_u32 s51, s41, s50\n"
"v_mfma_f32_16x16x32_bf16 v[184:187], v[68:71], a[168:171], v[184:187]\n"
"buffer_load_dwordx4 v10, s[72:75], s51 offen lds\n"
"v_mfma_f32_16x16x32_bf16 v[180:183], v[68:71], a[164:167], v[180:183]\n"
"ds_read_b64_tr_b16 v[80:81], v4 offset:2048\n"
"v_mul_f32 v108, s65, v108\n"
"v_mfma_f32_16x16x32_bf16 v[180:183], v[84:87], a[180:183], v[180:183]\n"
"buffer_store_dwordx4 v[120:123], v26, s[36:39], s63 offen sc1\n"
"v_mfma_f32_16x16x32_bf16 v[176:179], v[84:87], a[176:179], v[176:179]\n"
"buffer_load_dwordx4 v10, s[88:91], s51 offen offset:1024 lds\n"
"v_mfma_f32_16x16x32_bf16 v[176:179], v[68:71], a[160:163], v[176:179]\n"
"ds_read_b64_tr_b16 v[82:83], v4 offset:2560\n"
"s_waitcnt lgkmcnt(8)\n"
"v_mfma_f32_16x16x32_bf16 v[192:195], v[72:75], a[160:163], v[192:195]\n"
"ds_read_b64_tr_b16 v[68:69], v4 offset:1024\n"
"v_mul_f32 v109, s65, v109\n"
"v_mfma_f32_16x16x32_bf16 v[192:195], v[88:91], a[176:179], v[192:195]\n"
"buffer_store_dwordx2 v[62:63], v61, s[36:39], s77 offen sc1\n"
"v_mfma_f32_16x16x32_bf16 v[196:199], v[88:91], a[180:183], v[196:199]\n"
"ds_read_b64_tr_b16 v[70:71], v4 offset:1536\n"
"v_mul_f32 v110, s65, v110\n"
"v_mfma_f32_16x16x32_bf16 v[196:199], v[72:75], a[164:167], v[196:199]\n"
"buffer_load_dwordx4 v11, s[72:75], s51 offen offset:2048 lds\n"
"v_mfma_f32_16x16x32_bf16 v[200:203], v[72:75], a[168:171], v[200:203]\n"
"ds_read_b64_tr_b16 v[84:85], v4 offset:3072\n"
"v_mul_f32 v111, s65, v111\n"
"v_mfma_f32_16x16x32_bf16 v[200:203], v[88:91], a[184:187], v[200:203]\n"
"v_exp_f32 v108, v108\n"
"v_mfma_f32_16x16x32_bf16 v[204:207], v[88:91], a[188:191], v[204:207]\n"
"v_exp_f32 v109, v109\n"
"v_mfma_f32_16x16x32_bf16 v[204:207], v[72:75], a[172:175], v[204:207]\n"
"ds_read_b64_tr_b16 v[86:87], v4 offset:3584\n"
"s_waitcnt lgkmcnt(8)\n"
"v_mfma_f32_16x16x32_bf16 v[220:223], v[76:79], a[172:175], v[220:223]\n"
"ds_read_b64_tr_b16 v[72:73], v7 offset:0\n"
"s_cmp_eq_u32 s64, 1\n"
"v_mfma_f32_16x16x32_bf16 v[220:223], v[92:95], a[188:191], v[220:223]\n"
"s_cbranch_scc0 Lis11B%=\n"
"s_cmp_eq_u32 s67, 3\n"
"s_cbranch_scc1 Lic11B%=\n"
"s_lshl_b32 s48, s19, 12\n"
"s_add_u32 s48, s48, 73728\n"
"s_mul_i32 s80, s19, 6144\n"
"s_sub_u32 s24, s84, 2048\n"
"s_subb_u32 s25, s85, 0\n"
"s_and_b32 s25, s25, 0xffff\n"
"s_mov_b32 s26, 5310464\n"
"s_mov_b32 s27, 0x20000\n"
"s_mov_b32 s28, s36\n"
"s_mov_b32 s29, s37\n"
"s_mov_b32 s30, s38\n"
"s_mov_b32 s31, s39\n"
"v_readlane_b32 s46, v51, s44\n"
"v_readlane_b32 s47, v52, s44\n"
"s_add_u32 s44, s44, 1\n"
"s_add_u32 m0, s80, 49152\n"
"s_nop 2\n"
"buffer_load_dwordx4 v44, s[24:27], s47 offen offset:0 lds\n"
"buffer_load_dwordx4 v45, s[24:27], s47 offen offset:1024 lds\n"
"buffer_load_dwordx4 v46, s[24:27], s47 offen offset:2048 lds\n"
"s_add_u32 m0, s48, 32768\n"
"s_nop 0\n"
"buffer_load_dwordx4 v50, s[28:31], s46 offen offset:0 sc1 lds\n"
"buffer_load_dwordx4 v50, s[28:31], s46 offen offset:1024 sc1 lds\n"
"s_mov_b32 m0, 0x30000\n"
"s_branch Lis11B%=\n"
"Lic11B%=:\n"
"s_lshr_b32 s55, s34, 1\n"
"s_lshl_b32 s55, s55, 4\n"
"s_add_u32 s55, s55, s66\n"
"s_lshl_b32 s55, s55, 1\n"
"s_and_b32 s56, s34, 1\n"
"s_add_u32 s55, s55, s56\n"
"s_lshr_b32 s46, s55, 1\n"
"s_mul_i32 s46, s46, 18\n"
"s_bitcmp1_b32 s55, 0\n"
"s_cselect_b32 s56, 9, 0\n"
"s_cselect_b32 s47, 8, 8\n"
"s_add_u32 s46, s46, s56\n"
"s_lshr_b32 s56, s19, 1\n"
"s_and_b32 s57, s19, 1\n"
"s_mov_b32 s24, s84\n"
"s_and_b32 s25, s85, 0xffff\n"
"s_mov_b32 s26, 2359296\n"
"s_mov_b32 s27, 0x20000\n"
"s_mov_b32 s40, s86\n"
"s_and_b32 s41, s87, 0xffff\n"
"s_mov_b32 s42, 1152\n"
"s_mov_b32 s43, 0x20000\n"
"v_add_u32 v48, s46, v0\n"
"v_lshlrev_b32 v48, 1, v48\n"
"s_add_u32 s46, s46, s56\n"
"s_add_u32 s44, s56, 62\n"
"s_lshl_b32 s71, s46, 7\n"
"s_add_u32 s71, s71, 23592704\n"
"s_mul_i32 s53, s56, 26880\n"
"s_mul_i32 s58, s57, 5120\n"
"s_add_u32 s54, s53, s58\n"
"s_add_u32 s54, s54, 17664\n"
"s_mul_i32 s58, s57, 8320\n"
"s_add_u32 s53, s53, s58\n"
"s_mov_b32 s28, s36\n"
"s_mov_b32 s29, s37\n"
"s_mov_b32 s30, s38\n"
"s_mov_b32 s31, s39\n"
"s_mov_b32 s30, 0\n"
"v_add_u32 v57, 4, v42\n"
"v_add_u32 v56, 4, v44\n"
"v_add_u32 v58, 589824, v45\n"
"s_add_u32 m0, s54, 53760\n"
"s_lshl_b32 s45, s46, 13\n"
"buffer_load_dwordx4 v43, s[36:39], s45 offen offset:0 sc1 lds\n"
"s_add_u32 s45, s45, 1024\n"
"buffer_load_dwordx4 v43, s[36:39], s45 offen offset:1024 sc1 lds\n"
"s_add_u32 s45, s45, 1024\n"
"buffer_load_dwordx4 v43, s[36:39], s45 offen offset:2048 sc1 lds\n"
"s_add_u32 s45, s45, 1024\n"
"buffer_load_dwordx4 v43, s[36:39], s45 offen offset:3072 sc1 lds\n"
"s_add_u32 m0, s53, 53760\n"
"s_lshl_b32 s48, s46, 12\n"
"buffer_load_dword v44, s[24:27], s48 offen lds\n"
"s_add_u32 m0, s53, 53824\n"
"s_nop 0\n"
"buffer_load_dwordx4 v42, s[24:27], s48 offen offset:0 lds\n"
"buffer_load_dwordx4 v42, s[24:27], s48 offen offset:1024 lds\n"
"s_mov_b32 m0, 0x30000\n"
"Lis11B%=:\n"
"v_add_f32 v108, 1.0, v108\n"
"v_mfma_f32_16x16x32_bf16 v[216:219], v[92:95], a[184:187], v[216:219]\n"
"ds_read_b64_tr_b16 v[74:75], v7 offset:512\n"
"v_add_f32 v109, 1.0, v109\n"
"v_mfma_f32_16x16x32_bf16 v[216:219], v[76:79], a[168:171], v[216:219]\n"
"buffer_load_dwordx4 v11, s[88:91], s51 offen offset:3072 lds\n"
"v_mfma_f32_16x16x32_bf16 v[212:215], v[76:79], a[164:167], v[212:215]\n"
"ds_read_b64_tr_b16 v[88:89], v7 offset:2048\n"
"v_mul_f32 v140, s65, v140\n"
"v_mfma_f32_16x16x32_bf16 v[212:215], v[92:95], a[180:183], v[212:215]\n"
"v_exp_f32 v110, v110\n"
"v_mfma_f32_16x16x32_bf16 v[208:211], v[92:95], a[176:179], v[208:211]\n"
"v_exp_f32 v111, v111\n"
"v_mfma_f32_16x16x32_bf16 v[208:211], v[76:79], a[160:163], v[208:211]\n"
"ds_read_b64_tr_b16 v[90:91], v7 offset:2560\n"
"s_waitcnt lgkmcnt(8)\n"
"v_mfma_f32_16x16x32_bf16 v[160:163], v[64:67], a[192:195], v[160:163]\n"
"ds_read_b64_tr_b16 v[76:77], v7 offset:1024\n"
"s_add_u32 m0, s62, 81920\n"
"v_mfma_f32_16x16x32_bf16 v[160:163], v[80:83], a[208:211], v[160:163]\n"
"v_rcp_f32 v108, v108\n"
"v_mfma_f32_16x16x32_bf16 v[164:167], v[80:83], a[212:215], v[164:167]\n"
"ds_read_b64_tr_b16 v[78:79], v7 offset:1536\n"
"s_add_u32 s51, s42, s50\n"
"v_mfma_f32_16x16x32_bf16 v[164:167], v[64:67], a[196:199], v[164:167]\n"
"buffer_load_dwordx4 v10, s[72:75], s51 offen lds\n"
"v_mfma_f32_16x16x32_bf16 v[168:171], v[64:67], a[200:203], v[168:171]\n"
"ds_read_b64_tr_b16 v[92:93], v7 offset:3072\n"
"v_add_f32 v110, 1.0, v110\n"
"v_mfma_f32_16x16x32_bf16 v[168:171], v[80:83], a[216:219], v[168:171]\n"
"v_rcp_f32 v109, v109\n"
"v_mfma_f32_16x16x32_bf16 v[172:175], v[80:83], a[220:223], v[172:175]\n"
"buffer_load_dwordx4 v10, s[88:91], s51 offen offset:1024 lds\n"
"v_mfma_f32_16x16x32_bf16 v[172:175], v[64:67], a[204:207], v[172:175]\n"
"ds_read_b64_tr_b16 v[94:95], v7 offset:3584\n"
"s_waitcnt lgkmcnt(8)\n"
"v_mfma_f32_16x16x32_bf16 v[188:191], v[68:71], a[204:207], v[188:191]\n"
"ds_read_b64_tr_b16 v[64:65], v4 offset:16384\n"
"v_add_f32 v111, 1.0, v111\n"
"v_mfma_f32_16x16x32_bf16 v[188:191], v[84:87], a[220:223], v[188:191]\n"
"v_rcp_f32 v110, v110\n"
"v_mfma_f32_16x16x32_bf16 v[184:187], v[84:87], a[216:219], v[184:187]\n"
"ds_read_b64_tr_b16 v[66:67], v4 offset:16896\n"
"v_mul_f32 v108, v124, v108\n"
"v_mfma_f32_16x16x32_bf16 v[184:187], v[68:71], a[200:203], v[184:187]\n"
"buffer_load_dwordx4 v11, s[72:75], s51 offen offset:2048 lds\n"
"v_mfma_f32_16x16x32_bf16 v[180:183], v[68:71], a[196:199], v[180:183]\n"
"ds_read_b64_tr_b16 v[80:81], v4 offset:18432\n"
"v_mul_f32 v109, v125, v109\n"
"v_mfma_f32_16x16x32_bf16 v[180:183], v[84:87], a[212:215], v[180:183]\n"
"v_rcp_f32 v111, v111\n"
"v_mfma_f32_16x16x32_bf16 v[176:179], v[84:87], a[208:211], v[176:179]\n"
"v_exp_f32 v140, v140\n"
"v_mfma_f32_16x16x32_bf16 v[176:179], v[68:71], a[192:195], v[176:179]\n"
"ds_read_b64_tr_b16 v[82:83], v4 offset:18944\n"
"s_waitcnt lgkmcnt(8)\n"
"v_mfma_f32_16x16x32_bf16 v[192:195], v[72:75], a[192:195], v[192:195]\n"
"ds_read_b64_tr_b16 v[68:69], v4 offset:17408\n"
"v_mul_f32 v110, v126, v110\n"
"v_mfma_f32_16x16x32_bf16 v[192:195], v[88:91], a[208:211], v[192:195]\n"
"v_mul_f32 v111, v127, v111\n"
"v_cvt_pk_bf16_f32 v124, v108, v109\n"
"v_mfma_f32_16x16x32_bf16 v[196:199], v[88:91], a[212:215], v[196:199]\n"
"ds_read_b64_tr_b16 v[70:71], v4 offset:17920\n"
"v_cvt_pk_bf16_f32 v125, v110, v111\n"
"v_mfma_f32_16x16x32_bf16 v[196:199], v[72:75], a[196:199], v[196:199]\n"
"buffer_load_dwordx4 v11, s[88:91], s51 offen offset:3072 lds\n"
"v_mfma_f32_16x16x32_bf16 v[200:203], v[72:75], a[200:203], v[200:203]\n"
"ds_read_b64_tr_b16 v[84:85], v4 offset:19456\n"
"v_mul_f32 v141, s65, v141\n"
"v_mfma_f32_16x16x32_bf16 v[200:203], v[88:91], a[216:219], v[200:203]\n"
"v_exp_f32 v141, v141\n"
"v_mfma_f32_16x16x32_bf16 v[204:207], v[88:91], a[220:223], v[204:207]\n"
"v_mul_f32 v142, s65, v142\n"
"v_mul_f32 v143, s65, v143\n"
"v_mfma_f32_16x16x32_bf16 v[204:207], v[72:75], a[204:207], v[204:207]\n"
"ds_read_b64_tr_b16 v[86:87], v4 offset:19968\n"
"s_waitcnt lgkmcnt(8)\n"
"v_mfma_f32_16x16x32_bf16 v[220:223], v[76:79], a[204:207], v[220:223]\n"
"ds_read_b64_tr_b16 v[72:73], v7 offset:16384\n"
"s_add_u32 m0, s62, 98304\n"
"v_mfma_f32_16x16x32_bf16 v[220:223], v[92:95], a[220:223], v[220:223]\n"
"v_exp_f32 v142, v142\n"
"v_mfma_f32_16x16x32_bf16 v[216:219], v[92:95], a[216:219], v[216:219]\n"
"ds_read_b64_tr_b16 v[74:75], v7 offset:16896\n"
"s_add_u32 s51, s43, s50\n"
"v_mfma_f32_16x16x32_bf16 v[216:219], v[76:79], a[200:203], v[216:219]\n"
"buffer_load_dwordx4 v10, s[72:75], s51 offen lds\n"
"v_mfma_f32_16x16x32_bf16 v[212:215], v[76:79], a[196:199], v[212:215]\n"
"ds_read_b64_tr_b16 v[88:89], v7 offset:18432\n"
"v_add_f32 v140, 1.0, v140\n"
"v_mfma_f32_16x16x32_bf16 v[212:215], v[92:95], a[212:215], v[212:215]\n"
"v_exp_f32 v143, v143\n"
"v_mfma_f32_16x16x32_bf16 v[208:211], v[92:95], a[208:211], v[208:211]\n"
"buffer_load_dwordx4 v10, s[88:91], s51 offen offset:1024 lds\n"
"v_mfma_f32_16x16x32_bf16 v[208:211], v[76:79], a[192:195], v[208:211]\n"
"ds_read_b64_tr_b16 v[90:91], v7 offset:18944\n"
"s_waitcnt lgkmcnt(8)\n"
"v_mfma_f32_16x16x32_bf16 v[160:163], v[64:67], a[224:227], v[160:163]\n"
"ds_read_b64_tr_b16 v[76:77], v7 offset:17408\n"
"v_add_f32 v141, 1.0, v141\n"
"v_mfma_f32_16x16x32_bf16 v[160:163], v[80:83], a[240:243], v[160:163]\n"
"v_rcp_f32 v140, v140\n"
"v_mfma_f32_16x16x32_bf16 v[164:167], v[80:83], a[244:247], v[164:167]\n"
"ds_read_b64_tr_b16 v[78:79], v7 offset:17920\n"
"v_add_f32 v142, 1.0, v142\n"
"v_mfma_f32_16x16x32_bf16 v[164:167], v[64:67], a[228:231], v[164:167]\n"
"buffer_load_dwordx4 v11, s[72:75], s51 offen offset:2048 lds\n"
"v_mfma_f32_16x16x32_bf16 v[168:171], v[64:67], a[232:235], v[168:171]\n"
"ds_read_b64_tr_b16 v[92:93], v7 offset:19456\n"
"s_cmp_eq_u32 s64, 1\n"
"v_mfma_f32_16x16x32_bf16 v[168:171], v[80:83], a[248:251], v[168:171]\n"
"s_cbranch_scc0 Lis14B%=\n"
"s_cmp_eq_u32 s67, 3\n"
"s_cbranch_scc1 Lic14B%=\n"
"s_add_u32 m0, s48, 32768\n"
"s_nop 0\n"
"buffer_load_dwordx4 v50, s[28:31], s46 offen offset:2048 sc1 lds\n"
"buffer_load_dwordx4 v50, s[28:31], s46 offen offset:3072 sc1 lds\n"
"s_add_u32 m0, s80, 52224\n"
"s_nop 0\n"
"buffer_load_dwordx4 v47, s[24:27], s47 offen offset:0 lds\n"
"buffer_load_dwordx4 v48, s[24:27], s47 offen offset:1024 lds\n"
"buffer_load_dwordx4 v49, s[24:27], s47 offen offset:2048 lds\n"
"s_mov_b32 m0, 0x30000\n"
"s_branch Lis14B%=\n"
"Lic14B%=:\n"
"s_add_u32 m0, s53, 53824\n"
"s_nop 0\n"
"buffer_load_dwordx4 v42, s[24:27], s48 offen offset:2048 lds\n"
"buffer_load_dwordx4 v42, s[24:27], s48 offen offset:3072 lds\n"
"s_add_u32 m0, s53, 57920\n"
"s_add_u32 s46, s46, 2\n"
"buffer_load_dword v56, s[24:27], s48 offen lds\n"
"s_add_u32 m0, s53, 57984\n"
"s_min_u32 s46, s46, 575\n"
"buffer_load_dwordx4 v57, s[24:27], s48 offen offset:0 lds\n"
"buffer_load_dwordx4 v57, s[24:27], s48 offen offset:1024 lds\n"
"buffer_load_dwordx4 v57, s[24:27], s48 offen offset:2048 lds\n"
"buffer_load_dwordx4 v57, s[24:27], s48 offen offset:3072 lds\n"
"buffer_load_ushort v48, v48, s[40:43], 0 offen\n"
"s_mov_b32 m0, 0x30000\n"
"Lis14B%=:\n"
"v_add_f32 v143, 1.0, v143\n"
"v_mfma_f32_16x16x32_bf16 v[172:175], v[80:83], a[252:255], v[172:175]\n"
"v_rcp_f32 v141, v141\n"
"v_mfma_f32_16x16x32_bf16 v[172:175], v[64:67], a[236:239], v[172:175]\n"
"ds_read_b64_tr_b16 v[94:95], v7 offset:19968\n"
"s_waitcnt lgkmcnt(8)\n"
"v_mfma_f32_16x16x32_bf16 v[188:191], v[68:71], a[236:239], v[188:191]\n"
"ds_read_b64_tr_b16 v[64:65], v4 offset:32768\n"
"v_mul_f32 v140, v156, v140\n"
"v_mfma_f32_16x16x32_bf16 v[188:191], v[84:87], a[252:255], v[188:191]\n"
"v_rcp_f32 v142, v142\n"
"v_mfma_f32_16x16x32_bf16 v[184:187], v[84:87], a[248:251], v[184:187]\n"
"ds_read_b64_tr_b16 v[66:67], v4 offset:33280\n"
"v_mul_f32 v141, v157, v141\n"
"v_mfma_f32_16x16x32_bf16 v[184:187], v[68:71], a[232:235], v[184:187]\n"
"buffer_load_dwordx4 v11, s[88:91], s51 offen offset:3072 lds\n"
"v_mfma_f32_16x16x32_bf16 v[180:183], v[68:71], a[228:231], v[180:183]\n"
"ds_read_b64_tr_b16 v[80:81], v4 offset:34816\n"
"v_mul_f32 v142, v158, v142\n"
"v_mfma_f32_16x16x32_bf16 v[180:183], v[84:87], a[244:247], v[180:183]\n"
"v_rcp_f32 v143, v143\n"
"v_mfma_f32_16x16x32_bf16 v[176:179], v[84:87], a[240:243], v[176:179]\n"
"v_mul_f32 v143, v159, v143\n"
"v_cvt_pk_bf16_f32 v126, v140, v141\n"
"v_mfma_f32_16x16x32_bf16 v[176:179], v[68:71], a[224:227], v[176:179]\n"
"ds_read_b64_tr_b16 v[82:83], v4 offset:35328\n"
"s_waitcnt lgkmcnt(8)\n"
"v_mfma_f32_16x16x32_bf16 v[192:195], v[72:75], a[224:227], v[192:195]\n"
"ds_read_b64_tr_b16 v[68:69], v4 offset:33792\n"
"s_add_u32 m0, s62, 114688\n"
"v_mfma_f32_16x16x32_bf16 v[192:195], v[88:91], a[240:243], v[192:195]\n"
"buffer_load_dwordx2 v[54:55], v53, s[36:39], 0 offen sc1\n"
"v_mfma_f32_16x16x32_bf16 v[196:199], v[88:91], a[244:247], v[196:199]\n"
"ds_read_b64_tr_b16 v[70:71], v4 offset:34304\n"
"s_add_u32 s51, s44, s50\n"
"v_mfma_f32_16x16x32_bf16 v[196:199], v[72:75], a[228:231], v[196:199]\n"
"buffer_load_dwordx4 v10, s[72:75], s51 offen lds\n"
"v_mfma_f32_16x16x32_bf16 v[200:203], v[72:75], a[232:235], v[200:203]\n"
"ds_read_b64_tr_b16 v[84:85], v4 offset:35840\n"
"v_cvt_pk_bf16_f32 v127, v142, v143\n"
"v_mfma_f32_16x16x32_bf16 v[200:203], v[88:91], a[248:251], v[200:203]\n"
"buffer_store_dwordx4 v[124:127], v27, s[36:39], s63 offen sc1\n"
"v_mfma_f32_16x16x32_bf16 v[204:207], v[88:91], a[252:255], v[204:207]\n"
"buffer_load_dwordx4 v10, s[88:91], s51 offen offset:1024 lds\n"
"v_mfma_f32_16x16x32_bf16 v[204:207], v[72:75], a[236:239], v[204:207]\n"
"ds_read_b64_tr_b16 v[86:87], v4 offset:36352\n"
"s_waitcnt lgkmcnt(8)\n"
"v_mfma_f32_16x16x32_bf16 v[220:223], v[76:79], a[236:239], v[220:223]\n"
"ds_read_b64_tr_b16 v[72:73], v7 offset:32768\n"
"v_mfma_f32_16x16x32_bf16 v[220:223], v[92:95], a[252:255], v[220:223]\n"
"v_mfma_f32_16x16x32_bf16 v[216:219], v[92:95], a[248:251], v[216:219]\n"
"ds_read_b64_tr_b16 v[74:75], v7 offset:33280\n"
"v_mfma_f32_16x16x32_bf16 v[216:219], v[76:79], a[232:235], v[216:219]\n"
"buffer_load_dwordx4 v11, s[72:75], s51 offen offset:2048 lds\n"
"v_mfma_f32_16x16x32_bf16 v[212:215], v[76:79], a[228:231], v[212:215]\n"
"ds_read_b64_tr_b16 v[88:89], v7 offset:34816\n"
"v_mfma_f32_16x16x32_bf16 v[212:215], v[92:95], a[244:247], v[212:215]\n"
"v_mfma_f32_16x16x32_bf16 v[208:211], v[92:95], a[240:243], v[208:211]\n"
"v_mfma_f32_16x16x32_bf16 v[208:211], v[76:79], a[224:227], v[208:211]\n"
"ds_read_b64_tr_b16 v[90:91], v7 offset:35328\n"
"s_waitcnt lgkmcnt(8)\n"
"v_mfma_f32_16x16x32_bf16 v[160:163], v[64:67], v[224:227], v[160:163]\n"
"ds_read_b64_tr_b16 v[76:77], v7 offset:33792\n"
"v_mfma_f32_16x16x32_bf16 v[160:163], v[80:83], v[240:243], v[160:163]\n"
"v_mfma_f32_16x16x32_bf16 v[164:167], v[80:83], v[244:247], v[164:167]\n"
"ds_read_b64_tr_b16 v[78:79], v7 offset:34304\n"
"v_mfma_f32_16x16x32_bf16 v[164:167], v[64:67], v[228:231], v[164:167]\n"
"buffer_load_dwordx4 v11, s[88:91], s51 offen offset:3072 lds\n"
"v_mfma_f32_16x16x32_bf16 v[168:171], v[64:67], v[232:235], v[168:171]\n"
"ds_read_b64_tr_b16 v[92:93], v7 offset:35840\n"
"v_mfma_f32_16x16x32_bf16 v[168:171], v[80:83], v[248:251], v[168:171]\n"
"v_mfma_f32_16x16x32_bf16 v[172:175], v[80:83], v[252:255], v[172:175]\n"
"v_mfma_f32_16x16x32_bf16 v[172:175], v[64:67], v[236:239], v[172:175]\n"
"ds_read_b64_tr_b16 v[94:95], v7 offset:36352\n"
"s_waitcnt lgkmcnt(8)\n"
"s_waitcnt vmcnt(10)\n"
"s_barrier\n"
"v_mfma_f32_16x16x32_bf16 v[188:191], v[68:71], v[236:239], v[188:191]\n"
"ds_read_b64_tr_b16 v[64:65], v4 offset:49152\n"
"v_mfma_f32_16x16x32_bf16 v[188:191], v[84:87], v[252:255], v[188:191]\n"
"v_mfma_f32_16x16x32_bf16 v[184:187], v[84:87], v[248:251], v[184:187]\n"
"ds_read_b64_tr_b16 v[66:67], v4 offset:49664\n"
"s_add_u32 m0, s62, 131072\n"
"v_mfma_f32_16x16x32_bf16 v[184:187], v[68:71], v[232:235], v[184:187]\n"
"v_mfma_f32_16x16x32_bf16 v[180:183], v[68:71], v[228:231], v[180:183]\n"
"ds_read_b64_tr_b16 v[80:81], v4 offset:51200\n"
"s_add_u32 s51, s45, s50\n"
"v_mfma_f32_16x16x32_bf16 v[180:183], v[84:87], v[244:247], v[180:183]\n"
"buffer_load_dwordx4 v10, s[72:75], s51 offen lds\n"
"v_mfma_f32_16x16x32_bf16 v[176:179], v[84:87], v[240:243], v[176:179]\n"
"v_mfma_f32_16x16x32_bf16 v[176:179], v[68:71], v[224:227], v[176:179]\n"
"ds_read_b64_tr_b16 v[82:83], v4 offset:51712\n"
"s_waitcnt lgkmcnt(8)\n"
"v_mfma_f32_16x16x32_bf16 v[192:195], v[72:75], v[224:227], v[192:195]\n"
"ds_read_b64_tr_b16 v[68:69], v4 offset:50176\n"
"s_mov_b32 s77, s76\n"
"v_mfma_f32_16x16x32_bf16 v[192:195], v[88:91], v[240:243], v[192:195]\n"
"buffer_load_dwordx4 v10, s[88:91], s51 offen offset:1024 lds\n"
"v_mfma_f32_16x16x32_bf16 v[196:199], v[88:91], v[244:247], v[196:199]\n"
"ds_read_b64_tr_b16 v[70:71], v4 offset:50688\n"
"s_lshr_b32 s76, s49, 4\n"
"v_mfma_f32_16x16x32_bf16 v[196:199], v[72:75], v[228:231], v[196:199]\n"
"buffer_load_dwordx4 v11, s[72:75], s51 offen offset:2048 lds\n"
"v_mfma_f32_16x16x32_bf16 v[200:203], v[72:75], v[232:235], v[200:203]\n"
"ds_read_b64_tr_b16 v[84:85], v4 offset:52224\n"
"s_add_u32 s76, s76, s78\n"
"v_mfma_f32_16x16x32_bf16 v[200:203], v[88:91], v[248:251], v[200:203]\n"
"s_lshl_b32 s63, s49, 11\n"
"s_lshl_b32 s69, s66, 14\n"
"v_mfma_f32_16x16x32_bf16 v[204:207], v[88:91], v[252:255], v[204:207]\n"
"buffer_load_dwordx4 v11, s[88:91], s51 offen offset:3072 lds\n"
"v_mfma_f32_16x16x32_bf16 v[204:207], v[72:75], v[236:239], v[204:207]\n"
"ds_read_b64_tr_b16 v[86:87], v4 offset:52736\n"
"s_waitcnt lgkmcnt(8)\n"
"v_mfma_f32_16x16x32_bf16 v[220:223], v[76:79], v[236:239], v[220:223]\n"
"ds_read_b64_tr_b16 v[72:73], v7 offset:49152\n"
"s_add_u32 s63, s63, s69\n"
"v_mfma_f32_16x16x32_bf16 v[220:223], v[92:95], v[252:255], v[220:223]\n"
"v_mfma_f32_16x16x32_bf16 v[216:219], v[92:95], v[248:251], v[216:219]\n"
"ds_read_b64_tr_b16 v[74:75], v7 offset:49664\n"
"s_mov_b32 s49, s50\n"
"v_mfma_f32_16x16x32_bf16 v[216:219], v[76:79], v[232:235], v[216:219]\n"
"v_mfma_f32_16x16x32_bf16 v[212:215], v[76:79], v[228:231], v[212:215]\n"
"ds_read_b64_tr_b16 v[88:89], v7 offset:51200\n"
"s_add_u32 s50, s50, 128\n"
"v_mfma_f32_16x16x32_bf16 v[212:215], v[92:95], v[244:247], v[212:215]\n"
"s_add_u32 s68, s50, 128\n"
"s_sub_u32 s64, s64, 1\n"
"v_mfma_f32_16x16x32_bf16 v[208:211], v[92:95], v[240:243], v[208:211]\n"
"s_cmp_gt_u32 s64, 1\n"
"s_cselect_b32 s74, s74, 0\n"
"v_mfma_f32_16x16x32_bf16 v[208:211], v[76:79], v[224:227], v[208:211]\n"
"ds_read_b64_tr_b16 v[90:91], v7 offset:51712\n"
"s_cselect_b32 s90, s90, 0\n"
"s_cselect_b32 s62, s61, 0x1c000\n"
"s_waitcnt lgkmcnt(8)\n"
"s_mov_b32 s81, 1\n"
"s_cmp_eq_u32 s64, 0\n"
"s_cbranch_scc1 Ltrans%=\n"
"LpA%=:\n"
"v_mfma_f32_16x16x32_bf16 v[96:99], v[64:67], a[0:3], 0\n"
"ds_read_b64_tr_b16 v[76:77], v7 offset:50176\n"
"v_mfma_f32_16x16x32_bf16 v[96:99], v[80:83], a[16:19], v[96:99]\n"
"v_mul_f32 v160, s65, v160\n"
"v_mul_f32 v161, s65, v161\n"
"v_mfma_f32_16x16x32_bf16 v[100:103], v[80:83], a[20:23], 0\n"
"ds_read_b64_tr_b16 v[78:79], v7 offset:50688\n"
"s_add_u32 m0, s61, 0\n"
"v_mfma_f32_16x16x32_bf16 v[100:103], v[64:67], a[4:7], v[100:103]\n"
"v_exp_f32 v160, v160\n"
"v_mfma_f32_16x16x32_bf16 v[104:107], v[64:67], a[8:11], 0\n"
"ds_read_b64_tr_b16 v[92:93], v7 offset:52224\n"
"s_add_u32 s51, s46, s49\n"
"v_mfma_f32_16x16x32_bf16 v[104:107], v[80:83], a[24:27], v[104:107]\n"
"buffer_load_dwordx4 v10, s[24:27], s51 offen lds\n"
"v_mfma_f32_16x16x32_bf16 v[108:111], v[80:83], a[28:31], 0\n"
"v_exp_f32 v161, v161\n"
"v_mfma_f32_16x16x32_bf16 v[108:111], v[64:67], a[12:15], v[108:111]\n"
"ds_read_b64_tr_b16 v[94:95], v7 offset:52736\n"
"s_waitcnt lgkmcnt(8)\n"
"v_mfma_f32_16x16x32_bf16 v[124:127], v[68:71], a[12:15], 0\n"
"ds_read_b64_tr_b16 v[64:65], v5 offset:0\n"
"v_mul_f32 v162, s65, v162\n"
"v_mfma_f32_16x16x32_bf16 v[124:127], v[84:87], a[28:31], v[124:127]\n"
"v_exp_f32 v162, v162\n"
"v_mfma_f32_16x16x32_bf16 v[120:123], v[84:87], a[24:27], 0\n"
"ds_read_b64_tr_b16 v[66:67], v5 offset:512\n"
"v_mul_f32 v163, s65, v163\n"
"v_mfma_f32_16x16x32_bf16 v[120:123], v[68:71], a[8:11], v[120:123]\n"
"v_exp_f32 v163, v163\n"
"v_mfma_f32_16x16x32_bf16 v[116:119], v[68:71], a[4:7], 0\n"
"ds_read_b64_tr_b16 v[80:81], v5 offset:2048\n"
"v_add_f32 v160, 1.0, v160\n"
"v_mfma_f32_16x16x32_bf16 v[116:119], v[84:87], a[20:23], v[116:119]\n"
"buffer_load_dwordx4 v10, s[28:31], s51 offen offset:1024 lds\n"
"v_mfma_f32_16x16x32_bf16 v[112:115], v[84:87], a[16:19], 0\n"
"v_rcp_f32 v160, v160\n"
"v_mfma_f32_16x16x32_bf16 v[112:115], v[68:71], a[0:3], v[112:115]\n"
"ds_read_b64_tr_b16 v[82:83], v5 offset:2560\n"
"s_waitcnt lgkmcnt(8)\n"
"v_mfma_f32_16x16x32_bf16 v[128:131], v[72:75], a[0:3], 0\n"
"ds_read_b64_tr_b16 v[68:69], v5 offset:1024\n"
"v_add_f32 v161, 1.0, v161\n"
"v_mfma_f32_16x16x32_bf16 v[128:131], v[88:91], a[16:19], v[128:131]\n"
"v_rcp_f32 v161, v161\n"
"v_mfma_f32_16x16x32_bf16 v[132:135], v[88:91], a[20:23], 0\n"
"ds_read_b64_tr_b16 v[70:71], v5 offset:1536\n"
"v_add_f32 v162, 1.0, v162\n"
"v_mfma_f32_16x16x32_bf16 v[132:135], v[72:75], a[4:7], v[132:135]\n"
"v_rcp_f32 v162, v162\n"
"v_mfma_f32_16x16x32_bf16 v[136:139], v[72:75], a[8:11], 0\n"
"ds_read_b64_tr_b16 v[84:85], v5 offset:3072\n"
"v_add_f32 v163, 1.0, v163\n"
"v_mfma_f32_16x16x32_bf16 v[136:139], v[88:91], a[24:27], v[136:139]\n"
"buffer_load_dwordx4 v11, s[24:27], s51 offen offset:2048 lds\n"
"v_mfma_f32_16x16x32_bf16 v[140:143], v[88:91], a[28:31], 0\n"
"v_rcp_f32 v163, v163\n"
"v_mfma_f32_16x16x32_bf16 v[140:143], v[72:75], a[12:15], v[140:143]\n"
"ds_read_b64_tr_b16 v[86:87], v5 offset:3584\n"
"s_waitcnt lgkmcnt(8)\n"
"v_mfma_f32_16x16x32_bf16 v[156:159], v[76:79], a[12:15], 0\n"
"ds_read_b64_tr_b16 v[72:73], v8 offset:0\n"
"v_mul_f32 v160, v176, v160\n"
"v_mfma_f32_16x16x32_bf16 v[156:159], v[92:95], a[28:31], v[156:159]\n"
"v_mul_f32 v161, v177, v161\n"
"v_mul_f32 v162, v178, v162\n"
"v_mfma_f32_16x16x32_bf16 v[152:155], v[92:95], a[24:27], 0\n"
"ds_read_b64_tr_b16 v[74:75], v8 offset:512\n"
"v_mul_f32 v163, v179, v163\n"
"v_mfma_f32_16x16x32_bf16 v[152:155], v[76:79], a[8:11], v[152:155]\n"
"v_cvt_pk_bf16_f32 v176, v160, v161\n"
"v_cvt_pk_bf16_f32 v177, v162, v163\n"
"v_mfma_f32_16x16x32_bf16 v[148:151], v[76:79], a[4:7], 0\n"
"ds_read_b64_tr_b16 v[88:89], v8 offset:2048\n"
"v_mul_f32 v192, s65, v192\n"
"v_mfma_f32_16x16x32_bf16 v[148:151], v[92:95], a[20:23], v[148:151]\n"
"buffer_load_dwordx4 v11, s[28:31], s51 offen offset:3072 lds\n"
"v_mfma_f32_16x16x32_bf16 v[144:147], v[92:95], a[16:19], 0\n"
"v_exp_f32 v192, v192\n"
"v_mfma_f32_16x16x32_bf16 v[144:147], v[76:79], a[0:3], v[144:147]\n"
"ds_read_b64_tr_b16 v[90:91], v8 offset:2560\n"
"s_waitcnt lgkmcnt(8)\n"
"v_mfma_f32_16x16x32_bf16 v[96:99], v[64:67], a[32:35], v[96:99]\n"
"ds_read_b64_tr_b16 v[76:77], v8 offset:1024\n"
"v_mul_f32 v193, s65, v193\n"
"v_mfma_f32_16x16x32_bf16 v[96:99], v[80:83], a[48:51], v[96:99]\n"
"v_exp_f32 v193, v193\n"
"v_mfma_f32_16x16x32_bf16 v[100:103], v[80:83], a[52:55], v[100:103]\n"
"ds_read_b64_tr_b16 v[78:79], v8 offset:1536\n"
"s_add_u32 m0, s61, 16384\n"
"v_mfma_f32_16x16x32_bf16 v[100:103], v[64:67], a[36:39], v[100:103]\n"
"v_mul_f32 v194, s65, v194\n"
"v_mul_f32 v195, s65, v195\n"
"v_mfma_f32_16x16x32_bf16 v[104:107], v[64:67], a[40:43], v[104:107]\n"
"ds_read_b64_tr_b16 v[92:93], v8 offset:3072\n"
"s_add_u32 s51, s47, s49\n"
"v_mfma_f32_16x16x32_bf16 v[104:107], v[80:83], a[56:59], v[104:107]\n"
"buffer_load_dwordx4 v10, s[24:27], s51 offen lds\n"
"v_mfma_f32_16x16x32_bf16 v[108:111], v[80:83], a[60:63], v[108:111]\n"
"v_exp_f32 v194, v194\n"
"v_mfma_f32_16x16x32_bf16 v[108:111], v[64:67], a[44:47], v[108:111]\n"
"ds_read_b64_tr_b16 v[94:95], v8 offset:3584\n"
"s_waitcnt lgkmcnt(8)\n"
"v_mfma_f32_16x16x32_bf16 v[124:127], v[68:71], a[44:47], v[124:127]\n"
"ds_read_b64_tr_b16 v[64:65], v5 offset:16384\n"
"v_add_f32 v192, 1.0, v192\n"
"v_mfma_f32_16x16x32_bf16 v[124:127], v[84:87], a[60:63], v[124:127]\n"
"v_exp_f32 v195, v195\n"
"v_mfma_f32_16x16x32_bf16 v[120:123], v[84:87], a[56:59], v[120:123]\n"
"ds_read_b64_tr_b16 v[66:67], v5 offset:16896\n"
"v_add_f32 v193, 1.0, v193\n"
"v_mfma_f32_16x16x32_bf16 v[120:123], v[68:71], a[40:43], v[120:123]\n"
"v_rcp_f32 v192, v192\n"
"v_mfma_f32_16x16x32_bf16 v[116:119], v[68:71], a[36:39], v[116:119]\n"
"ds_read_b64_tr_b16 v[80:81], v5 offset:18432\n"
"v_add_f32 v194, 1.0, v194\n"
"v_mfma_f32_16x16x32_bf16 v[116:119], v[84:87], a[52:55], v[116:119]\n"
"buffer_load_dwordx4 v10, s[28:31], s51 offen offset:1024 lds\n"
"v_mfma_f32_16x16x32_bf16 v[112:115], v[84:87], a[48:51], v[112:115]\n"
"v_rcp_f32 v193, v193\n"
"v_mfma_f32_16x16x32_bf16 v[112:115], v[68:71], a[32:35], v[112:115]\n"
"ds_read_b64_tr_b16 v[82:83], v5 offset:18944\n"
"s_waitcnt lgkmcnt(8)\n"
"v_mfma_f32_16x16x32_bf16 v[128:131], v[72:75], a[32:35], v[128:131]\n"
"ds_read_b64_tr_b16 v[68:69], v5 offset:17408\n"
"v_add_f32 v195, 1.0, v195\n"
"v_mfma_f32_16x16x32_bf16 v[128:131], v[88:91], a[48:51], v[128:131]\n"
"v_rcp_f32 v194, v194\n"
"v_mfma_f32_16x16x32_bf16 v[132:135], v[88:91], a[52:55], v[132:135]\n"
"ds_read_b64_tr_b16 v[70:71], v5 offset:17920\n"
"v_mul_f32 v192, v208, v192\n"
"v_mfma_f32_16x16x32_bf16 v[132:135], v[72:75], a[36:39], v[132:135]\n"
"v_rcp_f32 v195, v195\n"
"v_mfma_f32_16x16x32_bf16 v[136:139], v[72:75], a[40:43], v[136:139]\n"
"ds_read_b64_tr_b16 v[84:85], v5 offset:19456\n"
"v_mul_f32 v193, v209, v193\n"
"v_mfma_f32_16x16x32_bf16 v[136:139], v[88:91], a[56:59], v[136:139]\n"
"buffer_load_dwordx4 v11, s[24:27], s51 offen offset:2048 lds\n"
"v_mfma_f32_16x16x32_bf16 v[140:143], v[88:91], a[60:63], v[140:143]\n"
"v_mul_f32 v194, v210, v194\n"
"v_mul_f32 v195, v211, v195\n"
"v_mfma_f32_16x16x32_bf16 v[140:143], v[72:75], a[44:47], v[140:143]\n"
"ds_read_b64_tr_b16 v[86:87], v5 offset:19968\n"
"s_waitcnt lgkmcnt(8)\n"
"v_mfma_f32_16x16x32_bf16 v[156:159], v[76:79], a[44:47], v[156:159]\n"
"ds_read_b64_tr_b16 v[72:73], v8 offset:16384\n"
"v_cvt_pk_bf16_f32 v178, v192, v193\n"
"v_mfma_f32_16x16x32_bf16 v[156:159], v[92:95], a[60:63], v[156:159]\n"
"v_cvt_pk_bf16_f32 v179, v194, v195\n"
"v_mul_f32 v164, s65, v164\n"
"v_mfma_f32_16x16x32_bf16 v[152:155], v[92:95], a[56:59], v[152:155]\n"
"ds_read_b64_tr_b16 v[74:75], v8 offset:16896\n"
"v_mul_f32 v165, s65, v165\n"
"v_mfma_f32_16x16x32_bf16 v[152:155], v[76:79], a[40:43], v[152:155]\n"
"buffer_store_dwordx4 v[176:179], v24, s[36:39], s63 offen sc1\n"
"v_mfma_f32_16x16x32_bf16 v[148:151], v[76:79], a[36:39], v[148:151]\n"
"ds_read_b64_tr_b16 v[88:89], v8 offset:18432\n"
"v_mul_f32 v166, s65, v166\n"
"v_mfma_f32_16x16x32_bf16 v[148:151], v[92:95], a[52:55], v[148:151]\n"
"buffer_load_dwordx4 v11, s[28:31], s51 offen offset:3072 lds\n"
"v_mfma_f32_16x16x32_bf16 v[144:147], v[92:95], a[48:51], v[144:147]\n"
"v_exp_f32 v164, v164\n"
"v_mfma_f32_16x16x32_bf16 v[144:147], v[76:79], a[32:35], v[144:147]\n"
"ds_read_b64_tr_b16 v[90:91], v8 offset:18944\n"
"s_waitcnt lgkmcnt(8)\n"
"v_mfma_f32_16x16x32_bf16 v[96:99], v[64:67], a[64:67], v[96:99]\n"
"ds_read_b64_tr_b16 v[76:77], v8 offset:17408\n"
"v_mul_f32 v167, s65, v167\n"
"v_mfma_f32_16x16x32_bf16 v[96:99], v[80:83], a[80:83], v[96:99]\n"
"v_exp_f32 v165, v165\n"
"v_mfma_f32_16x16x32_bf16 v[100:103], v[80:83], a[84:87], v[100:103]\n"
"ds_read_b64_tr_b16 v[78:79], v8 offset:17920\n"
"v_add_f32 v164, 1.0, v164\n"
"v_mfma_f32_16x16x32_bf16 v[100:103], v[64:67], a[68:71], v[100:103]\n"
"v_exp_f32 v166, v166\n"
"v_mfma_f32_16x16x32_bf16 v[104:107], v[64:67], a[72:75], v[104:107]\n"
"ds_read_b64_tr_b16 v[92:93], v8 offset:19456\n"
"v_add_f32 v165, 1.0, v165\n"
"v_mfma_f32_16x16x32_bf16 v[104:107], v[80:83], a[88:91], v[104:107]\n"
"v_exp_f32 v167, v167\n"
"v_mfma_f32_16x16x32_bf16 v[108:111], v[80:83], a[92:95], v[108:111]\n"
"v_rcp_f32 v164, v164\n"
"v_mfma_f32_16x16x32_bf16 v[108:111], v[64:67], a[76:79], v[108:111]\n"
"ds_read_b64_tr_b16 v[94:95], v8 offset:19968\n"
"s_waitcnt lgkmcnt(8)\n"
"s_waitcnt vmcnt(9)\n"
"s_barrier\n"
"v_mfma_f32_16x16x32_bf16 v[124:127], v[68:71], a[76:79], v[124:127]\n"
"ds_read_b64_tr_b16 v[64:65], v5 offset:32768\n"
"s_add_u32 m0, s61, 32768\n"
"v_mfma_f32_16x16x32_bf16 v[124:127], v[84:87], a[92:95], v[124:127]\n"
"v_rcp_f32 v165, v165\n"
"v_mfma_f32_16x16x32_bf16 v[120:123], v[84:87], a[88:91], v[120:123]\n"
"ds_read_b64_tr_b16 v[66:67], v5 offset:33280\n"
"s_add_u32 s51, s48, s49\n"
"v_mfma_f32_16x16x32_bf16 v[120:123], v[68:71], a[72:75], v[120:123]\n"
"buffer_load_dwordx4 v10, s[24:27], s51 offen lds\n"
"v_mfma_f32_16x16x32_bf16 v[116:119], v[68:71], a[68:71], v[116:119]\n"
"ds_read_b64_tr_b16 v[80:81], v5 offset:34816\n"
"v_add_f32 v166, 1.0, v166\n"
"v_mfma_f32_16x16x32_bf16 v[116:119], v[84:87], a[84:87], v[116:119]\n"
"v_rcp_f32 v166, v166\n"
"v_mfma_f32_16x16x32_bf16 v[112:115], v[84:87], a[80:83], v[112:115]\n"
"v_add_f32 v167, 1.0, v167\n"
"v_mul_f32 v164, v180, v164\n"
"v_mfma_f32_16x16x32_bf16 v[112:115], v[68:71], a[64:67], v[112:115]\n"
"ds_read_b64_tr_b16 v[82:83], v5 offset:35328\n"
"s_waitcnt lgkmcnt(8)\n"
"v_mfma_f32_16x16x32_bf16 v[128:131], v[72:75], a[64:67], v[128:131]\n"
"ds_read_b64_tr_b16 v[68:69], v5 offset:33792\n"
"v_mul_f32 v165, v181, v165\n"
"v_mfma_f32_16x16x32_bf16 v[128:131], v[88:91], a[80:83], v[128:131]\n"
"v_rcp_f32 v167, v167\n"
"v_mfma_f32_16x16x32_bf16 v[132:135], v[88:91], a[84:87], v[132:135]\n"
"ds_read_b64_tr_b16 v[70:71], v5 offset:34304\n"
"v_mul_f32 v166, v182, v166\n"
"v_mfma_f32_16x16x32_bf16 v[132:135], v[72:75], a[68:71], v[132:135]\n"
"buffer_load_dwordx4 v10, s[28:31], s51 offen offset:1024 lds\n"
"v_mfma_f32_16x16x32_bf16 v[136:139], v[72:75], a[72:75], v[136:139]\n"
"ds_read_b64_tr_b16 v[84:85], v5 offset:35840\n"
"v_mul_f32 v167, v183, v167\n"
"v_mfma_f32_16x16x32_bf16 v[136:139], v[88:91], a[88:91], v[136:139]\n"
"v_cvt_pk_bf16_f32 v180, v164, v165\n"
"v_cvt_pk_bf16_f32 v181, v166, v167\n"
"v_mfma_f32_16x16x32_bf16 v[140:143], v[88:91], a[92:95], v[140:143]\n"
"buffer_load_dwordx2 v[54:55], v53, s[36:39], 0 offen sc1\n"
"v_mfma_f32_16x16x32_bf16 v[140:143], v[72:75], a[76:79], v[140:143]\n"
"ds_read_b64_tr_b16 v[86:87], v5 offset:36352\n"
"s_waitcnt lgkmcnt(8)\n"
"v_mfma_f32_16x16x32_bf16 v[156:159], v[76:79], a[76:79], v[156:159]\n"
"ds_read_b64_tr_b16 v[72:73], v8 offset:32768\n"
"v_mul_f32 v196, s65, v196\n"
"v_mfma_f32_16x16x32_bf16 v[156:159], v[92:95], a[92:95], v[156:159]\n"
"v_exp_f32 v196, v196\n"
"v_mfma_f32_16x16x32_bf16 v[152:155], v[92:95], a[88:91], v[152:155]\n"
"ds_read_b64_tr_b16 v[74:75], v8 offset:33280\n"
"v_mul_f32 v197, s65, v197\n"
"v_mfma_f32_16x16x32_bf16 v[152:155], v[76:79], a[72:75], v[152:155]\n"
"buffer_load_dwordx4 v11, s[24:27], s51 offen offset:2048 lds\n"
"v_mfma_f32_16x16x32_bf16 v[148:151], v[76:79], a[68:71], v[148:151]\n"
"ds_read_b64_tr_b16 v[88:89], v8 offset:34816\n"
"v_mul_f32 v198, s65, v198\n"
"v_mfma_f32_16x16x32_bf16 v[148:151], v[92:95], a[84:87], v[148:151]\n"
"v_exp_f32 v197, v197\n"
"v_mfma_f32_16x16x32_bf16 v[144:147], v[92:95], a[80:83], v[144:147]\n"
"buffer_load_dwordx4 v11, s[28:31], s51 offen offset:3072 lds\n"
"v_mfma_f32_16x16x32_bf16 v[144:147], v[76:79], a[64:67], v[144:147]\n"
"ds_read_b64_tr_b16 v[90:91], v8 offset:35328\n"
"s_waitcnt lgkmcnt(8)\n"
"v_mfma_f32_16x16x32_bf16 v[96:99], v[64:67], a[96:99], v[96:99]\n"
"ds_read_b64_tr_b16 v[76:77], v8 offset:33792\n"
"s_add_u32 m0, s62, 49152\n"
"v_mfma_f32_16x16x32_bf16 v[96:99], v[80:83], a[112:115], v[96:99]\n"
"v_exp_f32 v198, v198\n"
"v_mfma_f32_16x16x32_bf16 v[100:103], v[80:83], a[116:119], v[100:103]\n"
"ds_read_b64_tr_b16 v[78:79], v8 offset:34304\n"
"s_add_u32 s51, s40, s50\n"
"v_mfma_f32_16x16x32_bf16 v[100:103], v[64:67], a[100:103], v[100:103]\n"
"buffer_load_dwordx4 v10, s[72:75], s51 offen lds\n"
"v_mfma_f32_16x16x32_bf16 v[104:107], v[64:67], a[104:107], v[104:107]\n"
"ds_read_b64_tr_b16 v[92:93], v8 offset:35840\n"
"v_mul_f32 v199, s65, v199\n"
"v_mfma_f32_16x16x32_bf16 v[104:107], v[80:83], a[120:123], v[104:107]\n"
"v_exp_f32 v199, v199\n"
"v_mfma_f32_16x16x32_bf16 v[108:111], v[80:83], a[124:127], v[108:111]\n"
"buffer_load_dwordx4 v10, s[88:91], s51 offen offset:1024 lds\n"
"v_mfma_f32_16x16x32_bf16 v[108:111], v[64:67], a[108:111], v[108:111]\n"
"ds_read_b64_tr_b16 v[94:95], v8 offset:36352\n"
"s_waitcnt lgkmcnt(8)\n"
"v_mfma_f32_16x16x32_bf16 v[124:127], v[68:71], a[108:111], v[124:127]\n"
"ds_read_b64_tr_b16 v[64:65], v5 offset:49152\n"
"v_add_f32 v196, 1.0, v196\n"
"v_mfma_f32_16x16x32_bf16 v[124:127], v[84:87], a[124:127], v[124:127]\n"
"v_rcp_f32 v196, v196\n"
"v_mfma_f32_16x16x32_bf16 v[120:123], v[84:87], a[120:123], v[120:123]\n"
"ds_read_b64_tr_b16 v[66:67], v5 offset:49664\n"
"v_add_f32 v197, 1.0, v197\n"
"v_mfma_f32_16x16x32_bf16 v[120:123], v[68:71], a[104:107], v[120:123]\n"
"buffer_load_dwordx4 v11, s[72:75], s51 offen offset:2048 lds\n"
"v_mfma_f32_16x16x32_bf16 v[116:119], v[68:71], a[100:103], v[116:119]\n"
"ds_read_b64_tr_b16 v[80:81], v5 offset:51200\n"
"v_add_f32 v198, 1.0, v198\n"
"v_mfma_f32_16x16x32_bf16 v[116:119], v[84:87], a[116:119], v[116:119]\n"
"v_rcp_f32 v197, v197\n"
"v_mfma_f32_16x16x32_bf16 v[112:115], v[84:87], a[112:115], v[112:115]\n"
"buffer_load_dwordx4 v11, s[88:91], s51 offen offset:3072 lds\n"
"v_mfma_f32_16x16x32_bf16 v[112:115], v[68:71], a[96:99], v[112:115]\n"
"ds_read_b64_tr_b16 v[82:83], v5 offset:51712\n"
"s_waitcnt lgkmcnt(8)\n"
"v_mfma_f32_16x16x32_bf16 v[128:131], v[72:75], a[96:99], v[128:131]\n"
"ds_read_b64_tr_b16 v[68:69], v5 offset:50176\n"
"v_add_f32 v199, 1.0, v199\n"
"v_mfma_f32_16x16x32_bf16 v[128:131], v[88:91], a[112:115], v[128:131]\n"
"v_rcp_f32 v198, v198\n"
"v_mfma_f32_16x16x32_bf16 v[132:135], v[88:91], a[116:119], v[132:135]\n"
"ds_read_b64_tr_b16 v[70:71], v5 offset:50688\n"
"v_mul_f32 v196, v212, v196\n"
"v_mfma_f32_16x16x32_bf16 v[132:135], v[72:75], a[100:103], v[132:135]\n"
"buffer_load_dword v15, v18, s[56:59], s71 offen\n"
"v_mfma_f32_16x16x32_bf16 v[136:139], v[72:75], a[104:107], v[136:139]\n"
"ds_read_b64_tr_b16 v[84:85], v5 offset:52224\n"
"v_mul_f32 v197, v213, v197\n"
"v_mfma_f32_16x16x32_bf16 v[136:139], v[88:91], a[120:123], v[136:139]\n"
"v_rcp_f32 v199, v199\n"
"v_mfma_f32_16x16x32_bf16 v[140:143], v[88:91], a[124:127], v[140:143]\n"
"s_add_u32 s71, s71, s60\n"
"v_mul_f32 v198, v214, v198\n"
"v_mfma_f32_16x16x32_bf16 v[140:143], v[72:75], a[108:111], v[140:143]\n"
"ds_read_b64_tr_b16 v[86:87], v5 offset:52736\n"
"s_waitcnt lgkmcnt(8)\n"
"v_mfma_f32_16x16x32_bf16 v[156:159], v[76:79], a[108:111], v[156:159]\n"
"ds_read_b64_tr_b16 v[72:73], v8 offset:49152\n"
"v_mul_f32 v199, v215, v199\n"
"v_mfma_f32_16x16x32_bf16 v[156:159], v[92:95], a[124:127], v[156:159]\n"
"v_cvt_pk_bf16_f32 v182, v196, v197\n"
"v_cvt_pk_bf16_f32 v183, v198, v199\n"
"v_mfma_f32_16x16x32_bf16 v[152:155], v[92:95], a[120:123], v[152:155]\n"
"ds_read_b64_tr_b16 v[74:75], v8 offset:49664\n"
"v_mul_f32 v168, s65, v168\n"
"v_mfma_f32_16x16x32_bf16 v[152:155], v[76:79], a[104:107], v[152:155]\n"
"buffer_store_dwordx4 v[180:183], v25, s[36:39], s63 offen sc1\n"
"v_mfma_f32_16x16x32_bf16 v[148:151], v[76:79], a[100:103], v[148:151]\n"
"ds_read_b64_tr_b16 v[88:89], v8 offset:51200\n"
"v_mul_f32 v169, s65, v169\n"
"v_mfma_f32_16x16x32_bf16 v[148:151], v[92:95], a[116:119], v[148:151]\n"
"v_exp_f32 v168, v168\n"
"v_mfma_f32_16x16x32_bf16 v[144:147], v[92:95], a[112:115], v[144:147]\n"
"v_exp_f32 v169, v169\n"
"v_mfma_f32_16x16x32_bf16 v[144:147], v[76:79], a[96:99], v[144:147]\n"
"ds_read_b64_tr_b16 v[90:91], v8 offset:51712\n"
"s_waitcnt lgkmcnt(8)\n"
"v_mfma_f32_16x16x32_bf16 v[96:99], v[64:67], a[128:131], v[96:99]\n"
"ds_read_b64_tr_b16 v[76:77], v8 offset:50176\n"
"v_mul_f32 v170, s65, v170\n"
"v_mfma_f32_16x16x32_bf16 v[96:99], v[80:83], a[144:147], v[96:99]\n"
"v_exp_f32 v170, v170\n"
"v_mfma_f32_16x16x32_bf16 v[100:103], v[80:83], a[148:151], v[100:103]\n"
"ds_read_b64_tr_b16 v[78:79], v8 offset:50688\n"
"v_mul_f32 v171, s65, v171\n"
"v_mfma_f32_16x16x32_bf16 v[100:103], v[64:67], a[132:135], v[100:103]\n"
"v_exp_f32 v171, v171\n"
"v_mfma_f32_16x16x32_bf16 v[104:107], v[64:67], a[136:139], v[104:107]\n"
"ds_read_b64_tr_b16 v[92:93], v8 offset:52224\n"
"v_add_f32 v168, 1.0, v168\n"
"v_mfma_f32_16x16x32_bf16 v[104:107], v[80:83], a[152:155], v[104:107]\n"
"v_rcp_f32 v168, v168\n"
"v_mfma_f32_16x16x32_bf16 v[108:111], v[80:83], a[156:159], v[108:111]\n"
"v_add_f32 v169, 1.0, v169\n"
"v_add_f32 v170, 1.0, v170\n"
"v_mfma_f32_16x16x32_bf16 v[108:111], v[64:67], a[140:143], v[108:111]\n"
"ds_read_b64_tr_b16 v[94:95], v8 offset:52736\n"
"s_waitcnt lgkmcnt(8)\n"
"v_mfma_f32_16x16x32_bf16 v[124:127], v[68:71], a[140:143], v[124:127]\n"
"ds_read_b64_tr_b16 v[64:65], v6 offset:0\n"
"v_add_f32 v171, 1.0, v171\n"
"v_mfma_f32_16x16x32_bf16 v[124:127], v[84:87], a[156:159], v[124:127]\n"
"v_rcp_f32 v169, v169\n"
"v_mfma_f32_16x16x32_bf16 v[120:123], v[84:87], a[152:155], v[120:123]\n"
"ds_read_b64_tr_b16 v[66:67], v6 offset:512\n"
"v_mul_f32 v168, v184, v168\n"
"v_mfma_f32_16x16x32_bf16 v[120:123], v[68:71], a[136:139], v[120:123]\n"
"v_rcp_f32 v170, v170\n"
"v_mfma_f32_16x16x32_bf16 v[116:119], v[68:71], a[132:135], v[116:119]\n"
"ds_read_b64_tr_b16 v[80:81], v6 offset:2048\n"
"v_mul_f32 v169, v185, v169\n"
"v_mfma_f32_16x16x32_bf16 v[116:119], v[84:87], a[148:151], v[116:119]\n"
"v_rcp_f32 v171, v171\n"
"v_mfma_f32_16x16x32_bf16 v[112:115], v[84:87], a[144:147], v[112:115]\n"
"v_mul_f32 v170, v186, v170\n"
"v_mul_f32 v171, v187, v171\n"
"v_mfma_f32_16x16x32_bf16 v[112:115], v[68:71], a[128:131], v[112:115]\n"
"ds_read_b64_tr_b16 v[82:83], v6 offset:2560\n"
"s_waitcnt lgkmcnt(8)\n"
"v_mfma_f32_16x16x32_bf16 v[128:131], v[72:75], a[128:131], v[128:131]\n"
"ds_read_b64_tr_b16 v[68:69], v6 offset:1024\n"
"v_cvt_pk_bf16_f32 v184, v168, v169\n"
"v_mfma_f32_16x16x32_bf16 v[128:131], v[88:91], a[144:147], v[128:131]\n"
"v_cvt_pk_bf16_f32 v185, v170, v171\n"
"v_mul_f32 v200, s65, v200\n"
"v_mfma_f32_16x16x32_bf16 v[132:135], v[88:91], a[148:151], v[132:135]\n"
"ds_read_b64_tr_b16 v[70:71], v6 offset:1536\n"
"s_waitcnt vmcnt(8)\n"
"v_mfma_f32_16x16x32_bf16 v[132:135], v[72:75], a[132:135], v[132:135]\n"
"v_exp_f32 v200, v200\n"
"v_mfma_f32_16x16x32_bf16 v[136:139], v[72:75], a[136:139], v[136:139]\n"
"ds_read_b64_tr_b16 v[84:85], v6 offset:3072\n"
"s_cmp_eq_u32 s64, 1\n"
"v_mfma_f32_16x16x32_bf16 v[136:139], v[88:91], a[152:155], v[136:139]\n"
"s_cbranch_scc0 Lis9A%=\n"
"s_cmp_eq_u32 s67, 3\n"
"s_cbranch_scc1 Lic9A%=\n"
"s_cmp_eq_u32 s19, 0\n"
"s_cselect_b64 s[82:83], s[92:93], 0\n"
"s_mov_b32 s44, 0\n"
"v_cmp_eq_u64 vcc, s[82:83], v[54:55]\n"
"s_nop 0\n"
"s_bitcmp1_b32 vcc_lo, 0\n"
"s_cbranch_scc0 LkA_slow%=\n"
"LkA_ok%=:\n"
"s_branch Lis9A%=\n"
"Lic9A%=:\n"
"s_cmp_eq_u32 s19, 0\n"
"s_cselect_b64 s[82:83], s[92:93], 0\n"
"s_mov_b32 s44, 0\n"
"v_cmp_eq_u64 vcc, s[82:83], v[54:55]\n"
"s_nop 0\n"
"s_cmp_eq_u64 vcc, exec\n"
"s_cbranch_scc0 LxA_slow%=\n"
"LxA_ok%=:\n"
"Lis9A%=:\n"
"v_mul_f32 v201, s65, v201\n"
"v_mfma_f32_16x16x32_bf16 v[140:143], v[88:91], a[156:159], v[140:143]\n"
"v_exp_f32 v201, v201\n"
"v_mfma_f32_16x16x32_bf16 v[140:143], v[72:75], a[140:143], v[140:143]\n"
"ds_read_b64_tr_b16 v[86:87], v6 offset:3584\n"
"s_waitcnt lgkmcnt(8)\n"
"v_mfma_f32_16x16x32_bf16 v[156:159], v[76:79], a[140:143], v[156:159]\n"
"ds_read_b64_tr_b16 v[72:73], v9 offset:0\n"
"v_mul_f32 v202, s65, v202\n"
"v_mfma_f32_16x16x32_bf16 v[156:159], v[92:95], a[156:159], v[156:159]\n"
"v_exp_f32 v202, v202\n"
"v_mfma_f32_16x16x32_bf16 v[152:155], v[92:95], a[152:155], v[152:155]\n"
"ds_read_b64_tr_b16 v[74:75], v9 offset:512\n"
"v_mul_f32 v203, s65, v203\n"
"v_mfma_f32_16x16x32_bf16 v[152:155], v[76:79], a[136:139], v[152:155]\n"
"v_exp_f32 v203, v203\n"
"v_mfma_f32_16x16x32_bf16 v[148:151], v[76:79], a[132:135], v[148:151]\n"
"ds_read_b64_tr_b16 v[88:89], v9 offset:2048\n"
"v_add_f32 v200, 1.0, v200\n"
"v_mfma_f32_16x16x32_bf16 v[148:151], v[92:95], a[148:151], v[148:151]\n"
"v_rcp_f32 v200, v200\n"
"v_mfma_f32_16x16x32_bf16 v[144:147], v[92:95], a[144:147], v[144:147]\n"
"v_add_f32 v201, 1.0, v201\n"
"v_add_f32 v202, 1.0, v202\n"
"v_mfma_f32_16x16x32_bf16 v[144:147], v[76:79], a[128:131], v[144:147]\n"
"ds_read_b64_tr_b16 v[90:91], v9 offset:2560\n"
"s_waitcnt lgkmcnt(8)\n"
"v_mfma_f32_16x16x32_bf16 v[96:99], v[64:67], a[160:163], v[96:99]\n"
"ds_read_b64_tr_b16 v[76:77], v9 offset:1024\n"
"v_add_f32 v203, 1.0, v203\n"
"v_mfma_f32_16x16x32_bf16 v[96:99], v[80:83], a[176:179], v[96:99]\n"
"v_rcp_f32 v201, v201\n"
"v_mfma_f32_16x16x32_bf16 v[100:103], v[80:83], a[180:183], v[100:103]\n"
"ds_read_b64_tr_b16 v[78:79], v9 offset:1536\n"
"v_mul_f32 v200, v216, v200\n"
"v_mfma_f32_16x16x32_bf16 v[100:103], v[64:67], a[164:167], v[100:103]\n"
"v_rcp_f32 v202, v202\n"
"v_mfma_f32_16x16x32_bf16 v[104:107], v[64:67], a[168:171], v[104:107]\n"
"ds_read_b64_tr_b16 v[92:93], v9 offset:3072\n"
"v_mul_f32 v201, v217, v201\n"
"v_mfma_f32_16x16x32_bf16 v[104:107], v[80:83], a[184:187], v[104:107]\n"
"v_rcp_f32 v203, v203\n"
"v_mfma_f32_16x16x32_bf16 v[108:111], v[80:83], a[188:191], v[108:111]\n"
"v_mul_f32 v202, v218, v202\n"
"v_mul_f32 v203, v219, v203\n"
"v_mfma_f32_16x16x32_bf16 v[108:111], v[64:67], a[172:175], v[108:111]\n"
"ds_read_b64_tr_b16 v[94:95], v9 offset:3584\n"
"s_waitcnt lgkmcnt(8)\n"
"s_waitcnt vmcnt(6)\n"
"s_barrier\n"
"v_mfma_f32_16x16x32_bf16 v[124:127], v[68:71], a[172:175], v[124:127]\n"
"ds_read_b64_tr_b16 v[64:65], v4 offset:0\n"
"s_add_u32 m0, s62, 65536\n"
"v_mfma_f32_16x16x32_bf16 v[124:127], v[84:87], a[188:191], v[124:127]\n"
"v_cvt_pk_bf16_f32 v186, v200, v201\n"
"v_cvt_pk_bf16_f32 v187, v202, v203\n"
"v_mfma_f32_16x16x32_bf16 v[120:123], v[84:87], a[184:187], v[120:123]\n"
"ds_read_b64_tr_b16 v[66:67], v4 offset:512\n"
"s_add_u32 s51, s41, s50\n"
"v_mfma_f32_16x16x32_bf16 v[120:123], v[68:71], a[168:171], v[120:123]\n"
"buffer_load_dwordx4 v10, s[72:75], s51 offen lds\n"
"v_mfma_f32_16x16x32_bf16 v[116:119], v[68:71], a[164:167], v[116:119]\n"
"ds_read_b64_tr_b16 v[80:81], v4 offset:2048\n"
"v_mul_f32 v172, s65, v172\n"
"v_mfma_f32_16x16x32_bf16 v[116:119], v[84:87], a[180:183], v[116:119]\n"
"buffer_store_dwordx4 v[184:187], v26, s[36:39], s63 offen sc1\n"
"v_mfma_f32_16x16x32_bf16 v[112:115], v[84:87], a[176:179], v[112:115]\n"
"buffer_load_dwordx4 v10, s[88:91], s51 offen offset:1024 lds\n"
"v_mfma_f32_16x16x32_bf16 v[112:115], v[68:71], a[160:163], v[112:115]\n"
"ds_read_b64_tr_b16 v[82:83], v4 offset:2560\n"
"s_waitcnt lgkmcnt(8)\n"
"v_mfma_f32_16x16x32_bf16 v[128:131], v[72:75], a[160:163], v[128:131]\n"
"ds_read_b64_tr_b16 v[68:69], v4 offset:1024\n"
"v_mul_f32 v173, s65, v173\n"
"v_mfma_f32_16x16x32_bf16 v[128:131], v[88:91], a[176:179], v[128:131]\n"
"buffer_store_dwordx2 v[62:63], v61, s[36:39], s77 offen sc1\n"
"v_mfma_f32_16x16x32_bf16 v[132:135], v[88:91], a[180:183], v[132:135]\n"
"ds_read_b64_tr_b16 v[70:71], v4 offset:1536\n"
"v_mul_f32 v174, s65, v174\n"
"v_mfma_f32_16x16x32_bf16 v[132:135], v[72:75], a[164:167], v[132:135]\n"
"buffer_load_dwordx4 v11, s[72:75], s51 offen offset:2048 lds\n"
"v_mfma_f32_16x16x32_bf16 v[136:139], v[72:75], a[168:171], v[136:139]\n"
"ds_read_b64_tr_b16 v[84:85], v4 offset:3072\n"
"v_mul_f32 v175, s65, v175\n"
"v_mfma_f32_16x16x32_bf16 v[136:139], v[88:91], a[184:187], v[136:139]\n"
"v_exp_f32 v172, v172\n"
"v_mfma_f32_16x16x32_bf16 v[140:143], v[88:91], a[188:191], v[140:143]\n"
"v_exp_f32 v173, v173\n"
"v_mfma_f32_16x16x32_bf16 v[140:143], v[72:75], a[172:175], v[140:143]\n"
"ds_read_b64_tr_b16 v[86:87], v4 offset:3584\n"
"s_waitcnt lgkmcnt(8)\n"
"v_mfma_f32_16x16x32_bf16 v[156:159], v[76:79], a[172:175], v[156:159]\n"
"ds_read_b64_tr_b16 v[72:73], v7 offset:0\n"
"s_cmp_eq_u32 s64, 1\n"
"v_mfma_f32_16x16x32_bf16 v[156:159], v[92:95], a[188:191], v[156:159]\n"
"s_cbranch_scc0 Lis11A%=\n"
"s_cmp_eq_u32 s67, 3\n"
"s_cbranch_scc1 Lic11A%=\n"
"s_lshl_b32 s48, s19, 12\n"
"s_add_u32 s48, s48, 73728\n"
"s_mul_i32 s80, s19, 6144\n"
"s_sub_u32 s24, s84, 2048\n"
"s_subb_u32 s25, s85, 0\n"
"s_and_b32 s25, s25, 0xffff\n"
"s_mov_b32 s26, 5310464\n"
"s_mov_b32 s27, 0x20000\n"
"s_mov_b32 s28, s36\n"
"s_mov_b32 s29, s37\n"
"s_mov_b32 s30, s38\n"
"s_mov_b32 s31, s39\n"
"v_readlane_b32 s46, v51, s44\n"
"v_readlane_b32 s47, v52, s44\n"
"s_add_u32 s44, s44, 1\n"
"s_add_u32 m0, s80, 49152\n"
"s_nop 2\n"
"buffer_load_dwordx4 v44, s[24:27], s47 offen offset:0 lds\n"
"buffer_load_dwordx4 v45, s[24:27], s47 offen offset:1024 lds\n"
"buffer_load_dwordx4 v46, s[24:27], s47 offen offset:2048 lds\n"
"s_add_u32 m0, s48, 32768\n"
"s_nop 0\n"
"buffer_load_dwordx4 v50, s[28:31], s46 offen offset:0 sc1 lds\n"
"buffer_load_dwordx4 v50, s[28:31], s46 offen offset:1024 sc1 lds\n"
"s_mov_b32 m0, 0x30000\n"
"s_branch Lis11A%=\n"
"Lic11A%=:\n"
"s_lshr_b32 s55, s34, 1\n"
"s_lshl_b32 s55, s55, 4\n"
"s_add_u32 s55, s55, s66\n"
"s_lshl_b32 s55, s55, 1\n"
"s_and_b32 s56, s34, 1\n"
"s_add_u32 s55, s55, s56\n"
"s_lshr_b32 s46, s55, 1\n"
"s_mul_i32 s46, s46, 18\n"
"s_bitcmp1_b32 s55, 0\n"
"s_cselect_b32 s56, 9, 0\n"
"s_cselect_b32 s47, 8, 8\n"
"s_add_u32 s46, s46, s56\n"
"s_lshr_b32 s56, s19, 1\n"
"s_and_b32 s57, s19, 1\n"
"s_mov_b32 s24, s84\n"
"s_and_b32 s25, s85, 0xffff\n"
"s_mov_b32 s26, 2359296\n"
"s_mov_b32 s27, 0x20000\n"
"s_mov_b32 s40, s86\n"
"s_and_b32 s41, s87, 0xffff\n"
"s_mov_b32 s42, 1152\n"
"s_mov_b32 s43, 0x20000\n"
"v_add_u32 v48, s46, v0\n"
"v_lshlrev_b32 v48, 1, v48\n"
"s_add_u32 s46, s46, s56\n"
"s_add_u32 s44, s56, 62\n"
"s_lshl_b32 s71, s46, 7\n"
"s_add_u32 s71, s71, 23592704\n"
"s_mul_i32 s53, s56, 26880\n"
"s_mul_i32 s58, s57, 5120\n"
"s_add_u32 s54, s53, s58\n"
"s_add_u32 s54, s54, 17664\n"
"s_mul_i32 s58, s57, 8320\n"
"s_add_u32 s53, s53, s58\n"
"s_mov_b32 s28, s36\n"
"s_mov_b32 s29, s37\n"
"s_mov_b32 s30, s38\n"
"s_mov_b32 s31, s39\n"
"s_mov_b32 s30, 0\n"
"v_add_u32 v57, 4, v42\n"
"v_add_u32 v56, 4, v44\n"
"v_add_u32 v58, 589824, v45\n"
"s_add_u32 m0, s54, 53760\n"
"s_lshl_b32 s45, s46, 13\n"
"buffer_load_dwordx4 v43, s[36:39], s45 offen offset:0 sc1 lds\n"
"s_add_u32 s45, s45, 1024\n"
"buffer_load_dwordx4 v43, s[36:39], s45 offen offset:1024 sc1 lds\n"
"s_add_u32 s45, s45, 1024\n"
"buffer_load_dwordx4 v43, s[36:39], s45 offen offset:2048 sc1 lds\n"
"s_add_u32 s45, s45, 1024\n"
"buffer_load_dwordx4 v43, s[36:39], s45 offen offset:3072 sc1 lds\n"
"s_add_u32 m0, s53, 53760\n"
"s_lshl_b32 s48, s46, 12\n"
"buffer_load_dword v44, s[24:27], s48 offen lds\n"
"s_add_u32 m0, s53, 53824\n"
"s_nop 0\n"
"buffer_load_dwordx4 v42, s[24:27], s48 offen offset:0 lds\n"
"buffer_load_dwordx4 v42, s[24:27], s48 offen offset:1024 lds\n"
"s_mov_b32 m0, 0x30000\n"
"Lis11A%=:\n"
"v_add_f32 v172, 1.0, v172\n"
"v_mfma_f32_16x16x32_bf16 v[152:155], v[92:95], a[184:187], v[152:155]\n"
"ds_read_b64_tr_b16 v[74:75], v7 offset:512\n"
"v_add_f32 v173, 1.0, v173\n"
"v_mfma_f32_16x16x32_bf16 v[152:155], v[76:79], a[168:171], v[152:155]\n"
"buffer_load_dwordx4 v11, s[88:91], s51 offen offset:3072 lds\n"
"v_mfma_f32_16x16x32_bf16 v[148:151], v[76:79], a[164:167], v[148:151]\n"
"ds_read_b64_tr_b16 v[88:89], v7 offset:2048\n"
"v_mul_f32 v204, s65, v204\n"
"v_mfma_f32_16x16x32_bf16 v[148:151], v[92:95], a[180:183], v[148:151]\n"
"v_exp_f32 v174, v174\n"
"v_mfma_f32_16x16x32_bf16 v[144:147], v[92:95], a[176:179], v[144:147]\n"
"v_exp_f32 v175, v175\n"
"v_mfma_f32_16x16x32_bf16 v[144:147], v[76:79], a[160:163], v[144:147]\n"
"ds_read_b64_tr_b16 v[90:91], v7 offset:2560\n"
"s_waitcnt lgkmcnt(8)\n"
"v_mfma_f32_16x16x32_bf16 v[96:99], v[64:67], a[192:195], v[96:99]\n"
"ds_read_b64_tr_b16 v[76:77], v7 offset:1024\n"
"s_add_u32 m0, s62, 81920\n"
"v_mfma_f32_16x16x32_bf16 v[96:99], v[80:83], a[208:211], v[96:99]\n"
"v_rcp_f32 v172, v172\n"
"v_mfma_f32_16x16x32_bf16 v[100:103], v[80:83], a[212:215], v[100:103]\n"
"ds_read_b64_tr_b16 v[78:79], v7 offset:1536\n"
"s_add_u32 s51, s42, s50\n"
"v_mfma_f32_16x16x32_bf16 v[100:103], v[64:67], a[196:199], v[100:103]\n"
"buffer_load_dwordx4 v10, s[72:75], s51 offen lds\n"
"v_mfma_f32_16x16x32_bf16 v[104:107], v[64:67], a[200:203], v[104:107]\n"
"ds_read_b64_tr_b16 v[92:93], v7 offset:3072\n"
"v_add_f32 v174, 1.0, v174\n"
"v_mfma_f32_16x16x32_bf16 v[104:107], v[80:83], a[216:219], v[104:107]\n"
"v_rcp_f32 v173, v173\n"
"v_mfma_f32_16x16x32_bf16 v[108:111], v[80:83], a[220:223], v[108:111]\n"
"buffer_load_dwordx4 v10, s[88:91], s51 offen offset:1024 lds\n"
"v_mfma_f32_16x16x32_bf16 v[108:111], v[64:67], a[204:207], v[108:111]\n"
"ds_read_b64_tr_b16 v[94:95], v7 offset:3584\n"
"s_waitcnt lgkmcnt(8)\n"
"v_mfma_f32_16x16x32_bf16 v[124:127], v[68:71], a[204:207], v[124:127]\n"
"ds_read_b64_tr_b16 v[64:65], v4 offset:16384\n"
"v_add_f32 v175, 1.0, v175\n"
"v_mfma_f32_16x16x32_bf16 v[124:127], v[84:87], a[220:223], v[124:127]\n"
"v_rcp_f32 v174, v174\n"
"v_mfma_f32_16x16x32_bf16 v[120:123], v[84:87], a[216:219], v[120:123]\n"
"ds_read_b64_tr_b16 v[66:67], v4 offset:16896\n"
"v_mul_f32 v172, v188, v172\n"
"v_mfma_f32_16x16x32_bf16 v[120:123], v[68:71], a[200:203], v[120:123]\n"
"buffer_load_dwordx4 v11, s[72:75], s51 offen offset:2048 lds\n"
"v_mfma_f32_16x16x32_bf16 v[116:119], v[68:71], a[196:199], v[116:119]\n"
"ds_read_b64_tr_b16 v[80:81], v4 offset:18432\n"
"v_mul_f32 v173, v189, v173\n"
"v_mfma_f32_16x16x32_bf16 v[116:119], v[84:87], a[212:215], v[116:119]\n"
"v_rcp_f32 v175, v175\n"
"v_mfma_f32_16x16x32_bf16 v[112:115], v[84:87], a[208:211], v[112:115]\n"
"v_exp_f32 v204, v204\n"
"v_mfma_f32_16x16x32_bf16 v[112:115], v[68:71], a[192:195], v[112:115]\n"
"ds_read_b64_tr_b16 v[82:83], v4 offset:18944\n"
"s_waitcnt lgkmcnt(8)\n"
"v_mfma_f32_16x16x32_bf16 v[128:131], v[72:75], a[192:195], v[128:131]\n"
"ds_read_b64_tr_b16 v[68:69], v4 offset:17408\n"
"v_mul_f32 v174, v190, v174\n"
"v_mfma_f32_16x16x32_bf16 v[128:131], v[88:91], a[208:211], v[128:131]\n"
"v_mul_f32 v175, v191, v175\n"
"v_cvt_pk_bf16_f32 v188, v172, v173\n"
"v_mfma_f32_16x16x32_bf16 v[132:135], v[88:91], a[212:215], v[132:135]\n"
"ds_read_b64_tr_b16 v[70:71], v4 offset:17920\n"
"v_cvt_pk_bf16_f32 v189, v174, v175\n"
"v_mfma_f32_16x16x32_bf16 v[132:135], v[72:75], a[196:199], v[132:135]\n"
"buffer_load_dwordx4 v11, s[88:91], s51 offen offset:3072 lds\n"
"v_mfma_f32_16x16x32_bf16 v[136:139], v[72:75], a[200:203], v[136:139]\n"
"ds_read_b64_tr_b16 v[84:85], v4 offset:19456\n"
"v_mul_f32 v205, s65, v205\n"
"v_mfma_f32_16x16x32_bf16 v[136:139], v[88:91], a[216:219], v[136:139]\n"
"v_exp_f32 v205, v205\n"
"v_mfma_f32_16x16x32_bf16 v[140:143], v[88:91], a[220:223], v[140:143]\n"
"v_mul_f32 v206, s65, v206\n"
"v_mul_f32 v207, s65, v207\n"
"v_mfma_f32_16x16x32_bf16 v[140:143], v[72:75], a[204:207], v[140:143]\n"
"ds_read_b64_tr_b16 v[86:87], v4 offset:19968\n"
"s_waitcnt lgkmcnt(8)\n"
"v_mfma_f32_16x16x32_bf16 v[156:159], v[76:79], a[204:207], v[156:159]\n"
"ds_read_b64_tr_b16 v[72:73], v7 offset:16384\n"
"s_add_u32 m0, s62, 98304\n"
"v_mfma_f32_16x16x32_bf16 v[156:159], v[92:95], a[220:223], v[156:159]\n"
"v_exp_f32 v206, v206\n"
"v_mfma_f32_16x16x32_bf16 v[152:155], v[92:95], a[216:219], v[152:155]\n"
"ds_read_b64_tr_b16 v[74:75], v7 offset:16896\n"
"s_add_u32 s51, s43, s50\n"
"v_mfma_f32_16x16x32_bf16 v[152:155], v[76:79], a[200:203], v[152:155]\n"
"buffer_load_dwordx4 v10, s[72:75], s51 offen lds\n"
"v_mfma_f32_16x16x32_bf16 v[148:151], v[76:79], a[196:199], v[148:151]\n"
"ds_read_b64_tr_b16 v[88:89], v7 offset:18432\n"
"v_add_f32 v204, 1.0, v204\n"
"v_mfma_f32_16x16x32_bf16 v[148:151], v[92:95], a[212:215], v[148:151]\n"
"v_exp_f32 v207, v207\n"
"v_mfma_f32_16x16x32_bf16 v[144:147], v[92:95], a[208:211], v[144:147]\n"
"buffer_load_dwordx4 v10, s[88:91], s51 offen offset:1024 lds\n"
"v_mfma_f32_16x16x32_bf16 v[144:147], v[76:79], a[192:195], v[144:147]\n"
"ds_read_b64_tr_b16 v[90:91], v7 offset:18944\n"
"s_waitcnt lgkmcnt(8)\n"
"v_mfma_f32_16x16x32_bf16 v[96:99], v[64:67], a[224:227], v[96:99]\n"
"ds_read_b64_tr_b16 v[76:77], v7 offset:17408\n"
"v_add_f32 v205, 1.0, v205\n"
"v_mfma_f32_16x16x32_bf16 v[96:99], v[80:83], a[240:243], v[96:99]\n"
"v_rcp_f32 v204, v204\n"
"v_mfma_f32_16x16x32_bf16 v[100:103], v[80:83], a[244:247], v[100:103]\n"
"ds_read_b64_tr_b16 v[78:79], v7 offset:17920\n"
"v_add_f32 v206, 1.0, v206\n"
"v_mfma_f32_16x16x32_bf16 v[100:103], v[64:67], a[228:231], v[100:103]\n"
"buffer_load_dwordx4 v11, s[72:75], s51 offen offset:2048 lds\n"
"v_mfma_f32_16x16x32_bf16 v[104:107], v[64:67], a[232:235], v[104:107]\n"
"ds_read_b64_tr_b16 v[92:93], v7 offset:19456\n"
"s_cmp_eq_u32 s64, 1\n"
"v_mfma_f32_16x16x32_bf16 v[104:107], v[80:83], a[248:251], v[104:107]\n"
"s_cbranch_scc0 Lis14A%=\n"
"s_cmp_eq_u32 s67, 3\n"
"s_cbranch_scc1 Lic14A%=\n"
"s_add_u32 m0, s48, 32768\n"
"s_nop 0\n"
"buffer_load_dwordx4 v50, s[28:31], s46 offen offset:2048 sc1 lds\n"
"buffer_load_dwordx4 v50, s[28:31], s46 offen offset:3072 sc1 lds\n"
"s_add_u32 m0, s80, 52224\n"
"s_nop 0\n"
"buffer_load_dwordx4 v47, s[24:27], s47 offen offset:0 lds\n"
"buffer_load_dwordx4 v48, s[24:27], s47 offen offset:1024 lds\n"
"buffer_load_dwordx4 v49, s[24:27], s47 offen offset:2048 lds\n"
"s_mov_b32 m0, 0x30000\n"
"s_branch Lis14A%=\n"
"Lic14A%=:\n"
"s_add_u32 m0, s53, 53824\n"
"s_nop 0\n"
"buffer_load_dwordx4 v42, s[24:27], s48 offen offset:2048 lds\n"
"buffer_load_dwordx4 v42, s[24:27], s48 offen offset:3072 lds\n"
"s_add_u32 m0, s53, 57920\n"
"s_add_u32 s46, s46, 2\n"
"buffer_load_dword v56, s[24:27], s48 offen lds\n"
"s_add_u32 m0, s53, 57984\n"
"s_min_u32 s46, s46, 575\n"
"buffer_load_dwordx4 v57, s[24:27], s48 offen offset:0 lds\n"
"buffer_load_dwordx4 v57, s[24:27], s48 offen offset:1024 lds\n"
"buffer_load_dwordx4 v57, s[24:27], s48 offen offset:2048 lds\n"
"buffer_load_dwordx4 v57, s[24:27], s48 offen offset:3072 lds\n"
"buffer_load_ushort v48, v48, s[40:43], 0 offen\n"
"s_mov_b32 m0, 0x30000\n"
"Lis14A%=:\n"
"v_add_f32 v207, 1.0, v207\n"
"v_mfma_f32_16x16x32_bf16 v[108:111], v[80:83], a[252:255], v[108:111]\n"
"v_rcp_f32 v205, v205\n"
"v_mfma_f32_16x16x32_bf16 v[108:111], v[64:67], a[236:239], v[108:111]\n"
"ds_read_b64_tr_b16 v[94:95], v7 offset:19968\n"
"s_waitcnt lgkmcnt(8)\n"
"v_mfma_f32_16x16x32_bf16 v[124:127], v[68:71], a[236:239], v[124:127]\n"
"ds_read_b64_tr_b16 v[64:65], v4 offset:32768\n"
"v_mul_f32 v204, v220, v204\n"
"v_mfma_f32_16x16x32_bf16 v[124:127], v[84:87], a[252:255], v[124:127]\n"
"v_rcp_f32 v206, v206\n"
"v_mfma_f32_16x16x32_bf16 v[120:123], v[84:87], a[248:251], v[120:123]\n"
"ds_read_b64_tr_b16 v[66:67], v4 offset:33280\n"
"v_mul_f32 v205, v221, v205\n"
"v_mfma_f32_16x16x32_bf16 v[120:123], v[68:71], a[232:235], v[120:123]\n"
"buffer_load_dwordx4 v11, s[88:91], s51 offen offset:3072 lds\n"
"v_mfma_f32_16x16x32_bf16 v[116:119], v[68:71], a[228:231], v[116:119]\n"
"ds_read_b64_tr_b16 v[80:81], v4 offset:34816\n"
"v_mul_f32 v206, v222, v206\n"
"v_mfma_f32_16x16x32_bf16 v[116:119], v[84:87], a[244:247], v[116:119]\n"
"v_rcp_f32 v207, v207\n"
"v_mfma_f32_16x16x32_bf16 v[112:115], v[84:87], a[240:243], v[112:115]\n"
"v_mul_f32 v207, v223, v207\n"
"v_cvt_pk_bf16_f32 v190, v204, v205\n"
"v_mfma_f32_16x16x32_bf16 v[112:115], v[68:71], a[224:227], v[112:115]\n"
"ds_read_b64_tr_b16 v[82:83], v4 offset:35328\n"
"s_waitcnt lgkmcnt(8)\n"
"v_mfma_f32_16x16x32_bf16 v[128:131], v[72:75], a[224:227], v[128:131]\n"
"ds_read_b64_tr_b16 v[68:69], v4 offset:33792\n"
"s_add_u32 m0, s62, 114688\n"
"v_mfma_f32_16x16x32_bf16 v[128:131], v[88:91], a[240:243], v[128:131]\n"
"buffer_load_dwordx2 v[54:55], v53, s[36:39], 0 offen sc1\n"
"v_mfma_f32_16x16x32_bf16 v[132:135], v[88:91], a[244:247], v[132:135]\n"
"ds_read_b64_tr_b16 v[70:71], v4 offset:34304\n"
"s_add_u32 s51, s44, s50\n"
"v_mfma_f32_16x16x32_bf16 v[132:135], v[72:75], a[228:231], v[132:135]\n"
"buffer_load_dwordx4 v10, s[72:75], s51 offen lds\n"
"v_mfma_f32_16x16x32_bf16 v[136:139], v[72:75], a[232:235], v[136:139]\n"
"ds_read_b64_tr_b16 v[84:85], v4 offset:35840\n"
"v_cvt_pk_bf16_f32 v191, v206, v207\n"
"v_mfma_f32_16x16x32_bf16 v[136:139], v[88:91], a[248:251], v[136:139]\n"
"buffer_store_dwordx4 v[188:191], v27, s[36:39], s63 offen sc1\n"
"v_mfma_f32_16x16x32_bf16 v[140:143], v[88:91], a[252:255], v[140:143]\n"
"buffer_load_dwordx4 v10, s[88:91], s51 offen offset:1024 lds\n"
"v_mfma_f32_16x16x32_bf16 v[140:143], v[72:75], a[236:239], v[140:143]\n"
"ds_read_b64_tr_b16 v[86:87], v4 offset:36352\n"
"s_waitcnt lgkmcnt(8)\n"
"v_mfma_f32_16x16x32_bf16 v[156:159], v[76:79], a[236:239], v[156:159]\n"
"ds_read_b64_tr_b16 v[72:73], v7 offset:32768\n"
"v_mfma_f32_16x16x32_bf16 v[156:159], v[92:95], a[252:255], v[156:159]\n"
"v_mfma_f32_16x16x32_bf16 v[152:155], v[92:95], a[248:251], v[152:155]\n"
"ds_read_b64_tr_b16 v[74:75], v7 offset:33280\n"
"v_mfma_f32_16x16x32_bf16 v[152:155], v[76:79], a[232:235], v[152:155]\n"
"buffer_load_dwordx4 v11, s[72:75], s51 offen offset:2048 lds\n"
"v_mfma_f32_16x16x32_bf16 v[148:151], v[76:79], a[228:231], v[148:151]\n"
"ds_read_b64_tr_b16 v[88:89], v7 offset:34816\n"
"v_mfma_f32_16x16x32_bf16 v[148:151], v[92:95], a[244:247], v[148:151]\n"
"v_mfma_f32_16x16x32_bf16 v[144:147], v[92:95], a[240:243], v[144:147]\n"
"v_mfma_f32_16x16x32_bf16 v[144:147], v[76:79], a[224:227], v[144:147]\n"
"ds_read_b64_tr_b16 v[90:91], v7 offset:35328\n"
"s_waitcnt lgkmcnt(8)\n"
"v_mfma_f32_16x16x32_bf16 v[96:99], v[64:67], v[224:227], v[96:99]\n"
"ds_read_b64_tr_b16 v[76:77], v7 offset:33792\n"
"v_mfma_f32_16x16x32_bf16 v[96:99], v[80:83], v[240:243], v[96:99]\n"
"v_mfma_f32_16x16x32_bf16 v[100:103], v[80:83], v[244:247], v[100:103]\n"
"ds_read_b64_tr_b16 v[78:79], v7 offset:34304\n"
"v_mfma_f32_16x16x32_bf16 v[100:103], v[64:67], v[228:231], v[100:103]\n"
"buffer_load_dwordx4 v11, s[88:91], s51 offen offset:3072 lds\n"
"v_mfma_f32_16x16x32_bf16 v[104:107], v[64:67], v[232:235], v[104:107]\n"
"ds_read_b64_tr_b16 v[92:93], v7 offset:35840\n"
"v_mfma_f32_16x16x32_bf16 v[104:107], v[80:83], v[248:251], v[104:107]\n"
"v_mfma_f32_16x16x32_bf16 v[108:111], v[80:83], v[252:255], v[108:111]\n"
"v_mfma_f32_16x16x32_bf16 v[108:111], v[64:67], v[236:239], v[108:111]\n"
"ds_read_b64_tr_b16 v[94:95], v7 offset:36352\n"
"s_waitcnt lgkmcnt(8)\n"
"s_waitcnt vmcnt(10)\n"
"s_barrier\n"
"v_mfma_f32_16x16x32_bf16 v[124:127], v[68:71], v[236:239], v[124:127]\n"
"ds_read_b64_tr_b16 v[64:65], v4 offset:49152\n"
"v_mfma_f32_16x16x32_bf16 v[124:127], v[84:87], v[252:255], v[124:127]\n"
"v_mfma_f32_16x16x32_bf16 v[120:123], v[84:87], v[248:251], v[120:123]\n"
"ds_read_b64_tr_b16 v[66:67], v4 offset:49664\n"
"s_add_u32 m0, s62, 131072\n"
"v_mfma_f32_16x16x32_bf16 v[120:123], v[68:71], v[232:235], v[120:123]\n"
"v_mfma_f32_16x16x32_bf16 v[116:119], v[68:71], v[228:231], v[116:119]\n"
"ds_read_b64_tr_b16 v[80:81], v4 offset:51200\n"
"s_add_u32 s51, s45, s50\n"
"v_mfma_f32_16x16x32_bf16 v[116:119], v[84:87], v[244:247], v[116:119]\n"
"buffer_load_dwordx4 v10, s[72:75], s51 offen lds\n"
"v_mfma_f32_16x16x32_bf16 v[112:115], v[84:87], v[240:243], v[112:115]\n"
"v_mfma_f32_16x16x32_bf16 v[112:115], v[68:71], v[224:227], v[112:115]\n"
"ds_read_b64_tr_b16 v[82:83], v4 offset:51712\n"
"s_waitcnt lgkmcnt(8)\n"
"v_mfma_f32_16x16x32_bf16 v[128:131], v[72:75], v[224:227], v[128:131]\n"
"ds_read_b64_tr_b16 v[68:69], v4 offset:50176\n"
"s_mov_b32 s77, s76\n"
"v_mfma_f32_16x16x32_bf16 v[128:131], v[88:91], v[240:243], v[128:131]\n"
"buffer_load_dwordx4 v10, s[88:91], s51 offen offset:1024 lds\n"
"v_mfma_f32_16x16x32_bf16 v[132:135], v[88:91], v[244:247], v[132:135]\n"
"ds_read_b64_tr_b16 v[70:71], v4 offset:50688\n"
"s_lshr_b32 s76, s49, 4\n"
"v_mfma_f32_16x16x32_bf16 v[132:135], v[72:75], v[228:231], v[132:135]\n"
"buffer_load_dwordx4 v11, s[72:75], s51 offen offset:2048 lds\n"
"v_mfma_f32_16x16x32_bf16 v[136:139], v[72:75], v[232:235], v[136:139]\n"
"ds_read_b64_tr_b16 v[84:85], v4 offset:52224\n"
"s_add_u32 s76, s76, s78\n"
"v_mfma_f32_16x16x32_bf16 v[136:139], v[88:91], v[248:251], v[136:139]\n"
"s_lshl_b32 s63, s49, 11\n"
"s_lshl_b32 s69, s66, 14\n"
"v_mfma_f32_16x16x32_bf16 v[140:143], v[88:91], v[252:255], v[140:143]\n"
"buffer_load_dwordx4 v11, s[88:91], s51 offen offset:3072 lds\n"
"v_mfma_f32_16x16x32_bf16 v[140:143], v[72:75], v[236:239], v[140:143]\n"
"ds_read_b64_tr_b16 v[86:87], v4 offset:52736\n"
"s_waitcnt lgkmcnt(8)\n"
"v_mfma_f32_16x16x32_bf16 v[156:159], v[76:79], v[236:239], v[156:159]\n"
"ds_read_b64_tr_b16 v[72:73], v7 offset:49152\n"
"s_add_u32 s63, s63, s69\n"
"v_mfma_f32_16x16x32_bf16 v[156:159], v[92:95], v[252:255], v[156:159]\n"
"v_mfma_f32_16x16x32_bf16 v[152:155], v[92:95], v[248:251], v[152:155]\n"
"ds_read_b64_tr_b16 v[74:75], v7 offset:49664\n"
"s_mov_b32 s49, s50\n"
"v_mfma_f32_16x16x32_bf16 v[152:155], v[76:79], v[232:235], v[152:155]\n"
"v_mfma_f32_16x16x32_bf16 v[148:151], v[76:79], v[228:231], v[148:151]\n"
"ds_read_b64_tr_b16 v[88:89], v7 offset:51200\n"
"s_add_u32 s50, s50, 128\n"
"v_mfma_f32_16x16x32_bf16 v[148:151], v[92:95], v[244:247], v[148:151]\n"
"s_add_u32 s68, s50, 128\n"
"s_sub_u32 s64, s64, 1\n"
"v_mfma_f32_16x16x32_bf16 v[144:147], v[92:95], v[240:243], v[144:147]\n"
"s_cmp_gt_u32 s64, 1\n"
"s_cselect_b32 s74, s74, 0\n"
"v_mfma_f32_16x16x32_bf16 v[144:147], v[76:79], v[224:227], v[144:147]\n"
"ds_read_b64_tr_b16 v[90:91], v7 offset:51712\n"
"s_cselect_b32 s90, s90, 0\n"
"s_cselect_b32 s62, s61, 0x1c000\n"
"s_waitcnt lgkmcnt(8)\n"
"s_mov_b32 s81, 0\n"
"s_cmp_lg_u32 s64, 0\n"
"s_cbranch_scc1 LpB%=\n"
"Ltrans%=:\n"
"s_waitcnt lgkmcnt(0)\n"
"s_cmp_eq_u32 s67, 3\n"
"s_cbranch_scc1 Lcpro%=\n"
"s_mov_b32 s50, s79\n"
"s_mov_b32 s40, s86\n"
"s_and_b32 s41, s87, 0xffff\n"
"s_mov_b32 s42, 2359296\n"
"s_mov_b32 s43, 0x20000\n"
"s_waitcnt vmcnt(0)\n"
"v_cmp_eq_u64 vcc, s[82:83], v[54:55]\n"
"s_nop 0\n"
"s_lshr_b64 s[56:57], vcc, s44\n"
"s_and_b32 s56, s56, 7\n"
"s_cmp_eq_u32 s56, 7\n"
"s_cbranch_scc0 Lc0_slow%=\n"
"Lc0_ok%=:\n"
"s_barrier\n"
"buffer_store_dwordx2 v[62:63], v61, s[36:39], s77 offen sc1\n"
"s_cmp_eq_u32 s81, 0\n"
"s_cbranch_scc1 LgA%=\n"
"v_mul_f32 v160, s65, v160\n"
"v_mul_f32 v161, s65, v161\n"
"v_mul_f32 v162, s65, v162\n"
"v_readlane_b32 s68, v51, s44\n"
"v_mul_f32 v163, s65, v163\n"
"v_mul_f32 v192, s65, v192\n"
"v_mul_f32 v193, s65, v193\n"
"ds_read_b64_tr_b16 a[16:17], v36 offset:0\n"
"v_mul_f32 v194, s65, v194\n"
"v_mul_f32 v195, s65, v195\n"
"v_exp_f32 v160, v160\n"
"v_readlane_b32 s69, v52, s44\n"
"v_exp_f32 v161, v161\n"
"v_exp_f32 v162, v162\n"
"v_exp_f32 v163, v163\n"
"ds_read_b64_tr_b16 a[18:19], v36 offset:6144\n"
"v_exp_f32 v192, v192\n"
"v_exp_f32 v193, v193\n"
"v_exp_f32 v194, v194\n"
"s_add_u32 s44, s44, 1\n"
"v_exp_f32 v195, v195\n"
"v_add_f32 v160, 1.0, v160\n"
"v_add_f32 v161, 1.0, v161\n"
"ds_read_b128 a[0:3], v40 offset:32768\n"
"v_add_f32 v162, 1.0, v162\n"
"v_add_f32 v163, 1.0, v163\n"
"v_add_f32 v192, 1.0, v192\n"
"s_add_u32 m0, s80, 0\n"
"v_add_f32 v193, 1.0, v193\n"
"v_add_f32 v194, 1.0, v194\n"
"v_add_f32 v195, 1.0, v195\n"
"ds_read_b128 a[4:7], v41 offset:32768\n"
"v_rcp_f32 v160, v160\n"
"v_rcp_f32 v161, v161\n"
"v_rcp_f32 v162, v162\n"
"buffer_load_dwordx4 v44, s[24:27], s69 offen offset:0 lds\n"
"v_rcp_f32 v163, v163\n"
"v_rcp_f32 v192, v192\n"
"v_rcp_f32 v193, v193\n"
"ds_read_b128 a[8:11], v42 offset:32768\n"
"v_rcp_f32 v194, v194\n"
"v_rcp_f32 v195, v195\n"
"v_mul_f32 v160, v176, v160\n"
"ds_read_b128 a[12:15], v43 offset:32768\n"
"v_mul_f32 v161, v177, v161\n"
"v_mul_f32 v162, v178, v162\n"
"v_mul_f32 v163, v179, v163\n"
"buffer_load_dwordx4 v45, s[24:27], s69 offen offset:1024 lds\n"
"v_mul_f32 v192, v208, v192\n"
"v_mul_f32 v193, v209, v193\n"
"v_mul_f32 v194, v210, v194\n"
"ds_read_b64_tr_b16 a[20:21], v37 offset:0\n"
"v_mul_f32 v195, v211, v195\n"
"v_cvt_pk_bf16_f32 v176, v160, v161\n"
"v_cvt_pk_bf16_f32 v177, v162, v163\n"
"buffer_load_dwordx4 v46, s[24:27], s69 offen offset:2048 lds\n"
"v_cvt_pk_bf16_f32 v178, v192, v193\n"
"v_cvt_pk_bf16_f32 v179, v194, v195\n"
"buffer_store_dwordx4 v[176:179], v24, s[36:39], s63 offen sc1\n"
"ds_read_b64_tr_b16 a[22:23], v37 offset:6144\n"
"v_mul_f32 v164, s65, v164\n"
"v_mul_f32 v165, s65, v165\n"
"v_mul_f32 v166, s65, v166\n"
"s_add_u32 m0, s48, 0\n"
"v_mul_f32 v167, s65, v167\n"
"v_mul_f32 v196, s65, v196\n"
"v_mul_f32 v197, s65, v197\n"
"ds_read_b64_tr_b16 a[24:25], v38 offset:0\n"
"v_mul_f32 v198, s65, v198\n"
"v_mul_f32 v199, s65, v199\n"
"v_exp_f32 v164, v164\n"
"buffer_load_dwordx4 v50, s[28:31], s68 offen offset:0 sc1 lds\n"
"v_exp_f32 v165, v165\n"
"v_exp_f32 v166, v166\n"
"v_exp_f32 v167, v167\n"
"ds_read_b64_tr_b16 a[26:27], v38 offset:6144\n"
"v_exp_f32 v196, v196\n"
"v_exp_f32 v197, v197\n"
"v_exp_f32 v198, v198\n"
"ds_read_b64_tr_b16 a[28:29], v39 offset:0\n"
"v_exp_f32 v199, v199\n"
"v_add_f32 v164, 1.0, v164\n"
"v_add_f32 v165, 1.0, v165\n"
"buffer_load_dwordx4 v50, s[28:31], s68 offen offset:1024 sc1 lds\n"
"v_add_f32 v166, 1.0, v166\n"
"v_add_f32 v167, 1.0, v167\n"
"v_add_f32 v196, 1.0, v196\n"
"ds_read_b64_tr_b16 a[30:31], v39 offset:6144\n"
"v_add_f32 v197, 1.0, v197\n"
"v_add_f32 v198, 1.0, v198\n"
"v_add_f32 v199, 1.0, v199\n"
"buffer_load_dwordx4 v50, s[28:31], s68 offen offset:2048 sc1 lds\n"
"v_rcp_f32 v164, v164\n"
"v_rcp_f32 v165, v165\n"
"v_rcp_f32 v166, v166\n"
"ds_read_b64_tr_b16 a[32:33], v36 offset:128\n"
"v_rcp_f32 v167, v167\n"
"v_rcp_f32 v196, v196\n"
"v_rcp_f32 v197, v197\n"
"buffer_load_dwordx4 v50, s[28:31], s68 offen offset:3072 sc1 lds\n"
"v_rcp_f32 v198, v198\n"
"v_rcp_f32 v199, v199\n"
"v_mul_f32 v164, v180, v164\n"
"ds_read_b64_tr_b16 a[34:35], v36 offset:6272\n"
"v_mul_f32 v165, v181, v165\n"
"v_mul_f32 v166, v182, v166\n"
"v_mul_f32 v167, v183, v167\n"
"s_add_u32 m0, s80, 3072\n"
"v_mul_f32 v196, v212, v196\n"
"v_mul_f32 v197, v213, v197\n"
"v_mul_f32 v198, v214, v198\n"
"ds_read_b64_tr_b16 a[36:37], v37 offset:128\n"
"v_mul_f32 v199, v215, v199\n"
"v_cvt_pk_bf16_f32 v180, v164, v165\n"
"v_cvt_pk_bf16_f32 v181, v166, v167\n"
"ds_read_b64_tr_b16 a[38:39], v37 offset:6272\n"
"v_cvt_pk_bf16_f32 v182, v196, v197\n"
"v_cvt_pk_bf16_f32 v183, v198, v199\n"
"buffer_store_dwordx4 v[180:183], v25, s[36:39], s63 offen sc1\n"
"buffer_load_dwordx4 v47, s[24:27], s69 offen offset:0 lds\n"
"v_mul_f32 v168, s65, v168\n"
"v_mul_f32 v169, s65, v169\n"
"v_mul_f32 v170, s65, v170\n"
"ds_read_b64_tr_b16 a[56:57], v36 offset:12288\n"
"v_mul_f32 v171, s65, v171\n"
"v_mul_f32 v200, s65, v200\n"
"v_mul_f32 v201, s65, v201\n"
"buffer_load_dwordx4 v48, s[24:27], s69 offen offset:1024 lds\n"
"v_mul_f32 v202, s65, v202\n"
"v_mul_f32 v203, s65, v203\n"
"v_exp_f32 v168, v168\n"
"ds_read_b64_tr_b16 a[58:59], v36 offset:18432\n"
"v_exp_f32 v169, v169\n"
"v_exp_f32 v170, v170\n"
"v_exp_f32 v171, v171\n"
"buffer_load_dwordx4 v49, s[24:27], s69 offen offset:2048 lds\n"
"v_exp_f32 v200, v200\n"
"v_exp_f32 v201, v201\n"
"v_exp_f32 v202, v202\n"
"ds_read_b128 a[40:43], v40 offset:33792\n"
"v_exp_f32 v203, v203\n"
"v_add_f32 v168, 1.0, v168\n"
"v_add_f32 v169, 1.0, v169\n"
"v_readlane_b32 s72, v51, s44\n"
"v_add_f32 v170, 1.0, v170\n"
"v_add_f32 v171, 1.0, v171\n"
"v_add_f32 v200, 1.0, v200\n"
"ds_read_b128 a[44:47], v41 offset:33792\n"
"v_add_f32 v201, 1.0, v201\n"
"v_add_f32 v202, 1.0, v202\n"
"v_add_f32 v203, 1.0, v203\n"
"v_readlane_b32 s73, v52, s44\n"
"v_rcp_f32 v168, v168\n"
"v_rcp_f32 v169, v169\n"
"v_rcp_f32 v170, v170\n"
"ds_read_b128 a[48:51], v42 offset:33792\n"
"v_rcp_f32 v171, v171\n"
"v_rcp_f32 v200, v200\n"
"v_rcp_f32 v201, v201\n"
"ds_read_b128 a[52:55], v43 offset:33792\n"
"v_rcp_f32 v202, v202\n"
"v_rcp_f32 v203, v203\n"
"v_mul_f32 v168, v184, v168\n"
"s_add_u32 s44, s44, 1\n"
"v_mul_f32 v169, v185, v169\n"
"v_mul_f32 v170, v186, v170\n"
"v_mul_f32 v171, v187, v171\n"
"ds_read_b64_tr_b16 a[60:61], v37 offset:12288\n"
"v_mul_f32 v200, v216, v200\n"
"v_mul_f32 v201, v217, v201\n"
"v_mul_f32 v202, v218, v202\n"
"s_add_u32 m0, s80, 24576\n"
"v_mul_f32 v203, v219, v203\n"
"v_cvt_pk_bf16_f32 v184, v168, v169\n"
"v_cvt_pk_bf16_f32 v185, v170, v171\n"
"ds_read_b64_tr_b16 a[62:63], v37 offset:18432\n"
"v_cvt_pk_bf16_f32 v186, v200, v201\n"
"v_cvt_pk_bf16_f32 v187, v202, v203\n"
"buffer_store_dwordx4 v[184:187], v26, s[36:39], s63 offen sc1\n"
"buffer_load_dwordx4 v44, s[24:27], s73 offen offset:0 lds\n"
"v_mul_f32 v172, s65, v172\n"
"v_mul_f32 v173, s65, v173\n"
"v_mul_f32 v174, s65, v174\n"
"ds_read_b64_tr_b16 a[64:65], v38 offset:12288\n"
"v_mul_f32 v175, s65, v175\n"
"v_mul_f32 v204, s65, v204\n"
"v_mul_f32 v205, s65, v205\n"
"buffer_load_dwordx4 v45, s[24:27], s73 offen offset:1024 lds\n"
"v_mul_f32 v206, s65, v206\n"
"v_mul_f32 v207, s65, v207\n"
"v_exp_f32 v172, v172\n"
"ds_read_b64_tr_b16 a[66:67], v38 offset:18432\n"
"v_exp_f32 v173, v173\n"
"v_exp_f32 v174, v174\n"
"v_exp_f32 v175, v175\n"
"ds_read_b64_tr_b16 a[68:69], v39 offset:12288\n"
"v_exp_f32 v204, v204\n"
"v_exp_f32 v205, v205\n"
"v_exp_f32 v206, v206\n"
"buffer_load_dwordx4 v46, s[24:27], s73 offen offset:2048 lds\n"
"v_exp_f32 v207, v207\n"
"v_add_f32 v172, 1.0, v172\n"
"v_add_f32 v173, 1.0, v173\n"
"ds_read_b64_tr_b16 a[70:71], v39 offset:18432\n"
"v_add_f32 v174, 1.0, v174\n"
"v_add_f32 v175, 1.0, v175\n"
"v_add_f32 v204, 1.0, v204\n"
"s_add_u32 m0, s48, 16384\n"
"v_add_f32 v205, 1.0, v205\n"
"v_add_f32 v206, 1.0, v206\n"
"v_add_f32 v207, 1.0, v207\n"
"ds_read_b64_tr_b16 a[72:73], v36 offset:12416\n"
"v_rcp_f32 v172, v172\n"
"v_rcp_f32 v173, v173\n"
"v_rcp_f32 v174, v174\n"
"buffer_load_dwordx4 v50, s[28:31], s72 offen offset:0 sc1 lds\n"
"v_rcp_f32 v175, v175\n"
"v_rcp_f32 v204, v204\n"
"v_rcp_f32 v205, v205\n"
"ds_read_b64_tr_b16 a[74:75], v36 offset:18560\n"
"v_rcp_f32 v206, v206\n"
"v_rcp_f32 v207, v207\n"
"v_mul_f32 v172, v188, v172\n"
"buffer_load_dwordx4 v50, s[28:31], s72 offen offset:1024 sc1 lds\n"
"v_mul_f32 v173, v189, v173\n"
"v_mul_f32 v174, v190, v174\n"
"v_mul_f32 v175, v191, v175\n"
"ds_read_b64_tr_b16 a[76:77], v37 offset:12416\n"
"v_mul_f32 v204, v220, v204\n"
"v_mul_f32 v205, v221, v205\n"
"v_mul_f32 v206, v222, v206\n"
"ds_read_b64_tr_b16 a[78:79], v37 offset:18560\n"
"v_mul_f32 v207, v223, v207\n"
"v_cvt_pk_bf16_f32 v188, v172, v173\n"
"v_cvt_pk_bf16_f32 v189, v174, v175\n"
"v_cvt_pk_bf16_f32 v190, v204, v205\n"
"v_cvt_pk_bf16_f32 v191, v206, v207\n"
"buffer_store_dwordx4 v[188:191], v27, s[36:39], s63 offen sc1\n"
"s_branch Lfe%=\n"
"LgA%=:\n"
"v_mul_f32 v96, s65, v96\n"
"v_mul_f32 v97, s65, v97\n"
"v_mul_f32 v98, s65, v98\n"
"v_readlane_b32 s68, v51, s44\n"
"v_mul_f32 v99, s65, v99\n"
"v_mul_f32 v128, s65, v128\n"
"v_mul_f32 v129, s65, v129\n"
"ds_read_b64_tr_b16 a[16:17], v36 offset:0\n"
"v_mul_f32 v130, s65, v130\n"
"v_mul_f32 v131, s65, v131\n"
"v_exp_f32 v96, v96\n"
"v_readlane_b32 s69, v52, s44\n"
"v_exp_f32 v97, v97\n"
"v_exp_f32 v98, v98\n"
"v_exp_f32 v99, v99\n"
"ds_read_b64_tr_b16 a[18:19], v36 offset:6144\n"
"v_exp_f32 v128, v128\n"
"v_exp_f32 v129, v129\n"
"v_exp_f32 v130, v130\n"
"s_add_u32 s44, s44, 1\n"
"v_exp_f32 v131, v131\n"
"v_add_f32 v96, 1.0, v96\n"
"v_add_f32 v97, 1.0, v97\n"
"ds_read_b128 a[0:3], v40 offset:32768\n"
"v_add_f32 v98, 1.0, v98\n"
"v_add_f32 v99, 1.0, v99\n"
"v_add_f32 v128, 1.0, v128\n"
"s_add_u32 m0, s80, 0\n"
"v_add_f32 v129, 1.0, v129\n"
"v_add_f32 v130, 1.0, v130\n"
"v_add_f32 v131, 1.0, v131\n"
"ds_read_b128 a[4:7], v41 offset:32768\n"
"v_rcp_f32 v96, v96\n"
"v_rcp_f32 v97, v97\n"
"v_rcp_f32 v98, v98\n"
"buffer_load_dwordx4 v44, s[24:27], s69 offen offset:0 lds\n"
"v_rcp_f32 v99, v99\n"
"v_rcp_f32 v128, v128\n"
"v_rcp_f32 v129, v129\n"
"ds_read_b128 a[8:11], v42 offset:32768\n"
"v_rcp_f32 v130, v130\n"
"v_rcp_f32 v131, v131\n"
"v_mul_f32 v96, v112, v96\n"
"ds_read_b128 a[12:15], v43 offset:32768\n"
"v_mul_f32 v97, v113, v97\n"
"v_mul_f32 v98, v114, v98\n"
"v_mul_f32 v99, v115, v99\n"
"buffer_load_dwordx4 v45, s[24:27], s69 offen offset:1024 lds\n"
"v_mul_f32 v128, v144, v128\n"
"v_mul_f32 v129, v145, v129\n"
"v_mul_f32 v130, v146, v130\n"
"ds_read_b64_tr_b16 a[20:21], v37 offset:0\n"
"v_mul_f32 v131, v147, v131\n"
"v_cvt_pk_bf16_f32 v112, v96, v97\n"
"v_cvt_pk_bf16_f32 v113, v98, v99\n"
"buffer_load_dwordx4 v46, s[24:27], s69 offen offset:2048 lds\n"
"v_cvt_pk_bf16_f32 v114, v128, v129\n"
"v_cvt_pk_bf16_f32 v115, v130, v131\n"
"buffer_store_dwordx4 v[112:115], v24, s[36:39], s63 offen sc1\n"
"ds_read_b64_tr_b16 a[22:23], v37 offset:6144\n"
"v_mul_f32 v100, s65, v100\n"
"v_mul_f32 v101, s65, v101\n"
"v_mul_f32 v102, s65, v102\n"
"s_add_u32 m0, s48, 0\n"
"v_mul_f32 v103, s65, v103\n"
"v_mul_f32 v132, s65, v132\n"
"v_mul_f32 v133, s65, v133\n"
"ds_read_b64_tr_b16 a[24:25], v38 offset:0\n"
"v_mul_f32 v134, s65, v134\n"
"v_mul_f32 v135, s65, v135\n"
"v_exp_f32 v100, v100\n"
"buffer_load_dwordx4 v50, s[28:31], s68 offen offset:0 sc1 lds\n"
"v_exp_f32 v101, v101\n"
"v_exp_f32 v102, v102\n"
"v_exp_f32 v103, v103\n"
"ds_read_b64_tr_b16 a[26:27], v38 offset:6144\n"
"v_exp_f32 v132, v132\n"
"v_exp_f32 v133, v133\n"
"v_exp_f32 v134, v134\n"
"ds_read_b64_tr_b16 a[28:29], v39 offset:0\n"
"v_exp_f32 v135, v135\n"
"v_add_f32 v100, 1.0, v100\n"
"v_add_f32 v101, 1.0, v101\n"
"buffer_load_dwordx4 v50, s[28:31], s68 offen offset:1024 sc1 lds\n"
"v_add_f32 v102, 1.0, v102\n"
"v_add_f32 v103, 1.0, v103\n"
"v_add_f32 v132, 1.0, v132\n"
"ds_read_b64_tr_b16 a[30:31], v39 offset:6144\n"
"v_add_f32 v133, 1.0, v133\n"
"v_add_f32 v134, 1.0, v134\n"
"v_add_f32 v135, 1.0, v135\n"
"buffer_load_dwordx4 v50, s[28:31], s68 offen offset:2048 sc1 lds\n"
"v_rcp_f32 v100, v100\n"
"v_rcp_f32 v101, v101\n"
"v_rcp_f32 v102, v102\n"
"ds_read_b64_tr_b16 a[32:33], v36 offset:128\n"
"v_rcp_f32 v103, v103\n"
"v_rcp_f32 v132, v132\n"
"v_rcp_f32 v133, v133\n"
"buffer_load_dwordx4 v50, s[28:31], s68 offen offset:3072 sc1 lds\n"
"v_rcp_f32 v134, v134\n"
"v_rcp_f32 v135, v135\n"
"v_mul_f32 v100, v116, v100\n"
"ds_read_b64_tr_b16 a[34:35], v36 offset:6272\n"
"v_mul_f32 v101, v117, v101\n"
"v_mul_f32 v102, v118, v102\n"
"v_mul_f32 v103, v119, v103\n"
"s_add_u32 m0, s80, 3072\n"
"v_mul_f32 v132, v148, v132\n"
"v_mul_f32 v133, v149, v133\n"
"v_mul_f32 v134, v150, v134\n"
"ds_read_b64_tr_b16 a[36:37], v37 offset:128\n"
"v_mul_f32 v135, v151, v135\n"
"v_cvt_pk_bf16_f32 v116, v100, v101\n"
"v_cvt_pk_bf16_f32 v117, v102, v103\n"
"ds_read_b64_tr_b16 a[38:39], v37 offset:6272\n"
"v_cvt_pk_bf16_f32 v118, v132, v133\n"
"v_cvt_pk_bf16_f32 v119, v134, v135\n"
"buffer_store_dwordx4 v[116:119], v25, s[36:39], s63 offen sc1\n"
"buffer_load_dwordx4 v47, s[24:27], s69 offen offset:0 lds\n"
"v_mul_f32 v104, s65, v104\n"
"v_mul_f32 v105, s65, v105\n"
"v_mul_f32 v106, s65, v106\n"
"ds_read_b64_tr_b16 a[56:57], v36 offset:12288\n"
"v_mul_f32 v107, s65, v107\n"
"v_mul_f32 v136, s65, v136\n"
"v_mul_f32 v137, s65, v137\n"
"buffer_load_dwordx4 v48, s[24:27], s69 offen offset:1024 lds\n"
"v_mul_f32 v138, s65, v138\n"
"v_mul_f32 v139, s65, v139\n"
"v_exp_f32 v104, v104\n"
"ds_read_b64_tr_b16 a[58:59], v36 offset:18432\n"
"v_exp_f32 v105, v105\n"
"v_exp_f32 v106, v106\n"
"v_exp_f32 v107, v107\n"
"buffer_load_dwordx4 v49, s[24:27], s69 offen offset:2048 lds\n"
"v_exp_f32 v136, v136\n"
"v_exp_f32 v137, v137\n"
"v_exp_f32 v138, v138\n"
"ds_read_b128 a[40:43], v40 offset:33792\n"
"v_exp_f32 v139, v139\n"
"v_add_f32 v104, 1.0, v104\n"
"v_add_f32 v105, 1.0, v105\n"
"v_readlane_b32 s72, v51, s44\n"
"v_add_f32 v106, 1.0, v106\n"
"v_add_f32 v107, 1.0, v107\n"
"v_add_f32 v136, 1.0, v136\n"
"ds_read_b128 a[44:47], v41 offset:33792\n"
"v_add_f32 v137, 1.0, v137\n"
"v_add_f32 v138, 1.0, v138\n"
"v_add_f32 v139, 1.0, v139\n"
"v_readlane_b32 s73, v52, s44\n"
"v_rcp_f32 v104, v104\n"
"v_rcp_f32 v105, v105\n"
"v_rcp_f32 v106, v106\n"
"ds_read_b128 a[48:51], v42 offset:33792\n"
"v_rcp_f32 v107, v107\n"
"v_rcp_f32 v136, v136\n"
"v_rcp_f32 v137, v137\n"
"ds_read_b128 a[52:55], v43 offset:33792\n"
"v_rcp_f32 v138, v138\n"
"v_rcp_f32 v139, v139\n"
"v_mul_f32 v104, v120, v104\n"
"s_add_u32 s44, s44, 1\n"
"v_mul_f32 v105, v121, v105\n"
"v_mul_f32 v106, v122, v106\n"
"v_mul_f32 v107, v123, v107\n"
"ds_read_b64_tr_b16 a[60:61], v37 offset:12288\n"
"v_mul_f32 v136, v152, v136\n"
"v_mul_f32 v137, v153, v137\n"
"v_mul_f32 v138, v154, v138\n"
"s_add_u32 m0, s80, 24576\n"
"v_mul_f32 v139, v155, v139\n"
"v_cvt_pk_bf16_f32 v120, v104, v105\n"
"v_cvt_pk_bf16_f32 v121, v106, v107\n"
"ds_read_b64_tr_b16 a[62:63], v37 offset:18432\n"
"v_cvt_pk_bf16_f32 v122, v136, v137\n"
"v_cvt_pk_bf16_f32 v123, v138, v139\n"
"buffer_store_dwordx4 v[120:123], v26, s[36:39], s63 offen sc1\n"
"buffer_load_dwordx4 v44, s[24:27], s73 offen offset:0 lds\n"
"v_mul_f32 v108, s65, v108\n"
"v_mul_f32 v109, s65, v109\n"
"v_mul_f32 v110, s65, v110\n"
"ds_read_b64_tr_b16 a[64:65], v38 offset:12288\n"
"v_mul_f32 v111, s65, v111\n"
"v_mul_f32 v140, s65, v140\n"
"v_mul_f32 v141, s65, v141\n"
"buffer_load_dwordx4 v45, s[24:27], s73 offen offset:1024 lds\n"
"v_mul_f32 v142, s65, v142\n"
"v_mul_f32 v143, s65, v143\n"
"v_exp_f32 v108, v108\n"
"ds_read_b64_tr_b16 a[66:67], v38 offset:18432\n"
"v_exp_f32 v109, v109\n"
"v_exp_f32 v110, v110\n"
"v_exp_f32 v111, v111\n"
"ds_read_b64_tr_b16 a[68:69], v39 offset:12288\n"
"v_exp_f32 v140, v140\n"
"v_exp_f32 v141, v141\n"
"v_exp_f32 v142, v142\n"
"buffer_load_dwordx4 v46, s[24:27], s73 offen offset:2048 lds\n"
"v_exp_f32 v143, v143\n"
"v_add_f32 v108, 1.0, v108\n"
"v_add_f32 v109, 1.0, v109\n"
"ds_read_b64_tr_b16 a[70:71], v39 offset:18432\n"
"v_add_f32 v110, 1.0, v110\n"
"v_add_f32 v111, 1.0, v111\n"
"v_add_f32 v140, 1.0, v140\n"
"s_add_u32 m0, s48, 16384\n"
"v_add_f32 v141, 1.0, v141\n"
"v_add_f32 v142, 1.0, v142\n"
"v_add_f32 v143, 1.0, v143\n"
"ds_read_b64_tr_b16 a[72:73], v36 offset:12416\n"
"v_rcp_f32 v108, v108\n"
"v_rcp_f32 v109, v109\n"
"v_rcp_f32 v110, v110\n"
"buffer_load_dwordx4 v50, s[28:31], s72 offen offset:0 sc1 lds\n"
"v_rcp_f32 v111, v111\n"
"v_rcp_f32 v140, v140\n"
"v_rcp_f32 v141, v141\n"
"ds_read_b64_tr_b16 a[74:75], v36 offset:18560\n"
"v_rcp_f32 v142, v142\n"
"v_rcp_f32 v143, v143\n"
"v_mul_f32 v108, v124, v108\n"
"buffer_load_dwordx4 v50, s[28:31], s72 offen offset:1024 sc1 lds\n"
"v_mul_f32 v109, v125, v109\n"
"v_mul_f32 v110, v126, v110\n"
"v_mul_f32 v111, v127, v111\n"
"ds_read_b64_tr_b16 a[76:77], v37 offset:12416\n"
"v_mul_f32 v140, v156, v140\n"
"v_mul_f32 v141, v157, v141\n"
"v_mul_f32 v142, v158, v142\n"
"ds_read_b64_tr_b16 a[78:79], v37 offset:18560\n"
"v_mul_f32 v143, v159, v143\n"
"v_cvt_pk_bf16_f32 v124, v108, v109\n"
"v_cvt_pk_bf16_f32 v125, v110, v111\n"
"v_cvt_pk_bf16_f32 v126, v140, v141\n"
"v_cvt_pk_bf16_f32 v127, v142, v143\n"
"buffer_store_dwordx4 v[124:127], v27, s[36:39], s63 offen sc1\n"
"Lfe%=:\n"
"v_mul_f32 v28, 0xbfb8aa3b, v30\n"
"v_exp_f32 v28, v28\n"
"s_nop 0\n"
"v_add_f32 v28, 1.0, v28\n"
"v_rcp_f32 v28, v28\n"
"s_nop 0\n"
"v_sub_f32 v29, 1.0, v28\n"
"s_waitcnt vmcnt(20)\n"
"v_cmp_eq_u64 vcc, s[82:83], v[54:55]\n"
"s_lshr_b64 s[56:57], vcc, s44\n"
"s_and_b32 s56, s56, 7\n"
"s_cmp_eq_u32 s56, 7\n"
"s_cbranch_scc0 Lcp2a_slow%=\n"
"Lcp2a_ok%=:\n"
"v_readlane_b32 s46, v51, s44\n"
"v_readlane_b32 s47, v52, s44\n"
"s_add_u32 s44, s44, 1\n"
"s_waitcnt vmcnt(7) lgkmcnt(0)\n"
"s_barrier\n"
"v_mfma_f32_16x16x32_bf16 v[96:99], a[16:19], a[0:3], 0\n"
"ds_read_b64_tr_b16 a[96:97], v32 offset:0\n"
"ds_read_b64_tr_b16 a[98:99], v32 offset:6144\n"
"s_add_u32 m0, s48, 16384\n"
"v_mfma_f32_16x16x32_bf16 v[96:99], a[56:59], a[40:43], v[96:99]\n"
"ds_read_b128 a[80:83], v40 offset:0\n"
"buffer_load_dwordx4 v50, s[28:31], s72 offen offset:2048 sc1 lds\n"
"v_mfma_f32_16x16x32_bf16 v[120:123], a[56:59], a[44:47], 0\n"
"ds_read_b128 a[84:87], v41 offset:0\n"
"s_and_b32 s54, s19, 1\n"
"v_mfma_f32_16x16x32_bf16 v[120:123], a[16:19], a[4:7], v[120:123]\n"
"buffer_load_dwordx4 v50, s[28:31], s72 offen offset:3072 sc1 lds\n"
"s_lshr_b32 s53, s19, 1\n"
"v_mfma_f32_16x16x32_bf16 v[144:147], a[16:19], a[8:11], 0\n"
"ds_read_b128 a[88:91], v42 offset:0\n"
"s_mul_i32 s51, s66, 3\n"
"v_mfma_f32_16x16x32_bf16 v[144:147], a[56:59], a[48:51], v[144:147]\n"
"ds_read_b128 a[92:95], v43 offset:0\n"
"s_add_u32 s51, s51, s67\n"
"v_mfma_f32_16x16x32_bf16 v[168:171], a[56:59], a[52:55], 0\n"
"ds_read_b64_tr_b16 a[100:101], v33 offset:0\n"
"s_mul_i32 s65, s51, 147456\n"
"v_mfma_f32_16x16x32_bf16 v[168:171], a[16:19], a[12:15], v[168:171]\n"
"ds_read_b64_tr_b16 a[102:103], v33 offset:6144\n"
"s_add_u32 m0, s80, 27648\n"
"v_mfma_f32_16x16x32_bf16 v[172:175], a[20:23], a[12:15], 0\n"
"buffer_load_dwordx4 v47, s[24:27], s73 offen offset:0 lds\n"
"s_mul_i32 s45, s19, 3072\n"
"v_mfma_f32_16x16x32_bf16 v[172:175], a[60:63], a[52:55], v[172:175]\n"
"ds_read_b64_tr_b16 a[104:105], v34 offset:0\n"
"s_add_u32 s65, s65, s45\n"
"v_mfma_f32_16x16x32_bf16 v[148:151], a[60:63], a[48:51], 0\n"
"ds_read_b64_tr_b16 a[106:107], v34 offset:6144\n"
"s_add_u32 s65, s65, 25952256\n"
"v_mfma_f32_16x16x32_bf16 v[148:151], a[20:23], a[8:11], v[148:151]\n"
"ds_read_b64_tr_b16 a[108:109], v35 offset:0\n"
"buffer_load_dwordx4 v48, s[24:27], s73 offen offset:1024 lds\n"
"v_mfma_f32_16x16x32_bf16 v[124:127], a[20:23], a[4:7], 0\n"
"ds_read_b64_tr_b16 a[110:111], v35 offset:6144\n"
"s_add_u32 s64, s45, 122880\n"
"v_mfma_f32_16x16x32_bf16 v[124:127], a[60:63], a[44:47], v[124:127]\n"
"buffer_load_dwordx4 v49, s[24:27], s73 offen offset:2048 lds\n"
"s_add_u32 s45, s34, 1\n"
"v_mfma_f32_16x16x32_bf16 v[100:103], a[60:63], a[40:43], 0\n"
"ds_read_b64_tr_b16 a[112:113], v32 offset:128\n"
"s_add_u32 m0, s80, 49152\n"
"v_mfma_f32_16x16x32_bf16 v[100:103], a[20:23], a[0:3], v[100:103]\n"
"ds_read_b64_tr_b16 a[114:115], v32 offset:6272\n"
"buffer_load_dwordx4 v44, s[24:27], s47 offen offset:0 lds\n"
"v_mfma_f32_16x16x32_bf16 v[104:107], a[24:27], a[0:3], 0\n"
"ds_read_b64_tr_b16 a[116:117], v33 offset:128\n"
"s_and_b32 s45, s45, 3\n"
"v_mfma_f32_16x16x32_bf16 v[104:107], a[64:67], a[40:43], v[104:107]\n"
"ds_read_b64_tr_b16 a[118:119], v33 offset:6272\n"
"s_mul_i32 s45, s45, 36864\n"
"v_mfma_f32_16x16x32_bf16 v[128:131], a[64:67], a[44:47], 0\n"
"buffer_load_dwordx4 v45, s[24:27], s47 offen offset:1024 lds\n"
"v_mfma_f32_16x16x32_bf16 v[128:131], a[24:27], a[4:7], v[128:131]\n"
"buffer_load_dwordx4 v46, s[24:27], s47 offen offset:2048 lds\n"
"s_add_u32 m0, s48, 32768\n"
"v_mfma_f32_16x16x32_bf16 v[152:155], a[24:27], a[8:11], 0\n"
"buffer_load_dwordx4 v50, s[28:31], s46 offen offset:0 sc1 lds\n"
"v_mfma_f32_16x16x32_bf16 v[152:155], a[64:67], a[48:51], v[152:155]\n"
"s_add_u32 s58, s45, s65\n"
"s_add_u32 s58, s58, 0\n"
"v_mfma_f32_16x16x32_bf16 v[176:179], a[64:67], a[52:55], 0\n"
"buffer_load_dwordx4 v50, s[28:31], s46 offen offset:1024 sc1 lds\n"
"v_mfma_f32_16x16x32_bf16 v[176:179], a[24:27], a[12:15], v[176:179]\n"
"s_add_u32 s45, s34, 2\n"
"s_and_b32 s45, s45, 3\n"
"v_mfma_f32_16x16x32_bf16 v[180:183], a[28:31], a[12:15], 0\n"
"ds_read_b64_tr_b16 a[136:137], v32 offset:12288\n"
"ds_read_b64_tr_b16 a[138:139], v32 offset:18432\n"
"v_mfma_f32_16x16x32_bf16 v[180:183], a[68:71], a[52:55], v[180:183]\n"
"ds_read_b128 a[120:123], v40 offset:1024\n"
"s_mul_i32 s45, s45, 36864\n"
"v_mfma_f32_16x16x32_bf16 v[156:159], a[68:71], a[48:51], 0\n"
"ds_read_b128 a[124:127], v41 offset:1024\n"
"s_add_u32 s59, s45, s65\n"
"v_mfma_f32_16x16x32_bf16 v[156:159], a[28:31], a[8:11], v[156:159]\n"
"buffer_load_dwordx4 v50, s[28:31], s46 offen offset:2048 sc1 lds\n"
"v_mfma_f32_16x16x32_bf16 v[132:135], a[28:31], a[4:7], 0\n"
"ds_read_b128 a[128:131], v42 offset:1024\n"
"s_add_u32 s59, s59, 12288\n"
"v_mfma_f32_16x16x32_bf16 v[132:135], a[68:71], a[44:47], v[132:135]\n"
"ds_read_b128 a[132:135], v43 offset:1024\n"
"s_add_u32 s45, s34, 3\n"
"v_mfma_f32_16x16x32_bf16 v[108:111], a[68:71], a[40:43], 0\n"
"ds_read_b64_tr_b16 a[140:141], v33 offset:12288\n"
"s_and_b32 s45, s45, 3\n"
"v_mfma_f32_16x16x32_bf16 v[108:111], a[28:31], a[0:3], v[108:111]\n"
"ds_read_b64_tr_b16 a[142:143], v33 offset:18432\n"
"s_mul_i32 s45, s45, 36864\n"
"v_mfma_f32_16x16x32_bf16 v[112:115], a[32:35], a[0:3], 0\n"
"buffer_load_dwordx4 v50, s[28:31], s46 offen offset:3072 sc1 lds\n"
"v_mfma_f32_16x16x32_bf16 v[112:115], a[72:75], a[40:43], v[112:115]\n"
"ds_read_b64_tr_b16 a[144:145], v34 offset:12288\n"
"s_add_u32 s60, s45, s65\n"
"v_mfma_f32_16x16x32_bf16 v[136:139], a[72:75], a[44:47], 0\n"
"ds_read_b64_tr_b16 a[146:147], v34 offset:18432\n"
"s_add_u32 s60, s60, 24576\n"
"v_mfma_f32_16x16x32_bf16 v[136:139], a[32:35], a[4:7], v[136:139]\n"
"ds_read_b64_tr_b16 a[148:149], v35 offset:12288\n"
"s_mul_i32 s45, s34, 36864\n"
"v_mfma_f32_16x16x32_bf16 v[160:163], a[32:35], a[8:11], 0\n"
"ds_read_b64_tr_b16 a[150:151], v35 offset:18432\n"
"s_add_u32 m0, s80, 52224\n"
"v_mfma_f32_16x16x32_bf16 v[160:163], a[72:75], a[48:51], v[160:163]\n"
"buffer_load_dwordx4 v47, s[24:27], s47 offen offset:0 lds\n"
"v_mfma_f32_16x16x32_bf16 v[184:187], a[72:75], a[52:55], 0\n"
"ds_read_b64_tr_b16 a[152:153], v32 offset:12416\n"
"s_add_u32 s65, s65, s45\n"
"v_mfma_f32_16x16x32_bf16 v[184:187], a[32:35], a[12:15], v[184:187]\n"
"ds_read_b64_tr_b16 a[154:155], v32 offset:18560\n"
"s_lshl_b32 s63, s66, 7\n"
"v_mfma_f32_16x16x32_bf16 v[188:191], a[36:39], a[12:15], 0\n"
"ds_read_b64_tr_b16 a[156:157], v33 offset:12416\n"
"s_lshl_b32 s45, s54, 4\n"
"v_mfma_f32_16x16x32_bf16 v[188:191], a[76:79], a[52:55], v[188:191]\n"
"ds_read_b64_tr_b16 a[158:159], v33 offset:18560\n"
"s_add_u32 s63, s63, s45\n"
"v_mfma_f32_16x16x32_bf16 v[164:167], a[76:79], a[48:51], 0\n"
"buffer_load_dwordx4 v48, s[24:27], s47 offen offset:1024 lds\n"
"v_mfma_f32_16x16x32_bf16 v[164:167], a[36:39], a[8:11], v[164:167]\n"
"v_readlane_b32 s68, v51, s44\n"
"s_lshl_b32 s45, s34, 5\n"
"v_mfma_f32_16x16x32_bf16 v[140:143], a[36:39], a[4:7], 0\n"
"v_readlane_b32 s69, v52, s44\n"
"s_add_u32 s63, s63, s45\n"
"v_mfma_f32_16x16x32_bf16 v[140:143], a[76:79], a[44:47], v[140:143]\n"
"buffer_load_dwordx4 v49, s[24:27], s47 offen offset:2048 lds\n"
"v_mfma_f32_16x16x32_bf16 v[116:119], a[76:79], a[40:43], 0\n"
"s_add_u32 s44, s44, 1\n"
"s_mul_i32 s62, s67, 192\n"
"v_mfma_f32_16x16x32_bf16 v[116:119], a[36:39], a[0:3], v[116:119]\n"
"s_waitcnt vmcnt(10) lgkmcnt(0)\n"
"s_barrier\n"
"v_mfma_f32_16x16x32_bf16 v[96:99], a[96:99], a[80:83], v[96:99]\n"
"ds_read_b64_tr_b16 a[176:177], v32 offset:24576\n"
"ds_read_b64_tr_b16 a[178:179], v32 offset:30720\n"
"v_mfma_f32_16x16x32_bf16 v[96:99], a[136:139], a[120:123], v[96:99]\n"
"ds_read_b128 a[160:163], v40 offset:16384\n"
"s_mul_i32 s45, s53, 96\n"
"v_mfma_f32_16x16x32_bf16 v[120:123], a[136:139], a[124:127], v[120:123]\n"
"ds_read_b128 a[164:167], v41 offset:16384\n"
"s_add_u32 m0, s80, 0\n"
"v_mfma_f32_16x16x32_bf16 v[120:123], a[96:99], a[84:87], v[120:123]\n"
"buffer_load_dwordx4 v44, s[24:27], s69 offen offset:0 lds\n"
"v_mfma_f32_16x16x32_bf16 v[144:147], a[96:99], a[88:91], v[144:147]\n"
"ds_read_b128 a[168:171], v42 offset:16384\n"
"s_add_u32 s62, s62, s45\n"
"v_mfma_f32_16x16x32_bf16 v[144:147], a[136:139], a[128:131], v[144:147]\n"
"ds_read_b128 a[172:175], v43 offset:16384\n"
"s_mul_i32 s61, s63, 1152\n"
"v_mfma_f32_16x16x32_bf16 v[168:171], a[136:139], a[132:135], v[168:171]\n"
"ds_read_b64_tr_b16 a[180:181], v33 offset:24576\n"
"s_lshl_b32 s45, s62, 1\n"
"v_mfma_f32_16x16x32_bf16 v[168:171], a[96:99], a[92:95], v[168:171]\n"
"ds_read_b64_tr_b16 a[182:183], v33 offset:30720\n"
"s_add_u32 s61, s61, s45\n"
"v_mfma_f32_16x16x32_bf16 v[172:175], a[100:103], a[92:95], v[172:175]\n"
"buffer_load_dwordx4 v45, s[24:27], s69 offen offset:1024 lds\n"
"v_mfma_f32_16x16x32_bf16 v[172:175], a[140:143], a[132:135], v[172:175]\n"
"ds_read_b64_tr_b16 a[184:185], v34 offset:24576\n"
"s_lshr_b32 s63, s63, 6\n"
"v_mfma_f32_16x16x32_bf16 v[148:151], a[140:143], a[128:131], v[148:151]\n"
"ds_read_b64_tr_b16 a[186:187], v34 offset:30720\n"
"s_mul_i32 s63, s63, 73728\n"
"v_mfma_f32_16x16x32_bf16 v[148:151], a[100:103], a[88:91], v[148:151]\n"
"ds_read_b64_tr_b16 a[188:189], v35 offset:24576\n"
"s_mul_i32 s45, s67, 24576\n"
"v_mfma_f32_16x16x32_bf16 v[124:127], a[100:103], a[84:87], v[124:127]\n"
"ds_read_b64_tr_b16 a[190:191], v35 offset:30720\n"
"s_add_u32 s63, s63, s45\n"
"v_mfma_f32_16x16x32_bf16 v[124:127], a[140:143], a[124:127], v[124:127]\n"
"buffer_load_dwordx4 v46, s[24:27], s69 offen offset:2048 lds\n"
"v_mfma_f32_16x16x32_bf16 v[100:103], a[140:143], a[120:123], v[100:103]\n"
"ds_read_b64_tr_b16 a[192:193], v32 offset:24704\n"
"s_and_b32 s45, s34, 1\n"
"v_mfma_f32_16x16x32_bf16 v[100:103], a[100:103], a[80:83], v[100:103]\n"
"ds_read_b64_tr_b16 a[194:195], v32 offset:30848\n"
"s_lshl_b32 s45, s45, 6\n"
"v_mfma_f32_16x16x32_bf16 v[104:107], a[104:107], a[80:83], v[104:107]\n"
"ds_read_b64_tr_b16 a[196:197], v33 offset:24704\n"
"s_add_u32 s63, s63, s45\n"
"v_mfma_f32_16x16x32_bf16 v[104:107], a[144:147], a[120:123], v[104:107]\n"
"ds_read_b64_tr_b16 a[198:199], v33 offset:30848\n"
"s_add_u32 m0, s48, 0\n"
"v_mfma_f32_16x16x32_bf16 v[128:131], a[144:147], a[124:127], v[128:131]\n"
"buffer_load_dwordx4 v50, s[28:31], s68 offen offset:0 sc1 lds\n"
"v_mfma_f32_16x16x32_bf16 v[128:131], a[104:107], a[84:87], v[128:131]\n"
"v_lshrrev_b32 v13, 2, v0\n"
"v_lshlrev_b32 v13, 7, v13\n"
"v_mfma_f32_16x16x32_bf16 v[152:155], a[104:107], a[88:91], v[152:155]\n"
"buffer_store_dwordx2 v[62:63], v61, s[36:39], s76 offen sc1\n"
"v_and_b32 v4, 3, v0\n"
"v_mfma_f32_16x16x32_bf16 v[152:155], a[144:147], a[128:131], v[152:155]\n"
"buffer_load_dwordx4 v50, s[28:31], s68 offen offset:1024 sc1 lds\n"
"v_mfma_f32_16x16x32_bf16 v[176:179], a[144:147], a[132:135], v[176:179]\n"
"v_lshl_add_u32 v13, v4, 4, v13\n"
"s_add_u32 s63, s63, 23592960\n"
"v_mfma_f32_16x16x32_bf16 v[176:179], a[104:107], a[92:95], v[176:179]\n"
"s_lshl_b32 s55, s51, 9\n"
"s_lshl_b32 s45, s19, 5\n"
"v_mfma_f32_16x16x32_bf16 v[180:183], a[108:111], a[92:95], v[180:183]\n"
"ds_read_b64_tr_b16 a[216:217], v32 offset:36864\n"
"ds_read_b64_tr_b16 a[218:219], v32 offset:43008\n"
"v_mfma_f32_16x16x32_bf16 v[180:183], a[148:151], a[132:135], v[180:183]\n"
"ds_read_b128 a[200:203], v40 offset:17408\n"
"s_add_u32 s55, s55, s45\n"
"v_mfma_f32_16x16x32_bf16 v[156:159], a[148:151], a[128:131], v[156:159]\n"
"ds_read_b128 a[204:207], v41 offset:17408\n"
"s_add_u32 s55, s55, 33040384\n"
"v_mfma_f32_16x16x32_bf16 v[156:159], a[108:111], a[88:91], v[156:159]\n"
"buffer_load_dwordx4 v50, s[28:31], s68 offen offset:2048 sc1 lds\n"
"v_mfma_f32_16x16x32_bf16 v[132:135], a[108:111], a[84:87], v[132:135]\n"
"ds_read_b128 a[208:211], v42 offset:17408\n"
"v_and_b32 v12, 3, v0\n"
"v_mfma_f32_16x16x32_bf16 v[132:135], a[148:151], a[124:127], v[132:135]\n"
"ds_read_b128 a[212:215], v43 offset:17408\n"
"v_lshlrev_b32 v11, 3, v12\n"
"v_mfma_f32_16x16x32_bf16 v[108:111], a[148:151], a[120:123], v[108:111]\n"
"ds_read_b64_tr_b16 a[220:221], v33 offset:36864\n"
"s_add_u32 s45, s34, 1\n"
"v_mfma_f32_16x16x32_bf16 v[108:111], a[108:111], a[80:83], v[108:111]\n"
"ds_read_b64_tr_b16 a[222:223], v33 offset:43008\n"
"v_add_u32 v14, s45, v12\n"
"v_mfma_f32_16x16x32_bf16 v[112:115], a[112:115], a[80:83], v[112:115]\n"
"buffer_load_dwordx4 v50, s[28:31], s68 offen offset:3072 sc1 lds\n"
"v_mfma_f32_16x16x32_bf16 v[112:115], a[152:155], a[120:123], v[112:115]\n"
"ds_read_b64_tr_b16 a[224:225], v34 offset:36864\n"
"v_and_b32 v14, 3, v14\n"
"v_mfma_f32_16x16x32_bf16 v[136:139], a[152:155], a[124:127], v[136:139]\n"
"ds_read_b64_tr_b16 a[226:227], v34 offset:43008\n"
"v_lshl_add_u32 v14, v14, 7, v11\n"
"v_mfma_f32_16x16x32_bf16 v[136:139], a[112:115], a[84:87], v[136:139]\n"
"ds_read_b64_tr_b16 a[228:229], v35 offset:36864\n"
"s_lshl_b32 s45, s34, 7\n"
"v_mfma_f32_16x16x32_bf16 v[160:163], a[112:115], a[88:91], v[160:163]\n"
"ds_read_b64_tr_b16 a[230:231], v35 offset:43008\n"
"s_add_u32 m0, s80, 3072\n"
"v_mfma_f32_16x16x32_bf16 v[160:163], a[152:155], a[128:131], v[160:163]\n"
"buffer_load_dwordx4 v47, s[24:27], s69 offen offset:0 lds\n"
"v_mfma_f32_16x16x32_bf16 v[184:187], a[152:155], a[132:135], v[184:187]\n"
"ds_read_b64_tr_b16 a[232:233], v32 offset:36992\n"
"v_min_u32 v12, 16, v11\n"
"v_mfma_f32_16x16x32_bf16 v[184:187], a[112:115], a[92:95], v[184:187]\n"
"ds_read_b64_tr_b16 a[234:235], v32 offset:43136\n"
"v_add_u32 v12, s45, v12\n"
"v_mfma_f32_16x16x32_bf16 v[188:191], a[116:119], a[92:95], v[188:191]\n"
"ds_read_b64_tr_b16 a[236:237], v33 offset:36992\n"
"v_lshlrev_b32 v8, 4, v0\n"
"v_mfma_f32_16x16x32_bf16 v[188:191], a[156:159], a[132:135], v[188:191]\n"
"ds_read_b64_tr_b16 a[238:239], v33 offset:43136\n"
"v_and_b32 v9, 15, v0\n"
"v_mfma_f32_16x16x32_bf16 v[164:167], a[156:159], a[128:131], v[164:167]\n"
"buffer_load_dwordx4 v48, s[24:27], s69 offen offset:1024 lds\n"
"v_mfma_f32_16x16x32_bf16 v[164:167], a[116:119], a[88:91], v[164:167]\n"
"v_readlane_b32 s72, v51, s44\n"
"v_mul_u32_u24 v9, 1152, v9\n"
"v_mfma_f32_16x16x32_bf16 v[140:143], a[116:119], a[84:87], v[140:143]\n"
"v_readlane_b32 s73, v52, s44\n"
"v_bfe_u32 v4, v0, 4, 1\n"
"v_mfma_f32_16x16x32_bf16 v[140:143], a[156:159], a[124:127], v[140:143]\n"
"buffer_load_dwordx4 v49, s[24:27], s69 offen offset:2048 lds\n"
"v_mfma_f32_16x16x32_bf16 v[116:119], a[156:159], a[120:123], v[116:119]\n"
"s_add_u32 s44, s44, 1\n"
"v_lshl_add_u32 v9, v4, 5, v9\n"
"v_mfma_f32_16x16x32_bf16 v[116:119], a[116:119], a[80:83], v[116:119]\n"
"s_waitcnt vmcnt(11) lgkmcnt(0)\n"
"s_barrier\n"
"v_mfma_f32_16x16x32_bf16 v[96:99], a[176:179], a[160:163], v[96:99]\n"
"ds_read_b64_tr_b16 a[16:17], v36 offset:0\n"
"ds_read_b64_tr_b16 a[18:19], v36 offset:6144\n"
"v_mfma_f32_16x16x32_bf16 v[96:99], a[216:219], a[200:203], v[96:99]\n"
"ds_read_b128 a[0:3], v40 offset:32768\n"
"buffer_load_dwordx2 v[54:55], v53, s[36:39], 0 offen sc1\n"
"v_mfma_f32_16x16x32_bf16 v[120:123], a[216:219], a[204:207], v[120:123]\n"
"ds_read_b128 a[4:7], v41 offset:32768\n"
"s_add_u32 m0, s80, 24576\n"
"v_mfma_f32_16x16x32_bf16 v[120:123], a[176:179], a[164:167], v[120:123]\n"
"buffer_load_dwordx4 v44, s[24:27], s73 offen offset:0 lds\n"
"v_mfma_f32_16x16x32_bf16 v[144:147], a[176:179], a[168:171], v[144:147]\n"
"ds_read_b128 a[8:11], v42 offset:32768\n"
"v_lshrrev_b32 v4, 5, v0\n"
"v_mfma_f32_16x16x32_bf16 v[144:147], a[216:219], a[208:211], v[144:147]\n"
"ds_read_b128 a[12:15], v43 offset:32768\n"
"v_lshl_add_u32 v9, v4, 4, v9\n"
"v_mfma_f32_16x16x32_bf16 v[168:171], a[216:219], a[212:215], v[168:171]\n"
"ds_read_b64_tr_b16 a[20:21], v37 offset:0\n"
"v_lshrrev_b32 v4, 4, v0\n"
"v_mfma_f32_16x16x32_bf16 v[168:171], a[176:179], a[172:175], v[168:171]\n"
"ds_read_b64_tr_b16 a[22:23], v37 offset:6144\n"
"v_and_b32 v10, 15, v0\n"
"v_mfma_f32_16x16x32_bf16 v[172:175], a[180:183], a[172:175], v[172:175]\n"
"buffer_load_dwordx4 v45, s[24:27], s73 offen offset:1024 lds\n"
"v_mfma_f32_16x16x32_bf16 v[172:175], a[220:223], a[212:215], v[172:175]\n"
"ds_read_b64_tr_b16 a[24:25], v38 offset:0\n"
"v_mul_u32_u24 v10, 192, v10\n"
"v_mfma_f32_16x16x32_bf16 v[148:151], a[220:223], a[208:211], v[148:151]\n"
"ds_read_b64_tr_b16 a[26:27], v38 offset:6144\n"
"v_lshl_add_u32 v10, v4, 3, v10\n"
"v_mfma_f32_16x16x32_bf16 v[148:151], a[180:183], a[168:171], v[148:151]\n"
"ds_read_b64_tr_b16 a[28:29], v39 offset:0\n"
"s_add_u32 s45, s64, 24576\n"
"v_mfma_f32_16x16x32_bf16 v[124:127], a[180:183], a[164:167], v[124:127]\n"
"ds_read_b64_tr_b16 a[30:31], v39 offset:6144\n"
"v_add_u32 v10, s45, v10\n"
"v_mfma_f32_16x16x32_bf16 v[124:127], a[220:223], a[204:207], v[124:127]\n"
"buffer_load_dwordx4 v46, s[24:27], s73 offen offset:2048 lds\n"
"v_mfma_f32_16x16x32_bf16 v[100:103], a[220:223], a[200:203], v[100:103]\n"
"ds_read_b64_tr_b16 a[32:33], v36 offset:128\n"
"v_lshrrev_b32 v4, 2, v0\n"
"v_mfma_f32_16x16x32_bf16 v[100:103], a[180:183], a[160:163], v[100:103]\n"
"ds_read_b64_tr_b16 a[34:35], v36 offset:6272\n"
"v_lshl_add_u32 v11, v4, 6, v11\n"
"v_mfma_f32_16x16x32_bf16 v[104:107], a[184:187], a[160:163], v[104:107]\n"
"ds_read_b64_tr_b16 a[36:37], v37 offset:128\n"
"s_mul_i32 s45, s53, 6144\n"
"v_mfma_f32_16x16x32_bf16 v[104:107], a[224:227], a[200:203], v[104:107]\n"
"ds_read_b64_tr_b16 a[38:39], v37 offset:6272\n"
"s_add_u32 m0, s48, 16384\n"
"v_mfma_f32_16x16x32_bf16 v[128:131], a[224:227], a[204:207], v[128:131]\n"
"buffer_load_dwordx4 v50, s[28:31], s72 offen offset:0 sc1 lds\n"
"v_mfma_f32_16x16x32_bf16 v[128:131], a[184:187], a[164:167], v[128:131]\n"
"s_sub_u32 s50, s50, 3\n"
"s_lshl_b32 s62, s54, 5\n"
"v_mfma_f32_16x16x32_bf16 v[152:155], a[184:187], a[168:171], v[152:155]\n"
"s_add_u32 s45, s45, s62\n"
"s_add_u32 s45, s45, 122880\n"
"v_mfma_f32_16x16x32_bf16 v[152:155], a[224:227], a[208:211], v[152:155]\n"
"buffer_load_dwordx4 v50, s[28:31], s72 offen offset:1024 sc1 lds\n"
"v_mfma_f32_16x16x32_bf16 v[176:179], a[224:227], a[212:215], v[176:179]\n"
"v_add_u32 v11, s45, v11\n"
"v_mfma_f32_16x16x32_bf16 v[176:179], a[184:187], a[172:175], v[176:179]\n"
"s_cmp_gt_u32 s50, 3\n"
"s_cbranch_scc0 Lp2c%=\n"
"Lp2l%=:\n"
"v_mfma_f32_16x16x32_bf16 v[180:183], a[188:191], a[172:175], v[180:183]\n"
"ds_read_b64_tr_b16 a[56:57], v36 offset:12288\n"
"ds_read_b64_tr_b16 a[58:59], v36 offset:18432\n"
"v_mfma_f32_16x16x32_bf16 v[180:183], a[228:231], a[212:215], v[180:183]\n"
"ds_read_b128 a[40:43], v40 offset:33792\n"
"v_mfma_f32_16x16x32_bf16 v[156:159], a[228:231], a[208:211], v[156:159]\n"
"ds_read_b128 a[44:47], v41 offset:33792\n"
"v_mfma_f32_16x16x32_bf16 v[156:159], a[188:191], a[168:171], v[156:159]\n"
"buffer_load_dwordx4 v50, s[28:31], s72 offen offset:2048 sc1 lds\n"
"v_mfma_f32_16x16x32_bf16 v[132:135], a[188:191], a[164:167], v[132:135]\n"
"ds_read_b128 a[48:51], v42 offset:33792\n"
"v_mfma_f32_16x16x32_bf16 v[132:135], a[228:231], a[204:207], v[132:135]\n"
"ds_read_b128 a[52:55], v43 offset:33792\n"
"v_mfma_f32_16x16x32_bf16 v[108:111], a[228:231], a[200:203], v[108:111]\n"
"ds_read_b64_tr_b16 a[60:61], v37 offset:12288\n"
"v_mfma_f32_16x16x32_bf16 v[108:111], a[188:191], a[160:163], v[108:111]\n"
"ds_read_b64_tr_b16 a[62:63], v37 offset:18432\n"
"v_mfma_f32_16x16x32_bf16 v[112:115], a[192:195], a[160:163], v[112:115]\n"
"buffer_load_dwordx4 v50, s[28:31], s72 offen offset:3072 sc1 lds\n"
"v_mfma_f32_16x16x32_bf16 v[112:115], a[232:235], a[200:203], v[112:115]\n"
"ds_read_b64_tr_b16 a[64:65], v38 offset:12288\n"
"s_waitcnt vmcnt(7)\n"
"v_mfma_f32_16x16x32_bf16 v[136:139], a[232:235], a[204:207], v[136:139]\n"
"ds_read_b64_tr_b16 a[66:67], v38 offset:18432\n"
"v_cmp_eq_u64 vcc, s[82:83], v[54:55]\n"
"v_mfma_f32_16x16x32_bf16 v[136:139], a[192:195], a[164:167], v[136:139]\n"
"ds_read_b64_tr_b16 a[68:69], v39 offset:12288\n"
"s_lshr_b64 s[56:57], vcc, s44\n"
"v_mfma_f32_16x16x32_bf16 v[160:163], a[192:195], a[168:171], v[160:163]\n"
"ds_read_b64_tr_b16 a[70:71], v39 offset:18432\n"
"s_add_u32 m0, s80, 27648\n"
"v_mfma_f32_16x16x32_bf16 v[160:163], a[232:235], a[208:211], v[160:163]\n"
"buffer_load_dwordx4 v47, s[24:27], s73 offen offset:0 lds\n"
"v_mfma_f32_16x16x32_bf16 v[184:187], a[232:235], a[212:215], v[184:187]\n"
"ds_read_b64_tr_b16 a[72:73], v36 offset:12416\n"
"s_and_b32 s56, s56, 7\n"
"v_mfma_f32_16x16x32_bf16 v[184:187], a[192:195], a[172:175], v[184:187]\n"
"ds_read_b64_tr_b16 a[74:75], v36 offset:18560\n"
"s_cmp_eq_u32 s56, 7\n"
"v_mfma_f32_16x16x32_bf16 v[188:191], a[196:199], a[172:175], v[188:191]\n"
"ds_read_b64_tr_b16 a[76:77], v37 offset:12416\n"
"s_cbranch_scc0 Lcp2b_slow%=\n"
"Lcp2b_ok%=:\n"
"v_mfma_f32_16x16x32_bf16 v[188:191], a[236:239], a[212:215], v[188:191]\n"
"ds_read_b64_tr_b16 a[78:79], v37 offset:18560\n"
"v_mfma_f32_16x16x32_bf16 v[164:167], a[236:239], a[208:211], v[164:167]\n"
"buffer_load_dwordx4 v48, s[24:27], s73 offen offset:1024 lds\n"
"v_mfma_f32_16x16x32_bf16 v[164:167], a[196:199], a[168:171], v[164:167]\n"
"v_readlane_b32 s46, v51, s44\n"
"v_mfma_f32_16x16x32_bf16 v[140:143], a[196:199], a[164:167], v[140:143]\n"
"v_readlane_b32 s47, v52, s44\n"
"v_mfma_f32_16x16x32_bf16 v[140:143], a[236:239], a[204:207], v[140:143]\n"
"buffer_load_dwordx4 v49, s[24:27], s73 offen offset:2048 lds\n"
"v_mfma_f32_16x16x32_bf16 v[116:119], a[236:239], a[200:203], v[116:119]\n"
"s_add_u32 s44, s44, 1\n"
"v_mfma_f32_16x16x32_bf16 v[116:119], a[196:199], a[160:163], v[116:119]\n"
"s_waitcnt vmcnt(10) lgkmcnt(0)\n"
"s_barrier\n"
"v_mfma_f32_16x16x32_bf16 v[96:99], a[16:19], a[0:3], v[96:99]\n"
"ds_read_b64_tr_b16 a[96:97], v32 offset:0\n"
"ds_read_b64_tr_b16 a[98:99], v32 offset:6144\n"
"v_mfma_f32_16x16x32_bf16 v[96:99], a[56:59], a[40:43], v[96:99]\n"
"ds_read_b128 a[80:83], v40 offset:0\n"
"v_mfma_f32_16x16x32_bf16 v[120:123], a[56:59], a[44:47], v[120:123]\n"
"ds_read_b128 a[84:87], v41 offset:0\n"
"s_add_u32 m0, s80, 49152\n"
"v_mfma_f32_16x16x32_bf16 v[120:123], a[16:19], a[4:7], v[120:123]\n"
"buffer_load_dwordx4 v44, s[24:27], s47 offen offset:0 lds\n"
"v_mfma_f32_16x16x32_bf16 v[144:147], a[16:19], a[8:11], v[144:147]\n"
"ds_read_b128 a[88:91], v42 offset:0\n"
"v_mfma_f32_16x16x32_bf16 v[144:147], a[56:59], a[48:51], v[144:147]\n"
"ds_read_b128 a[92:95], v43 offset:0\n"
"v_mfma_f32_16x16x32_bf16 v[168:171], a[56:59], a[52:55], v[168:171]\n"
"ds_read_b64_tr_b16 a[100:101], v33 offset:0\n"
"v_mfma_f32_16x16x32_bf16 v[168:171], a[16:19], a[12:15], v[168:171]\n"
"ds_read_b64_tr_b16 a[102:103], v33 offset:6144\n"
"v_mfma_f32_16x16x32_bf16 v[172:175], a[20:23], a[12:15], v[172:175]\n"
"buffer_load_dwordx4 v45, s[24:27], s47 offen offset:1024 lds\n"
"v_mfma_f32_16x16x32_bf16 v[172:175], a[60:63], a[52:55], v[172:175]\n"
"ds_read_b64_tr_b16 a[104:105], v34 offset:0\n"
"v_mfma_f32_16x16x32_bf16 v[148:151], a[60:63], a[48:51], v[148:151]\n"
"ds_read_b64_tr_b16 a[106:107], v34 offset:6144\n"
"v_mfma_f32_16x16x32_bf16 v[148:151], a[20:23], a[8:11], v[148:151]\n"
"ds_read_b64_tr_b16 a[108:109], v35 offset:0\n"
"v_mfma_f32_16x16x32_bf16 v[124:127], a[20:23], a[4:7], v[124:127]\n"
"ds_read_b64_tr_b16 a[110:111], v35 offset:6144\n"
"v_mfma_f32_16x16x32_bf16 v[124:127], a[60:63], a[44:47], v[124:127]\n"
"buffer_load_dwordx4 v46, s[24:27], s47 offen offset:2048 lds\n"
"v_mfma_f32_16x16x32_bf16 v[100:103], a[60:63], a[40:43], v[100:103]\n"
"ds_read_b64_tr_b16 a[112:113], v32 offset:128\n"
"v_mfma_f32_16x16x32_bf16 v[100:103], a[20:23], a[0:3], v[100:103]\n"
"ds_read_b64_tr_b16 a[114:115], v32 offset:6272\n"
"v_mfma_f32_16x16x32_bf16 v[104:107], a[24:27], a[0:3], v[104:107]\n"
"ds_read_b64_tr_b16 a[116:117], v33 offset:128\n"
"v_mfma_f32_16x16x32_bf16 v[104:107], a[64:67], a[40:43], v[104:107]\n"
"ds_read_b64_tr_b16 a[118:119], v33 offset:6272\n"
"s_add_u32 m0, s48, 32768\n"
"v_mfma_f32_16x16x32_bf16 v[128:131], a[64:67], a[44:47], v[128:131]\n"
"buffer_load_dwordx4 v50, s[28:31], s46 offen offset:0 sc1 lds\n"
"v_mfma_f32_16x16x32_bf16 v[128:131], a[24:27], a[4:7], v[128:131]\n"
"v_mfma_f32_16x16x32_bf16 v[152:155], a[24:27], a[8:11], v[152:155]\n"
"v_mfma_f32_16x16x32_bf16 v[152:155], a[64:67], a[48:51], v[152:155]\n"
"buffer_load_dwordx4 v50, s[28:31], s46 offen offset:1024 sc1 lds\n"
"v_mfma_f32_16x16x32_bf16 v[176:179], a[64:67], a[52:55], v[176:179]\n"
"v_mfma_f32_16x16x32_bf16 v[176:179], a[24:27], a[12:15], v[176:179]\n"
"v_mfma_f32_16x16x32_bf16 v[180:183], a[28:31], a[12:15], v[180:183]\n"
"ds_read_b64_tr_b16 a[136:137], v32 offset:12288\n"
"ds_read_b64_tr_b16 a[138:139], v32 offset:18432\n"
"v_mfma_f32_16x16x32_bf16 v[180:183], a[68:71], a[52:55], v[180:183]\n"
"ds_read_b128 a[120:123], v40 offset:1024\n"
"v_mfma_f32_16x16x32_bf16 v[156:159], a[68:71], a[48:51], v[156:159]\n"
"ds_read_b128 a[124:127], v41 offset:1024\n"
"v_mfma_f32_16x16x32_bf16 v[156:159], a[28:31], a[8:11], v[156:159]\n"
"buffer_load_dwordx4 v50, s[28:31], s46 offen offset:2048 sc1 lds\n"
"v_mfma_f32_16x16x32_bf16 v[132:135], a[28:31], a[4:7], v[132:135]\n"
"ds_read_b128 a[128:131], v42 offset:1024\n"
"v_mfma_f32_16x16x32_bf16 v[132:135], a[68:71], a[44:47], v[132:135]\n"
"ds_read_b128 a[132:135], v43 offset:1024\n"
"v_mfma_f32_16x16x32_bf16 v[108:111], a[68:71], a[40:43], v[108:111]\n"
"ds_read_b64_tr_b16 a[140:141], v33 offset:12288\n"
"v_mfma_f32_16x16x32_bf16 v[108:111], a[28:31], a[0:3], v[108:111]\n"
"ds_read_b64_tr_b16 a[142:143], v33 offset:18432\n"
"v_mfma_f32_16x16x32_bf16 v[112:115], a[32:35], a[0:3], v[112:115]\n"
"buffer_load_dwordx4 v50, s[28:31], s46 offen offset:3072 sc1 lds\n"
"v_mfma_f32_16x16x32_bf16 v[112:115], a[72:75], a[40:43], v[112:115]\n"
"ds_read_b64_tr_b16 a[144:145], v34 offset:12288\n"
"v_mfma_f32_16x16x32_bf16 v[136:139], a[72:75], a[44:47], v[136:139]\n"
"ds_read_b64_tr_b16 a[146:147], v34 offset:18432\n"
"v_mfma_f32_16x16x32_bf16 v[136:139], a[32:35], a[4:7], v[136:139]\n"
"ds_read_b64_tr_b16 a[148:149], v35 offset:12288\n"
"v_mfma_f32_16x16x32_bf16 v[160:163], a[32:35], a[8:11], v[160:163]\n"
"ds_read_b64_tr_b16 a[150:151], v35 offset:18432\n"
"s_add_u32 m0, s80, 52224\n"
"v_mfma_f32_16x16x32_bf16 v[160:163], a[72:75], a[48:51], v[160:163]\n"
"buffer_load_dwordx4 v47, s[24:27], s47 offen offset:0 lds\n"
"v_mfma_f32_16x16x32_bf16 v[184:187], a[72:75], a[52:55], v[184:187]\n"
"ds_read_b64_tr_b16 a[152:153], v32 offset:12416\n"
"v_mfma_f32_16x16x32_bf16 v[184:187], a[32:35], a[12:15], v[184:187]\n"
"ds_read_b64_tr_b16 a[154:155], v32 offset:18560\n"
"v_mfma_f32_16x16x32_bf16 v[188:191], a[36:39], a[12:15], v[188:191]\n"
"ds_read_b64_tr_b16 a[156:157], v33 offset:12416\n"
"v_mfma_f32_16x16x32_bf16 v[188:191], a[76:79], a[52:55], v[188:191]\n"
"ds_read_b64_tr_b16 a[158:159], v33 offset:18560\n"
"v_mfma_f32_16x16x32_bf16 v[164:167], a[76:79], a[48:51], v[164:167]\n"
"buffer_load_dwordx4 v48, s[24:27], s47 offen offset:1024 lds\n"
"v_mfma_f32_16x16x32_bf16 v[164:167], a[36:39], a[8:11], v[164:167]\n"
"v_readlane_b32 s68, v51, s44\n"
"v_mfma_f32_16x16x32_bf16 v[140:143], a[36:39], a[4:7], v[140:143]\n"
"v_readlane_b32 s69, v52, s44\n"
"v_mfma_f32_16x16x32_bf16 v[140:143], a[76:79], a[44:47], v[140:143]\n"
"buffer_load_dwordx4 v49, s[24:27], s47 offen offset:2048 lds\n"
"v_mfma_f32_16x16x32_bf16 v[116:119], a[76:79], a[40:43], v[116:119]\n"
"s_add_u32 s44, s44, 1\n"
"v_mfma_f32_16x16x32_bf16 v[116:119], a[36:39], a[0:3], v[116:119]\n"
"s_waitcnt vmcnt(10) lgkmcnt(0)\n"
"s_barrier\n"
"v_mfma_f32_16x16x32_bf16 v[96:99], a[96:99], a[80:83], v[96:99]\n"
"ds_read_b64_tr_b16 a[176:177], v32 offset:24576\n"
"ds_read_b64_tr_b16 a[178:179], v32 offset:30720\n"
"v_mfma_f32_16x16x32_bf16 v[96:99], a[136:139], a[120:123], v[96:99]\n"
"ds_read_b128 a[160:163], v40 offset:16384\n"
"v_mfma_f32_16x16x32_bf16 v[120:123], a[136:139], a[124:127], v[120:123]\n"
"ds_read_b128 a[164:167], v41 offset:16384\n"
"s_add_u32 m0, s80, 0\n"
"v_mfma_f32_16x16x32_bf16 v[120:123], a[96:99], a[84:87], v[120:123]\n"
"buffer_load_dwordx4 v44, s[24:27], s69 offen offset:0 lds\n"
"v_mfma_f32_16x16x32_bf16 v[144:147], a[96:99], a[88:91], v[144:147]\n"
"ds_read_b128 a[168:171], v42 offset:16384\n"
"v_mfma_f32_16x16x32_bf16 v[144:147], a[136:139], a[128:131], v[144:147]\n"
"ds_read_b128 a[172:175], v43 offset:16384\n"
"v_mfma_f32_16x16x32_bf16 v[168:171], a[136:139], a[132:135], v[168:171]\n"
"ds_read_b64_tr_b16 a[180:181], v33 offset:24576\n"
"v_mfma_f32_16x16x32_bf16 v[168:171], a[96:99], a[92:95], v[168:171]\n"
"ds_read_b64_tr_b16 a[182:183], v33 offset:30720\n"
"v_mfma_f32_16x16x32_bf16 v[172:175], a[100:103], a[92:95], v[172:175]\n"
"buffer_load_dwordx4 v45, s[24:27], s69 offen offset:1024 lds\n"
"v_mfma_f32_16x16x32_bf16 v[172:175], a[140:143], a[132:135], v[172:175]\n"
"ds_read_b64_tr_b16 a[184:185], v34 offset:24576\n"
"v_mfma_f32_16x16x32_bf16 v[148:151], a[140:143], a[128:131], v[148:151]\n"
"ds_read_b64_tr_b16 a[186:187], v34 offset:30720\n"
"v_mfma_f32_16x16x32_bf16 v[148:151], a[100:103], a[88:91], v[148:151]\n"
"ds_read_b64_tr_b16 a[188:189], v35 offset:24576\n"
"v_mfma_f32_16x16x32_bf16 v[124:127], a[100:103], a[84:87], v[124:127]\n"
"ds_read_b64_tr_b16 a[190:191], v35 offset:30720\n"
"v_mfma_f32_16x16x32_bf16 v[124:127], a[140:143], a[124:127], v[124:127]\n"
"buffer_load_dwordx4 v46, s[24:27], s69 offen offset:2048 lds\n"
"v_mfma_f32_16x16x32_bf16 v[100:103], a[140:143], a[120:123], v[100:103]\n"
"ds_read_b64_tr_b16 a[192:193], v32 offset:24704\n"
"v_mfma_f32_16x16x32_bf16 v[100:103], a[100:103], a[80:83], v[100:103]\n"
"ds_read_b64_tr_b16 a[194:195], v32 offset:30848\n"
"v_mfma_f32_16x16x32_bf16 v[104:107], a[104:107], a[80:83], v[104:107]\n"
"ds_read_b64_tr_b16 a[196:197], v33 offset:24704\n"
"v_mfma_f32_16x16x32_bf16 v[104:107], a[144:147], a[120:123], v[104:107]\n"
"ds_read_b64_tr_b16 a[198:199], v33 offset:30848\n"
"s_add_u32 m0, s48, 0\n"
"v_mfma_f32_16x16x32_bf16 v[128:131], a[144:147], a[124:127], v[128:131]\n"
"buffer_load_dwordx4 v50, s[28:31], s68 offen offset:0 sc1 lds\n"
"v_mfma_f32_16x16x32_bf16 v[128:131], a[104:107], a[84:87], v[128:131]\n"
"v_mfma_f32_16x16x32_bf16 v[152:155], a[104:107], a[88:91], v[152:155]\n"
"v_mfma_f32_16x16x32_bf16 v[152:155], a[144:147], a[128:131], v[152:155]\n"
"buffer_load_dwordx4 v50, s[28:31], s68 offen offset:1024 sc1 lds\n"
"v_mfma_f32_16x16x32_bf16 v[176:179], a[144:147], a[132:135], v[176:179]\n"
"v_mfma_f32_16x16x32_bf16 v[176:179], a[104:107], a[92:95], v[176:179]\n"
"v_mfma_f32_16x16x32_bf16 v[180:183], a[108:111], a[92:95], v[180:183]\n"
"ds_read_b64_tr_b16 a[216:217], v32 offset:36864\n"
"ds_read_b64_tr_b16 a[218:219], v32 offset:43008\n"
"v_mfma_f32_16x16x32_bf16 v[180:183], a[148:151], a[132:135], v[180:183]\n"
"ds_read_b128 a[200:203], v40 offset:17408\n"
"v_mfma_f32_16x16x32_bf16 v[156:159], a[148:151], a[128:131], v[156:159]\n"
"ds_read_b128 a[204:207], v41 offset:17408\n"
"v_mfma_f32_16x16x32_bf16 v[156:159], a[108:111], a[88:91], v[156:159]\n"
"buffer_load_dwordx4 v50, s[28:31], s68 offen offset:2048 sc1 lds\n"
"v_mfma_f32_16x16x32_bf16 v[132:135], a[108:111], a[84:87], v[132:135]\n"
"ds_read_b128 a[208:211], v42 offset:17408\n"
"v_mfma_f32_16x16x32_bf16 v[132:135], a[148:151], a[124:127], v[132:135]\n"
"ds_read_b128 a[212:215], v43 offset:17408\n"
"v_mfma_f32_16x16x32_bf16 v[108:111], a[148:151], a[120:123], v[108:111]\n"
"ds_read_b64_tr_b16 a[220:221], v33 offset:36864\n"
"v_mfma_f32_16x16x32_bf16 v[108:111], a[108:111], a[80:83], v[108:111]\n"
"ds_read_b64_tr_b16 a[222:223], v33 offset:43008\n"
"v_mfma_f32_16x16x32_bf16 v[112:115], a[112:115], a[80:83], v[112:115]\n"
"buffer_load_dwordx4 v50, s[28:31], s68 offen offset:3072 sc1 lds\n"
"v_mfma_f32_16x16x32_bf16 v[112:115], a[152:155], a[120:123], v[112:115]\n"
"ds_read_b64_tr_b16 a[224:225], v34 offset:36864\n"
"v_mfma_f32_16x16x32_bf16 v[136:139], a[152:155], a[124:127], v[136:139]\n"
"ds_read_b64_tr_b16 a[226:227], v34 offset:43008\n"
"v_mfma_f32_16x16x32_bf16 v[136:139], a[112:115], a[84:87], v[136:139]\n"
"ds_read_b64_tr_b16 a[228:229], v35 offset:36864\n"
"v_mfma_f32_16x16x32_bf16 v[160:163], a[112:115], a[88:91], v[160:163]\n"
"ds_read_b64_tr_b16 a[230:231], v35 offset:43008\n"
"s_add_u32 m0, s80, 3072\n"
"v_mfma_f32_16x16x32_bf16 v[160:163], a[152:155], a[128:131], v[160:163]\n"
"buffer_load_dwordx4 v47, s[24:27], s69 offen offset:0 lds\n"
"v_mfma_f32_16x16x32_bf16 v[184:187], a[152:155], a[132:135], v[184:187]\n"
"ds_read_b64_tr_b16 a[232:233], v32 offset:36992\n"
"v_mfma_f32_16x16x32_bf16 v[184:187], a[112:115], a[92:95], v[184:187]\n"
"ds_read_b64_tr_b16 a[234:235], v32 offset:43136\n"
"v_mfma_f32_16x16x32_bf16 v[188:191], a[116:119], a[92:95], v[188:191]\n"
"ds_read_b64_tr_b16 a[236:237], v33 offset:36992\n"
"v_mfma_f32_16x16x32_bf16 v[188:191], a[156:159], a[132:135], v[188:191]\n"
"ds_read_b64_tr_b16 a[238:239], v33 offset:43136\n"
"v_mfma_f32_16x16x32_bf16 v[164:167], a[156:159], a[128:131], v[164:167]\n"
"buffer_load_dwordx4 v48, s[24:27], s69 offen offset:1024 lds\n"
"v_mfma_f32_16x16x32_bf16 v[164:167], a[116:119], a[88:91], v[164:167]\n"
"v_readlane_b32 s72, v51, s44\n"
"v_mfma_f32_16x16x32_bf16 v[140:143], a[116:119], a[84:87], v[140:143]\n"
"v_readlane_b32 s73, v52, s44\n"
"v_mfma_f32_16x16x32_bf16 v[140:143], a[156:159], a[124:127], v[140:143]\n"
"buffer_load_dwordx4 v49, s[24:27], s69 offen offset:2048 lds\n"
"v_mfma_f32_16x16x32_bf16 v[116:119], a[156:159], a[120:123], v[116:119]\n"
"s_add_u32 s44, s44, 1\n"
"v_mfma_f32_16x16x32_bf16 v[116:119], a[116:119], a[80:83], v[116:119]\n"
"s_waitcnt vmcnt(10) lgkmcnt(0)\n"
"s_barrier\n"
"v_mfma_f32_16x16x32_bf16 v[96:99], a[176:179], a[160:163], v[96:99]\n"
"ds_read_b64_tr_b16 a[16:17], v36 offset:0\n"
"ds_read_b64_tr_b16 a[18:19], v36 offset:6144\n"
"v_mfma_f32_16x16x32_bf16 v[96:99], a[216:219], a[200:203], v[96:99]\n"
"ds_read_b128 a[0:3], v40 offset:32768\n"
"buffer_load_dwordx2 v[54:55], v53, s[36:39], 0 offen sc1\n"
"v_mfma_f32_16x16x32_bf16 v[120:123], a[216:219], a[204:207], v[120:123]\n"
"ds_read_b128 a[4:7], v41 offset:32768\n"
"s_add_u32 m0, s80, 24576\n"
"v_mfma_f32_16x16x32_bf16 v[120:123], a[176:179], a[164:167], v[120:123]\n"
"buffer_load_dwordx4 v44, s[24:27], s73 offen offset:0 lds\n"
"v_mfma_f32_16x16x32_bf16 v[144:147], a[176:179], a[168:171], v[144:147]\n"
"ds_read_b128 a[8:11], v42 offset:32768\n"
"v_mfma_f32_16x16x32_bf16 v[144:147], a[216:219], a[208:211], v[144:147]\n"
"ds_read_b128 a[12:15], v43 offset:32768\n"
"v_mfma_f32_16x16x32_bf16 v[168:171], a[216:219], a[212:215], v[168:171]\n"
"ds_read_b64_tr_b16 a[20:21], v37 offset:0\n"
"v_mfma_f32_16x16x32_bf16 v[168:171], a[176:179], a[172:175], v[168:171]\n"
"ds_read_b64_tr_b16 a[22:23], v37 offset:6144\n"
"v_mfma_f32_16x16x32_bf16 v[172:175], a[180:183], a[172:175], v[172:175]\n"
"buffer_load_dwordx4 v45, s[24:27], s73 offen offset:1024 lds\n"
"v_mfma_f32_16x16x32_bf16 v[172:175], a[220:223], a[212:215], v[172:175]\n"
"ds_read_b64_tr_b16 a[24:25], v38 offset:0\n"
"v_mfma_f32_16x16x32_bf16 v[148:151], a[220:223], a[208:211], v[148:151]\n"
"ds_read_b64_tr_b16 a[26:27], v38 offset:6144\n"
"v_mfma_f32_16x16x32_bf16 v[148:151], a[180:183], a[168:171], v[148:151]\n"
"ds_read_b64_tr_b16 a[28:29], v39 offset:0\n"
"v_mfma_f32_16x16x32_bf16 v[124:127], a[180:183], a[164:167], v[124:127]\n"
"ds_read_b64_tr_b16 a[30:31], v39 offset:6144\n"
"v_mfma_f32_16x16x32_bf16 v[124:127], a[220:223], a[204:207], v[124:127]\n"
"buffer_load_dwordx4 v46, s[24:27], s73 offen offset:2048 lds\n"
"v_mfma_f32_16x16x32_bf16 v[100:103], a[220:223], a[200:203], v[100:103]\n"
"ds_read_b64_tr_b16 a[32:33], v36 offset:128\n"
"v_mfma_f32_16x16x32_bf16 v[100:103], a[180:183], a[160:163], v[100:103]\n"
"ds_read_b64_tr_b16 a[34:35], v36 offset:6272\n"
"v_mfma_f32_16x16x32_bf16 v[104:107], a[184:187], a[160:163], v[104:107]\n"
"ds_read_b64_tr_b16 a[36:37], v37 offset:128\n"
"v_mfma_f32_16x16x32_bf16 v[104:107], a[224:227], a[200:203], v[104:107]\n"
"ds_read_b64_tr_b16 a[38:39], v37 offset:6272\n"
"s_add_u32 m0, s48, 16384\n"
"v_mfma_f32_16x16x32_bf16 v[128:131], a[224:227], a[204:207], v[128:131]\n"
"buffer_load_dwordx4 v50, s[28:31], s72 offen offset:0 sc1 lds\n"
"v_mfma_f32_16x16x32_bf16 v[128:131], a[184:187], a[164:167], v[128:131]\n"
"s_sub_u32 s50, s50, 3\n"
"v_mfma_f32_16x16x32_bf16 v[152:155], a[184:187], a[168:171], v[152:155]\n"
"v_mfma_f32_16x16x32_bf16 v[152:155], a[224:227], a[208:211], v[152:155]\n"
"buffer_load_dwordx4 v50, s[28:31], s72 offen offset:1024 sc1 lds\n"
"v_mfma_f32_16x16x32_bf16 v[176:179], a[224:227], a[212:215], v[176:179]\n"
"v_mfma_f32_16x16x32_bf16 v[176:179], a[184:187], a[172:175], v[176:179]\n"
"s_cmp_gt_u32 s50, 3\n"
"s_cbranch_scc1 Lp2l%=\n"
"Lp2c%=:\n"
"v_mfma_f32_16x16x32_bf16 v[180:183], a[188:191], a[172:175], v[180:183]\n"
"ds_read_b64_tr_b16 a[56:57], v36 offset:12288\n"
"ds_read_b64_tr_b16 a[58:59], v36 offset:18432\n"
"v_mfma_f32_16x16x32_bf16 v[180:183], a[228:231], a[212:215], v[180:183]\n"
"ds_read_b128 a[40:43], v40 offset:33792\n"
"v_mfma_f32_16x16x32_bf16 v[156:159], a[228:231], a[208:211], v[156:159]\n"
"ds_read_b128 a[44:47], v41 offset:33792\n"
"v_mfma_f32_16x16x32_bf16 v[156:159], a[188:191], a[168:171], v[156:159]\n"
"buffer_load_dwordx4 v50, s[28:31], s72 offen offset:2048 sc1 lds\n"
"v_mfma_f32_16x16x32_bf16 v[132:135], a[188:191], a[164:167], v[132:135]\n"
"ds_read_b128 a[48:51], v42 offset:33792\n"
"v_mfma_f32_16x16x32_bf16 v[132:135], a[228:231], a[204:207], v[132:135]\n"
"ds_read_b128 a[52:55], v43 offset:33792\n"
"v_mfma_f32_16x16x32_bf16 v[108:111], a[228:231], a[200:203], v[108:111]\n"
"ds_read_b64_tr_b16 a[60:61], v37 offset:12288\n"
"v_mfma_f32_16x16x32_bf16 v[108:111], a[188:191], a[160:163], v[108:111]\n"
"ds_read_b64_tr_b16 a[62:63], v37 offset:18432\n"
"v_mfma_f32_16x16x32_bf16 v[112:115], a[192:195], a[160:163], v[112:115]\n"
"buffer_load_dwordx4 v50, s[28:31], s72 offen offset:3072 sc1 lds\n"
"v_mfma_f32_16x16x32_bf16 v[112:115], a[232:235], a[200:203], v[112:115]\n"
"ds_read_b64_tr_b16 a[64:65], v38 offset:12288\n"
"v_mfma_f32_16x16x32_bf16 v[136:139], a[232:235], a[204:207], v[136:139]\n"
"ds_read_b64_tr_b16 a[66:67], v38 offset:18432\n"
"v_mfma_f32_16x16x32_bf16 v[136:139], a[192:195], a[164:167], v[136:139]\n"
"ds_read_b64_tr_b16 a[68:69], v39 offset:12288\n"
"v_mfma_f32_16x16x32_bf16 v[160:163], a[192:195], a[168:171], v[160:163]\n"
"ds_read_b64_tr_b16 a[70:71], v39 offset:18432\n"
"s_add_u32 m0, s80, 27648\n"
"v_mfma_f32_16x16x32_bf16 v[160:163], a[232:235], a[208:211], v[160:163]\n"
"buffer_load_dwordx4 v47, s[24:27], s73 offen offset:0 lds\n"
"v_mfma_f32_16x16x32_bf16 v[184:187], a[232:235], a[212:215], v[184:187]\n"
"ds_read_b64_tr_b16 a[72:73], v36 offset:12416\n"
"v_mfma_f32_16x16x32_bf16 v[184:187], a[192:195], a[172:175], v[184:187]\n"
"ds_read_b64_tr_b16 a[74:75], v36 offset:18560\n"
"v_mfma_f32_16x16x32_bf16 v[188:191], a[196:199], a[172:175], v[188:191]\n"
"ds_read_b64_tr_b16 a[76:77], v37 offset:12416\n"
"v_mfma_f32_16x16x32_bf16 v[188:191], a[236:239], a[212:215], v[188:191]\n"
"ds_read_b64_tr_b16 a[78:79], v37 offset:18560\n"
"v_mfma_f32_16x16x32_bf16 v[164:167], a[236:239], a[208:211], v[164:167]\n"
"buffer_load_dwordx4 v48, s[24:27], s73 offen offset:1024 lds\n"
"v_mfma_f32_16x16x32_bf16 v[164:167], a[196:199], a[168:171], v[164:167]\n"
"v_mfma_f32_16x16x32_bf16 v[140:143], a[196:199], a[164:167], v[140:143]\n"
"v_mfma_f32_16x16x32_bf16 v[140:143], a[236:239], a[204:207], v[140:143]\n"
"buffer_load_dwordx4 v49, s[24:27], s73 offen offset:2048 lds\n"
"v_mfma_f32_16x16x32_bf16 v[116:119], a[236:239], a[200:203], v[116:119]\n"
"v_mfma_f32_16x16x32_bf16 v[116:119], a[196:199], a[160:163], v[116:119]\n"
"s_waitcnt vmcnt(11) lgkmcnt(0)\n"
"s_barrier\n"
"v_mfma_f32_16x16x32_bf16 v[96:99], a[16:19], a[0:3], v[96:99]\n"
"ds_read_b64_tr_b16 a[96:97], v32 offset:0\n"
"ds_read_b64_tr_b16 a[98:99], v32 offset:6144\n"
"v_mfma_f32_16x16x32_bf16 v[96:99], a[56:59], a[40:43], v[96:99]\n"
"ds_read_b128 a[80:83], v40 offset:0\n"
"v_mfma_f32_16x16x32_bf16 v[120:123], a[56:59], a[44:47], v[120:123]\n"
"ds_read_b128 a[84:87], v41 offset:0\n"
"v_mfma_f32_16x16x32_bf16 v[120:123], a[16:19], a[4:7], v[120:123]\n"
"v_mfma_f32_16x16x32_bf16 v[144:147], a[16:19], a[8:11], v[144:147]\n"
"ds_read_b128 a[88:91], v42 offset:0\n"
"v_mfma_f32_16x16x32_bf16 v[144:147], a[56:59], a[48:51], v[144:147]\n"
"ds_read_b128 a[92:95], v43 offset:0\n"
"v_mfma_f32_16x16x32_bf16 v[168:171], a[56:59], a[52:55], v[168:171]\n"
"ds_read_b64_tr_b16 a[100:101], v33 offset:0\n"
"v_mfma_f32_16x16x32_bf16 v[168:171], a[16:19], a[12:15], v[168:171]\n"
"ds_read_b64_tr_b16 a[102:103], v33 offset:6144\n"
"v_mfma_f32_16x16x32_bf16 v[172:175], a[20:23], a[12:15], v[172:175]\n"
"v_mfma_f32_16x16x32_bf16 v[172:175], a[60:63], a[52:55], v[172:175]\n"
"ds_read_b64_tr_b16 a[104:105], v34 offset:0\n"
"v_mfma_f32_16x16x32_bf16 v[148:151], a[60:63], a[48:51], v[148:151]\n"
"ds_read_b64_tr_b16 a[106:107], v34 offset:6144\n"
"v_mfma_f32_16x16x32_bf16 v[148:151], a[20:23], a[8:11], v[148:151]\n"
"ds_read_b64_tr_b16 a[108:109], v35 offset:0\n"
"v_mfma_f32_16x16x32_bf16 v[124:127], a[20:23], a[4:7], v[124:127]\n"
"ds_read_b64_tr_b16 a[110:111], v35 offset:6144\n"
"v_mfma_f32_16x16x32_bf16 v[124:127], a[60:63], a[44:47], v[124:127]\n"
"v_mfma_f32_16x16x32_bf16 v[100:103], a[60:63], a[40:43], v[100:103]\n"
"ds_read_b64_tr_b16 a[112:113], v32 offset:128\n"
"v_mfma_f32_16x16x32_bf16 v[100:103], a[20:23], a[0:3], v[100:103]\n"
"ds_read_b64_tr_b16 a[114:115], v32 offset:6272\n"
"v_mfma_f32_16x16x32_bf16 v[104:107], a[24:27], a[0:3], v[104:107]\n"
"ds_read_b64_tr_b16 a[116:117], v33 offset:128\n"
"v_mfma_f32_16x16x32_bf16 v[104:107], a[64:67], a[40:43], v[104:107]\n"
"ds_read_b64_tr_b16 a[118:119], v33 offset:6272\n"
"v_mfma_f32_16x16x32_bf16 v[128:131], a[64:67], a[44:47], v[128:131]\n"
"ds_read_b64 v[228:229], v10 offset:0\n"
"ds_read_b64 v[230:231], v10 offset:32\n"
"v_mfma_f32_16x16x32_bf16 v[128:131], a[24:27], a[4:7], v[128:131]\n"
"ds_read_b64 v[232:233], v10 offset:64\n"
"ds_read_b64 v[234:235], v10 offset:96\n"
"v_mfma_f32_16x16x32_bf16 v[152:155], a[24:27], a[8:11], v[152:155]\n"
"ds_read_b64 v[236:237], v10 offset:128\n"
"ds_read_b64 v[238:239], v10 offset:160\n"
"v_mfma_f32_16x16x32_bf16 v[152:155], a[64:67], a[48:51], v[152:155]\n"
"v_mfma_f32_16x16x32_bf16 v[176:179], a[64:67], a[52:55], v[176:179]\n"
"v_mfma_f32_16x16x32_bf16 v[176:179], a[24:27], a[12:15], v[176:179]\n"
"v_mfma_f32_16x16x32_bf16 v[180:183], a[28:31], a[12:15], v[180:183]\n"
"ds_read_b64_tr_b16 a[136:137], v32 offset:12288\n"
"ds_read_b64_tr_b16 a[138:139], v32 offset:18432\n"
"v_mfma_f32_16x16x32_bf16 v[180:183], a[68:71], a[52:55], v[180:183]\n"
"ds_read_b128 a[120:123], v40 offset:1024\n"
"v_mfma_f32_16x16x32_bf16 v[156:159], a[68:71], a[48:51], v[156:159]\n"
"ds_read_b128 a[124:127], v41 offset:1024\n"
"v_mfma_f32_16x16x32_bf16 v[156:159], a[28:31], a[8:11], v[156:159]\n"
"v_mfma_f32_16x16x32_bf16 v[132:135], a[28:31], a[4:7], v[132:135]\n"
"ds_read_b128 a[128:131], v42 offset:1024\n"
"v_mfma_f32_16x16x32_bf16 v[132:135], a[68:71], a[44:47], v[132:135]\n"
"ds_read_b128 a[132:135], v43 offset:1024\n"
"v_mfma_f32_16x16x32_bf16 v[108:111], a[68:71], a[40:43], v[108:111]\n"
"ds_read_b64_tr_b16 a[140:141], v33 offset:12288\n"
"v_mfma_f32_16x16x32_bf16 v[108:111], a[28:31], a[0:3], v[108:111]\n"
"ds_read_b64_tr_b16 a[142:143], v33 offset:18432\n"
"v_mfma_f32_16x16x32_bf16 v[112:115], a[32:35], a[0:3], v[112:115]\n"
"v_mfma_f32_16x16x32_bf16 v[112:115], a[72:75], a[40:43], v[112:115]\n"
"ds_read_b64_tr_b16 a[144:145], v34 offset:12288\n"
"v_mfma_f32_16x16x32_bf16 v[136:139], a[72:75], a[44:47], v[136:139]\n"
"ds_read_b64_tr_b16 a[146:147], v34 offset:18432\n"
"v_mfma_f32_16x16x32_bf16 v[136:139], a[32:35], a[4:7], v[136:139]\n"
"ds_read_b64_tr_b16 a[148:149], v35 offset:12288\n"
"v_mfma_f32_16x16x32_bf16 v[160:163], a[32:35], a[8:11], v[160:163]\n"
"ds_read_b64_tr_b16 a[150:151], v35 offset:18432\n"
"v_mfma_f32_16x16x32_bf16 v[160:163], a[72:75], a[48:51], v[160:163]\n"
"v_mfma_f32_16x16x32_bf16 v[184:187], a[72:75], a[52:55], v[184:187]\n"
"ds_read_b64_tr_b16 a[152:153], v32 offset:12416\n"
"v_mfma_f32_16x16x32_bf16 v[184:187], a[32:35], a[12:15], v[184:187]\n"
"ds_read_b64_tr_b16 a[154:155], v32 offset:18560\n"
"v_mfma_f32_16x16x32_bf16 v[188:191], a[36:39], a[12:15], v[188:191]\n"
"ds_read_b64_tr_b16 a[156:157], v33 offset:12416\n"
"v_mfma_f32_16x16x32_bf16 v[188:191], a[76:79], a[52:55], v[188:191]\n"
"ds_read_b64_tr_b16 a[158:159], v33 offset:18560\n"
"v_mfma_f32_16x16x32_bf16 v[164:167], a[76:79], a[48:51], v[164:167]\n"
"v_mfma_f32_16x16x32_bf16 v[164:167], a[36:39], a[8:11], v[164:167]\n"
"v_mfma_f32_16x16x32_bf16 v[140:143], a[36:39], a[4:7], v[140:143]\n"
"v_mfma_f32_16x16x32_bf16 v[140:143], a[76:79], a[44:47], v[140:143]\n"
"v_mfma_f32_16x16x32_bf16 v[116:119], a[76:79], a[40:43], v[116:119]\n"
"v_mfma_f32_16x16x32_bf16 v[116:119], a[36:39], a[0:3], v[116:119]\n"
"s_waitcnt vmcnt(0) lgkmcnt(0)\n"
"s_barrier\n"
"v_mfma_f32_16x16x32_bf16 v[96:99], a[96:99], a[80:83], v[96:99]\n"
"ds_read_b64_tr_b16 a[176:177], v32 offset:24576\n"
"ds_read_b64_tr_b16 a[178:179], v32 offset:30720\n"
"v_mfma_f32_16x16x32_bf16 v[96:99], a[136:139], a[120:123], v[96:99]\n"
"ds_read_b128 a[160:163], v40 offset:16384\n"
"v_lshlrev_b32 v60, 16, v228\n"
"v_mfma_f32_16x16x32_bf16 v[120:123], a[136:139], a[124:127], v[120:123]\n"
"ds_read_b128 a[164:167], v41 offset:16384\n"
"v_and_b32 v61, 0xffff0000, v228\n"
"v_mfma_f32_16x16x32_bf16 v[120:123], a[96:99], a[84:87], v[120:123]\n"
"v_lshlrev_b32 v62, 16, v229\n"
"v_and_b32 v63, 0xffff0000, v229\n"
"v_mfma_f32_16x16x32_bf16 v[144:147], a[96:99], a[88:91], v[144:147]\n"
"ds_read_b128 a[168:171], v42 offset:16384\n"
"v_lshlrev_b32 v64, 16, v230\n"
"v_mfma_f32_16x16x32_bf16 v[144:147], a[136:139], a[128:131], v[144:147]\n"
"ds_read_b128 a[172:175], v43 offset:16384\n"
"v_and_b32 v65, 0xffff0000, v230\n"
"v_mfma_f32_16x16x32_bf16 v[168:171], a[136:139], a[132:135], v[168:171]\n"
"ds_read_b64_tr_b16 a[180:181], v33 offset:24576\n"
"v_lshlrev_b32 v66, 16, v231\n"
"v_mfma_f32_16x16x32_bf16 v[168:171], a[96:99], a[92:95], v[168:171]\n"
"ds_read_b64_tr_b16 a[182:183], v33 offset:30720\n"
"v_and_b32 v67, 0xffff0000, v231\n"
"v_mfma_f32_16x16x32_bf16 v[172:175], a[100:103], a[92:95], v[172:175]\n"
"v_lshlrev_b32 v68, 16, v232\n"
"v_and_b32 v69, 0xffff0000, v232\n"
"v_mfma_f32_16x16x32_bf16 v[172:175], a[140:143], a[132:135], v[172:175]\n"
"ds_read_b64_tr_b16 a[184:185], v34 offset:24576\n"
"v_lshlrev_b32 v70, 16, v233\n"
"v_mfma_f32_16x16x32_bf16 v[148:151], a[140:143], a[128:131], v[148:151]\n"
"ds_read_b64_tr_b16 a[186:187], v34 offset:30720\n"
"v_and_b32 v71, 0xffff0000, v233\n"
"v_mfma_f32_16x16x32_bf16 v[148:151], a[100:103], a[88:91], v[148:151]\n"
"ds_read_b64_tr_b16 a[188:189], v35 offset:24576\n"
"v_lshlrev_b32 v72, 16, v234\n"
"v_mfma_f32_16x16x32_bf16 v[124:127], a[100:103], a[84:87], v[124:127]\n"
"ds_read_b64_tr_b16 a[190:191], v35 offset:30720\n"
"v_and_b32 v73, 0xffff0000, v234\n"
"v_mfma_f32_16x16x32_bf16 v[124:127], a[140:143], a[124:127], v[124:127]\n"
"v_lshlrev_b32 v74, 16, v235\n"
"v_and_b32 v75, 0xffff0000, v235\n"
"v_mfma_f32_16x16x32_bf16 v[100:103], a[140:143], a[120:123], v[100:103]\n"
"ds_read_b64_tr_b16 a[192:193], v32 offset:24704\n"
"v_lshlrev_b32 v76, 16, v236\n"
"v_mfma_f32_16x16x32_bf16 v[100:103], a[100:103], a[80:83], v[100:103]\n"
"ds_read_b64_tr_b16 a[194:195], v32 offset:30848\n"
"v_and_b32 v77, 0xffff0000, v236\n"
"v_mfma_f32_16x16x32_bf16 v[104:107], a[104:107], a[80:83], v[104:107]\n"
"ds_read_b64_tr_b16 a[196:197], v33 offset:24704\n"
"v_lshlrev_b32 v78, 16, v237\n"
"v_mfma_f32_16x16x32_bf16 v[104:107], a[144:147], a[120:123], v[104:107]\n"
"ds_read_b64_tr_b16 a[198:199], v33 offset:30848\n"
"v_and_b32 v79, 0xffff0000, v237\n"
"v_mfma_f32_16x16x32_bf16 v[128:131], a[144:147], a[124:127], v[128:131]\n"
"v_lshlrev_b32 v80, 16, v238\n"
"v_and_b32 v81, 0xffff0000, v238\n"
"v_mfma_f32_16x16x32_bf16 v[128:131], a[104:107], a[84:87], v[128:131]\n"
"v_lshlrev_b32 v82, 16, v239\n"
"v_and_b32 v83, 0xffff0000, v239\n"
"v_mfma_f32_16x16x32_bf16 v[152:155], a[104:107], a[88:91], v[152:155]\n"
"v_mul_f32 v60, v28, v60\n"
"v_mul_f32 v61, v28, v61\n"
"v_mfma_f32_16x16x32_bf16 v[152:155], a[144:147], a[128:131], v[152:155]\n"
"v_mul_f32 v62, v28, v62\n"
"v_mul_f32 v63, v28, v63\n"
"v_mfma_f32_16x16x32_bf16 v[176:179], a[144:147], a[132:135], v[176:179]\n"
"v_mul_f32 v64, v28, v64\n"
"v_mul_f32 v65, v28, v65\n"
"v_mfma_f32_16x16x32_bf16 v[176:179], a[104:107], a[92:95], v[176:179]\n"
"v_mul_f32 v66, v28, v66\n"
"v_mul_f32 v67, v28, v67\n"
"v_mfma_f32_16x16x32_bf16 v[180:183], a[108:111], a[92:95], v[180:183]\n"
"ds_read_b64_tr_b16 a[216:217], v32 offset:36864\n"
"ds_read_b64_tr_b16 a[218:219], v32 offset:43008\n"
"v_mfma_f32_16x16x32_bf16 v[180:183], a[148:151], a[132:135], v[180:183]\n"
"ds_read_b128 a[200:203], v40 offset:17408\n"
"v_mul_f32 v68, v28, v68\n"
"v_mfma_f32_16x16x32_bf16 v[156:159], a[148:151], a[128:131], v[156:159]\n"
"ds_read_b128 a[204:207], v41 offset:17408\n"
"v_mul_f32 v69, v28, v69\n"
"v_mfma_f32_16x16x32_bf16 v[156:159], a[108:111], a[88:91], v[156:159]\n"
"v_mul_f32 v70, v28, v70\n"
"v_mul_f32 v71, v28, v71\n"
"v_mfma_f32_16x16x32_bf16 v[132:135], a[108:111], a[84:87], v[132:135]\n"
"ds_read_b128 a[208:211], v42 offset:17408\n"
"v_mul_f32 v72, v28, v72\n"
"v_mfma_f32_16x16x32_bf16 v[132:135], a[148:151], a[124:127], v[132:135]\n"
"ds_read_b128 a[212:215], v43 offset:17408\n"
"v_mul_f32 v73, v28, v73\n"
"v_mfma_f32_16x16x32_bf16 v[108:111], a[148:151], a[120:123], v[108:111]\n"
"ds_read_b64_tr_b16 a[220:221], v33 offset:36864\n"
"v_mul_f32 v74, v28, v74\n"
"v_mfma_f32_16x16x32_bf16 v[108:111], a[108:111], a[80:83], v[108:111]\n"
"ds_read_b64_tr_b16 a[222:223], v33 offset:43008\n"
"v_mul_f32 v75, v28, v75\n"
"v_mfma_f32_16x16x32_bf16 v[112:115], a[112:115], a[80:83], v[112:115]\n"
"v_mul_f32 v76, v28, v76\n"
"v_mul_f32 v77, v28, v77\n"
"v_mfma_f32_16x16x32_bf16 v[112:115], a[152:155], a[120:123], v[112:115]\n"
"ds_read_b64_tr_b16 a[224:225], v34 offset:36864\n"
"v_mul_f32 v78, v28, v78\n"
"v_mfma_f32_16x16x32_bf16 v[136:139], a[152:155], a[124:127], v[136:139]\n"
"ds_read_b64_tr_b16 a[226:227], v34 offset:43008\n"
"v_mul_f32 v79, v28, v79\n"
"v_mfma_f32_16x16x32_bf16 v[136:139], a[112:115], a[84:87], v[136:139]\n"
"ds_read_b64_tr_b16 a[228:229], v35 offset:36864\n"
"v_mul_f32 v80, v28, v80\n"
"v_mfma_f32_16x16x32_bf16 v[160:163], a[112:115], a[88:91], v[160:163]\n"
"ds_read_b64_tr_b16 a[230:231], v35 offset:43008\n"
"v_mul_f32 v81, v28, v81\n"
"v_mfma_f32_16x16x32_bf16 v[160:163], a[152:155], a[128:131], v[160:163]\n"
"v_mul_f32 v82, v28, v82\n"
"v_mul_f32 v83, v28, v83\n"
"v_mfma_f32_16x16x32_bf16 v[184:187], a[152:155], a[132:135], v[184:187]\n"
"ds_read_b64_tr_b16 a[232:233], v32 offset:36992\n"
"v_mfma_f32_16x16x32_bf16 v[184:187], a[112:115], a[92:95], v[184:187]\n"
"ds_read_b64_tr_b16 a[234:235], v32 offset:43136\n"
"v_mfma_f32_16x16x32_bf16 v[188:191], a[116:119], a[92:95], v[188:191]\n"
"ds_read_b64_tr_b16 a[236:237], v33 offset:36992\n"
"v_mfma_f32_16x16x32_bf16 v[188:191], a[156:159], a[132:135], v[188:191]\n"
"ds_read_b64_tr_b16 a[238:239], v33 offset:43136\n"
"v_mfma_f32_16x16x32_bf16 v[164:167], a[156:159], a[128:131], v[164:167]\n"
"buffer_load_dwordx2 v[54:55], v53, s[36:39], 0 offen sc1\n"
"v_mfma_f32_16x16x32_bf16 v[164:167], a[116:119], a[88:91], v[164:167]\n"
"v_mfma_f32_16x16x32_bf16 v[140:143], a[116:119], a[84:87], v[140:143]\n"
"v_mfma_f32_16x16x32_bf16 v[140:143], a[156:159], a[124:127], v[140:143]\n"
"v_mfma_f32_16x16x32_bf16 v[116:119], a[156:159], a[120:123], v[116:119]\n"
"v_mfma_f32_16x16x32_bf16 v[116:119], a[116:119], a[80:83], v[116:119]\n"
"s_waitcnt lgkmcnt(0)\n"
"v_mfma_f32_16x16x32_bf16 v[120:123], a[176:179], a[164:167], v[120:123]\n"
"v_mfma_f32_16x16x32_bf16 v[120:123], a[216:219], a[204:207], v[120:123]\n"
"v_mfma_f32_16x16x32_bf16 v[124:127], a[220:223], a[204:207], v[124:127]\n"
"v_mfma_f32_16x16x32_bf16 v[124:127], a[180:183], a[164:167], v[124:127]\n"
"v_mfma_f32_16x16x32_bf16 v[128:131], a[184:187], a[164:167], v[128:131]\n"
"v_mfma_f32_16x16x32_bf16 v[128:131], a[224:227], a[204:207], v[128:131]\n"
"v_mfma_f32_16x16x32_bf16 v[132:135], a[228:231], a[204:207], v[132:135]\n"
"v_mfma_f32_16x16x32_bf16 v[132:135], a[188:191], a[164:167], v[132:135]\n"
"v_mfma_f32_16x16x32_bf16 v[136:139], a[192:195], a[164:167], v[136:139]\n"
"v_mfma_f32_16x16x32_bf16 v[136:139], a[232:235], a[204:207], v[136:139]\n"
"v_mfma_f32_16x16x32_bf16 v[140:143], a[236:239], a[204:207], v[140:143]\n"
"v_mfma_f32_16x16x32_bf16 v[140:143], a[196:199], a[164:167], v[140:143]\n"
"v_mfma_f32_16x16x32_bf16 v[144:147], a[176:179], a[168:171], v[144:147]\n"
"v_mfma_f32_16x16x32_bf16 v[144:147], a[216:219], a[208:211], v[144:147]\n"
"s_mov_b32 s44, 0\n"
"v_mfma_f32_16x16x32_bf16 v[148:151], a[220:223], a[208:211], v[148:151]\n"
"s_waitcnt vmcnt(0)\n"
"s_cmp_lg_u32 s19, 0\n"
"s_cbranch_scc1 Lyin%=\n"
"v_cmp_eq_u64 vcc, s[82:83], v[54:55]\n"
"s_mov_b32 s44, 1\n"
"s_cmp_eq_u32 vcc_hi, -1\n"
"s_cbranch_scc0 Lyin%=\n"
"s_mov_b32 s44, 0\n"
"s_mov_b32 m0, 122880\n"
"s_add_u32 s45, s63, 0\n"
"buffer_load_dwordx4 v13, s[28:31], s45 offen offset:0 sc1 lds\n"
"s_add_u32 s45, s63, 1024\n"
"buffer_load_dwordx4 v13, s[28:31], s45 offen offset:1024 sc1 lds\n"
"s_add_u32 s45, s63, 2048\n"
"buffer_load_dwordx4 v13, s[28:31], s45 offen offset:2048 sc1 lds\n"
"s_add_u32 s45, s63, 3072\n"
"buffer_load_dwordx4 v13, s[28:31], s45 offen offset:3072 sc1 lds\n"
"s_mov_b32 m0, 126976\n"
"s_add_u32 s45, s63, 8192\n"
"buffer_load_dwordx4 v13, s[28:31], s45 offen offset:0 sc1 lds\n"
"s_add_u32 s45, s63, 9216\n"
"buffer_load_dwordx4 v13, s[28:31], s45 offen offset:1024 sc1 lds\n"
"s_add_u32 s45, s63, 10240\n"
"buffer_load_dwordx4 v13, s[28:31], s45 offen offset:2048 sc1 lds\n"
"s_add_u32 s45, s63, 11264\n"
"buffer_load_dwordx4 v13, s[28:31], s45 offen offset:3072 sc1 lds\n"
"s_mov_b32 m0, 131072\n"
"s_add_u32 s45, s63, 16384\n"
"buffer_load_dwordx4 v13, s[28:31], s45 offen offset:0 sc1 lds\n"
"s_add_u32 s45, s63, 17408\n"
"buffer_load_dwordx4 v13, s[28:31], s45 offen offset:1024 sc1 lds\n"
"s_add_u32 s45, s63, 18432\n"
"buffer_load_dwordx4 v13, s[28:31], s45 offen offset:2048 sc1 lds\n"
"s_add_u32 s45, s63, 19456\n"
"buffer_load_dwordx4 v13, s[28:31], s45 offen offset:3072 sc1 lds\n"
"Lyin%=:\n"
"v_mfma_f32_16x16x32_bf16 v[148:151], a[180:183], a[168:171], v[148:151]\n"
"v_cvt_pk_bf16_f32 v192, v120, v121\n"
"v_cvt_pk_bf16_f32 v193, v122, v123\n"
"v_mfma_f32_16x16x32_bf16 v[152:155], a[184:187], a[168:171], v[152:155]\n"
"v_cvt_pk_bf16_f32 v194, v124, v125\n"
"v_cvt_pk_bf16_f32 v195, v126, v127\n"
"v_mfma_f32_16x16x32_bf16 v[152:155], a[224:227], a[208:211], v[152:155]\n"
"buffer_store_dwordx4 v[192:195], v8, s[36:39], s58 offen offset:0 sc1\n"
"v_cvt_pk_bf16_f32 v196, v128, v129\n"
"v_mfma_f32_16x16x32_bf16 v[156:159], a[228:231], a[208:211], v[156:159]\n"
"v_cvt_pk_bf16_f32 v197, v130, v131\n"
"v_cvt_pk_bf16_f32 v198, v132, v133\n"
"v_mfma_f32_16x16x32_bf16 v[156:159], a[188:191], a[168:171], v[156:159]\n"
"v_cvt_pk_bf16_f32 v199, v134, v135\n"
"buffer_store_dwordx4 v[196:199], v8, s[36:39], s58 offen offset:1024 sc1\n"
"v_mfma_f32_16x16x32_bf16 v[160:163], a[192:195], a[168:171], v[160:163]\n"
"v_cvt_pk_bf16_f32 v200, v136, v137\n"
"v_cvt_pk_bf16_f32 v201, v138, v139\n"
"v_mfma_f32_16x16x32_bf16 v[160:163], a[232:235], a[208:211], v[160:163]\n"
"v_cvt_pk_bf16_f32 v202, v140, v141\n"
"v_cvt_pk_bf16_f32 v203, v142, v143\n"
"v_mfma_f32_16x16x32_bf16 v[164:167], a[236:239], a[208:211], v[164:167]\n"
"buffer_store_dwordx4 v[200:203], v8, s[36:39], s58 offen offset:2048 sc1\n"
"v_mfma_f32_16x16x32_bf16 v[164:167], a[196:199], a[168:171], v[164:167]\n"
"v_mfma_f32_16x16x32_bf16 v[168:171], a[176:179], a[172:175], v[168:171]\n"
"v_mfma_f32_16x16x32_bf16 v[168:171], a[216:219], a[212:215], v[168:171]\n"
"v_mfma_f32_16x16x32_bf16 v[172:175], a[220:223], a[212:215], v[172:175]\n"
"v_mfma_f32_16x16x32_bf16 v[172:175], a[180:183], a[172:175], v[172:175]\n"
"v_cvt_pk_bf16_f32 v204, v144, v145\n"
"v_cvt_pk_bf16_f32 v205, v146, v147\n"
"v_mfma_f32_16x16x32_bf16 v[176:179], a[184:187], a[172:175], v[176:179]\n"
"v_cvt_pk_bf16_f32 v206, v148, v149\n"
"v_cvt_pk_bf16_f32 v207, v150, v151\n"
"v_mfma_f32_16x16x32_bf16 v[176:179], a[224:227], a[212:215], v[176:179]\n"
"buffer_store_dwordx4 v[204:207], v8, s[36:39], s59 offen offset:0 sc1\n"
"v_cvt_pk_bf16_f32 v208, v152, v153\n"
"v_mfma_f32_16x16x32_bf16 v[180:183], a[228:231], a[212:215], v[180:183]\n"
"v_cvt_pk_bf16_f32 v209, v154, v155\n"
"v_cvt_pk_bf16_f32 v210, v156, v157\n"
"v_mfma_f32_16x16x32_bf16 v[180:183], a[188:191], a[172:175], v[180:183]\n"
"v_cvt_pk_bf16_f32 v211, v158, v159\n"
"buffer_store_dwordx4 v[208:211], v8, s[36:39], s59 offen offset:1024 sc1\n"
"v_mfma_f32_16x16x32_bf16 v[184:187], a[192:195], a[172:175], v[184:187]\n"
"v_cvt_pk_bf16_f32 v212, v160, v161\n"
"v_cvt_pk_bf16_f32 v213, v162, v163\n"
"v_mfma_f32_16x16x32_bf16 v[184:187], a[232:235], a[212:215], v[184:187]\n"
"v_cvt_pk_bf16_f32 v214, v164, v165\n"
"v_cvt_pk_bf16_f32 v215, v166, v167\n"
"v_mfma_f32_16x16x32_bf16 v[188:191], a[236:239], a[212:215], v[188:191]\n"
"buffer_store_dwordx4 v[212:215], v8, s[36:39], s59 offen offset:2048 sc1\n"
"v_mfma_f32_16x16x32_bf16 v[188:191], a[196:199], a[172:175], v[188:191]\n"
"v_mfma_f32_16x16x32_bf16 v[96:99], a[176:179], a[160:163], v[96:99]\n"
"v_mfma_f32_16x16x32_bf16 v[96:99], a[216:219], a[200:203], v[96:99]\n"
"v_mfma_f32_16x16x32_bf16 v[100:103], a[220:223], a[200:203], v[100:103]\n"
"v_mfma_f32_16x16x32_bf16 v[100:103], a[180:183], a[160:163], v[100:103]\n"
"v_cvt_pk_bf16_f32 v216, v168, v169\n"
"v_cvt_pk_bf16_f32 v217, v170, v171\n"
"v_mfma_f32_16x16x32_bf16 v[104:107], a[184:187], a[160:163], v[104:107]\n"
"v_cvt_pk_bf16_f32 v218, v172, v173\n"
"v_cvt_pk_bf16_f32 v219, v174, v175\n"
"v_mfma_f32_16x16x32_bf16 v[104:107], a[224:227], a[200:203], v[104:107]\n"
"buffer_store_dwordx4 v[216:219], v8, s[36:39], s60 offen offset:0 sc1\n"
"v_cvt_pk_bf16_f32 v220, v176, v177\n"
"v_mfma_f32_16x16x32_bf16 v[108:111], a[228:231], a[200:203], v[108:111]\n"
"v_cvt_pk_bf16_f32 v221, v178, v179\n"
"v_cvt_pk_bf16_f32 v222, v180, v181\n"
"v_mfma_f32_16x16x32_bf16 v[108:111], a[188:191], a[160:163], v[108:111]\n"
"v_cvt_pk_bf16_f32 v223, v182, v183\n"
"buffer_store_dwordx4 v[220:223], v8, s[36:39], s60 offen offset:1024 sc1\n"
"v_mfma_f32_16x16x32_bf16 v[112:115], a[192:195], a[160:163], v[112:115]\n"
"v_cvt_pk_bf16_f32 v224, v184, v185\n"
"v_cvt_pk_bf16_f32 v225, v186, v187\n"
"v_mfma_f32_16x16x32_bf16 v[112:115], a[232:235], a[200:203], v[112:115]\n"
"v_cvt_pk_bf16_f32 v226, v188, v189\n"
"v_cvt_pk_bf16_f32 v227, v190, v191\n"
"v_mfma_f32_16x16x32_bf16 v[116:119], a[236:239], a[200:203], v[116:119]\n"
"buffer_store_dwordx4 v[224:227], v8, s[36:39], s60 offen offset:2048 sc1\n"
"v_mfma_f32_16x16x32_bf16 v[116:119], a[196:199], a[160:163], v[116:119]\n"
"s_cmp_eq_u32 s44, 0\n"
"s_cbranch_scc1 Lyback%=\n"
"s_branch Lcy_slow%=\n"
"Lcy_ok%=:\n"
"s_mov_b32 m0, 122880\n"
"s_add_u32 s45, s63, 0\n"
"buffer_load_dwordx4 v13, s[28:31], s45 offen offset:0 sc1 lds\n"
"s_add_u32 s45, s63, 1024\n"
"buffer_load_dwordx4 v13, s[28:31], s45 offen offset:1024 sc1 lds\n"
"s_add_u32 s45, s63, 2048\n"
"buffer_load_dwordx4 v13, s[28:31], s45 offen offset:2048 sc1 lds\n"
"s_add_u32 s45, s63, 3072\n"
"buffer_load_dwordx4 v13, s[28:31], s45 offen offset:3072 sc1 lds\n"
"s_mov_b32 m0, 126976\n"
"s_add_u32 s45, s63, 8192\n"
"buffer_load_dwordx4 v13, s[28:31], s45 offen offset:0 sc1 lds\n"
"s_add_u32 s45, s63, 9216\n"
"buffer_load_dwordx4 v13, s[28:31], s45 offen offset:1024 sc1 lds\n"
"s_add_u32 s45, s63, 10240\n"
"buffer_load_dwordx4 v13, s[28:31], s45 offen offset:2048 sc1 lds\n"
"s_add_u32 s45, s63, 11264\n"
"buffer_load_dwordx4 v13, s[28:31], s45 offen offset:3072 sc1 lds\n"
"s_mov_b32 m0, 131072\n"
"s_add_u32 s45, s63, 16384\n"
"buffer_load_dwordx4 v13, s[28:31], s45 offen offset:0 sc1 lds\n"
"s_add_u32 s45, s63, 17408\n"
"buffer_load_dwordx4 v13, s[28:31], s45 offen offset:1024 sc1 lds\n"
"s_add_u32 s45, s63, 18432\n"
"buffer_load_dwordx4 v13, s[28:31], s45 offen offset:2048 sc1 lds\n"
"s_add_u32 s45, s63, 19456\n"
"buffer_load_dwordx4 v13, s[28:31], s45 offen offset:3072 sc1 lds\n"
"s_waitcnt vmcnt(0)\n"
"Lyback%=:\n"
"v_mov_b32 v6, 0x3f80\n"
"v_mov_b32 v7, 0x3f800000\n"
"s_add_u32 s58, s65, 12288\n"
"s_add_u32 s59, s65, 24576\n"
"v_mov_b32 v26, s92\n"
"v_mov_b32 v27, s93\n"
"v_mul_f32 v96, v29, v96\n"
"v_mul_f32 v97, v29, v97\n"
"v_mul_f32 v98, v29, v98\n"
"v_mul_f32 v99, v29, v99\n"
"v_mul_f32 v100, v29, v100\n"
"v_mul_f32 v101, v29, v101\n"
"v_mul_f32 v102, v29, v102\n"
"v_mul_f32 v103, v29, v103\n"
"v_mul_f32 v104, v29, v104\n"
"v_mul_f32 v105, v29, v105\n"
"v_mul_f32 v106, v29, v106\n"
"v_mul_f32 v107, v29, v107\n"
"v_mul_f32 v108, v29, v108\n"
"v_mul_f32 v109, v29, v109\n"
"v_mul_f32 v110, v29, v110\n"
"v_mul_f32 v111, v29, v111\n"
"v_mul_f32 v112, v29, v112\n"
"v_mul_f32 v113, v29, v113\n"
"v_mul_f32 v114, v29, v114\n"
"v_mul_f32 v115, v29, v115\n"
"v_mul_f32 v116, v29, v116\n"
"v_mul_f32 v117, v29, v117\n"
"v_mul_f32 v118, v29, v118\n"
"v_mul_f32 v119, v29, v119\n"
"s_waitcnt vmcnt(3)\n"
"s_mov_b64 exec, 3\n"
"buffer_store_dwordx2 v[26:27], v14, s[36:39], s55 offen sc1\n"
"s_mov_b64 exec, -1\n"
"s_waitcnt vmcnt(1)\n"
"s_mov_b64 exec, 4\n"
"buffer_store_dwordx2 v[26:27], v14, s[36:39], s55 offen sc1\n"
"s_mov_b64 exec, -1\n"
"buffer_load_dwordx2 v[16:17], v12, s[36:39], s55 offen sc1\n"
"s_barrier\n"
"ds_read_b64_tr_b16 v[240:241], v11 offset:0\n"
"ds_read_b64_tr_b16 v[242:243], v11 offset:1024\n"
"ds_read_b64_tr_b16 v[244:245], v11 offset:2048\n"
"ds_read_b64_tr_b16 v[246:247], v11 offset:3072\n"
"ds_read_b64_tr_b16 v[248:249], v11 offset:4096\n"
"ds_read_b64_tr_b16 v[250:251], v11 offset:5120\n"
"s_waitcnt lgkmcnt(0)\n"
"v_lshlrev_b32 v160, 16, v240\n"
"v_and_b32 v161, 0xffff0000, v240\n"
"v_lshlrev_b32 v162, 16, v241\n"
"v_and_b32 v163, 0xffff0000, v241\n"
"v_lshlrev_b32 v164, 16, v242\n"
"v_and_b32 v165, 0xffff0000, v242\n"
"v_lshlrev_b32 v166, 16, v243\n"
"v_and_b32 v167, 0xffff0000, v243\n"
"v_lshlrev_b32 v168, 16, v244\n"
"v_and_b32 v169, 0xffff0000, v244\n"
"v_lshlrev_b32 v170, 16, v245\n"
"v_and_b32 v171, 0xffff0000, v245\n"
"v_lshlrev_b32 v172, 16, v246\n"
"v_and_b32 v173, 0xffff0000, v246\n"
"v_lshlrev_b32 v174, 16, v247\n"
"v_and_b32 v175, 0xffff0000, v247\n"
"v_lshlrev_b32 v176, 16, v248\n"
"v_and_b32 v177, 0xffff0000, v248\n"
"v_lshlrev_b32 v178, 16, v249\n"
"v_and_b32 v179, 0xffff0000, v249\n"
"v_lshlrev_b32 v180, 16, v250\n"
"v_and_b32 v181, 0xffff0000, v250\n"
"v_lshlrev_b32 v182, 16, v251\n"
"v_and_b32 v183, 0xffff0000, v251\n"
"v_fmac_f32 v96, v160, v60\n"
"v_fmac_f32 v97, v161, v61\n"
"v_fmac_f32 v98, v162, v62\n"
"v_fmac_f32 v99, v163, v63\n"
"v_fmac_f32 v100, v164, v64\n"
"v_fmac_f32 v101, v165, v65\n"
"v_fmac_f32 v102, v166, v66\n"
"v_fmac_f32 v103, v167, v67\n"
"v_fmac_f32 v104, v168, v68\n"
"v_fmac_f32 v105, v169, v69\n"
"v_fmac_f32 v106, v170, v70\n"
"v_fmac_f32 v107, v171, v71\n"
"v_fmac_f32 v108, v172, v72\n"
"v_fmac_f32 v109, v173, v73\n"
"v_fmac_f32 v110, v174, v74\n"
"v_fmac_f32 v111, v175, v75\n"
"v_fmac_f32 v112, v176, v76\n"
"v_fmac_f32 v113, v177, v77\n"
"v_fmac_f32 v114, v178, v78\n"
"v_fmac_f32 v115, v179, v79\n"
"v_fmac_f32 v116, v180, v80\n"
"v_fmac_f32 v117, v181, v81\n"
"v_fmac_f32 v118, v182, v82\n"
"v_fmac_f32 v119, v183, v83\n"
"s_waitcnt vmcnt(0)\n"
"v_cmp_eq_u64 vcc, s[92:93], v[16:17]\n"
"s_nop 0\n"
"s_and_b32 s45, vcc_lo, 3\n"
"s_cmp_eq_u32 s45, 3\n"
"s_cbranch_scc1 Lq1d%=\n"
"s_mov_b32 s52, 0\n"
"Lq1%=:\n"
"buffer_load_dwordx2 v[16:17], v12, s[36:39], s55 offen sc1\n"
"s_waitcnt vmcnt(0)\n"
"v_cmp_eq_u64 vcc, s[92:93], v[16:17]\n"
"s_add_u32 s52, s52, 1\n"
"s_and_b32 s45, vcc_lo, 3\n"
"s_cmp_eq_u32 s45, 3\n"
"s_cbranch_scc1 Lq1d%=\n"
"s_cmp_lt_u32 s52, 0x40000\n"
"s_cbranch_scc1 Lq1%=\n"
"Lq1d%=:\n"
"buffer_load_dwordx4 v[120:123], v8, s[36:39], s65 offen offset:0 sc1\n"
"buffer_load_dwordx4 v[124:127], v8, s[36:39], s65 offen offset:1024 sc1\n"
"buffer_load_dwordx4 v[128:131], v8, s[36:39], s65 offen offset:2048 sc1\n"
"buffer_load_dwordx4 v[132:135], v8, s[36:39], s58 offen offset:0 sc1\n"
"buffer_load_dwordx4 v[136:139], v8, s[36:39], s58 offen offset:1024 sc1\n"
"buffer_load_dwordx4 v[140:143], v8, s[36:39], s58 offen offset:2048 sc1\n"
"s_bitcmp1_b32 vcc_lo, 2\n"
"s_cbranch_scc1 Lq2d%=\n"
"s_mov_b32 s52, 0\n"
"Lq2%=:\n"
"buffer_load_dwordx2 v[16:17], v12, s[36:39], s55 offen sc1\n"
"s_waitcnt vmcnt(0)\n"
"v_cmp_eq_u64 vcc, s[92:93], v[16:17]\n"
"s_add_u32 s52, s52, 1\n"
"s_and_b32 s45, vcc_lo, 4\n"
"s_cmp_eq_u32 s45, 4\n"
"s_cbranch_scc1 Lq2d%=\n"
"s_cmp_lt_u32 s52, 0x40000\n"
"s_cbranch_scc1 Lq2%=\n"
"Lq2d%=:\n"
"buffer_load_dwordx4 v[144:147], v8, s[36:39], s59 offen offset:0 sc1\n"
"buffer_load_dwordx4 v[148:151], v8, s[36:39], s59 offen offset:1024 sc1\n"
"buffer_load_dwordx4 v[152:155], v8, s[36:39], s59 offen offset:2048 sc1\n"
"s_waitcnt vmcnt(6)\n"
"v_lshlrev_b32 v192, 16, v120\n"
"v_and_b32 v193, 0xffff0000, v120\n"
"v_lshlrev_b32 v194, 16, v121\n"
"v_and_b32 v195, 0xffff0000, v121\n"
"v_lshlrev_b32 v196, 16, v122\n"
"v_and_b32 v197, 0xffff0000, v122\n"
"v_lshlrev_b32 v198, 16, v123\n"
"v_and_b32 v199, 0xffff0000, v123\n"
"v_lshlrev_b32 v200, 16, v124\n"
"v_and_b32 v201, 0xffff0000, v124\n"
"v_lshlrev_b32 v202, 16, v125\n"
"v_and_b32 v203, 0xffff0000, v125\n"
"v_lshlrev_b32 v204, 16, v126\n"
"v_and_b32 v205, 0xffff0000, v126\n"
"v_lshlrev_b32 v206, 16, v127\n"
"v_and_b32 v207, 0xffff0000, v127\n"
"v_lshlrev_b32 v208, 16, v128\n"
"v_and_b32 v209, 0xffff0000, v128\n"
"v_lshlrev_b32 v210, 16, v129\n"
"v_and_b32 v211, 0xffff0000, v129\n"
"v_lshlrev_b32 v212, 16, v130\n"
"v_and_b32 v213, 0xffff0000, v130\n"
"v_lshlrev_b32 v214, 16, v131\n"
"v_and_b32 v215, 0xffff0000, v131\n"
"s_waitcnt vmcnt(3)\n"
"v_dot2c_f32_bf16 v192, v132, v6\n"
"v_dot2c_f32_bf16 v193, v132, v7\n"
"v_dot2c_f32_bf16 v194, v133, v6\n"
"v_dot2c_f32_bf16 v195, v133, v7\n"
"v_dot2c_f32_bf16 v196, v134, v6\n"
"v_dot2c_f32_bf16 v197, v134, v7\n"
"v_dot2c_f32_bf16 v198, v135, v6\n"
"v_dot2c_f32_bf16 v199, v135, v7\n"
"v_dot2c_f32_bf16 v200, v136, v6\n"
"v_dot2c_f32_bf16 v201, v136, v7\n"
"v_dot2c_f32_bf16 v202, v137, v6\n"
"v_dot2c_f32_bf16 v203, v137, v7\n"
"v_dot2c_f32_bf16 v204, v138, v6\n"
"v_dot2c_f32_bf16 v205, v138, v7\n"
"v_dot2c_f32_bf16 v206, v139, v6\n"
"v_dot2c_f32_bf16 v207, v139, v7\n"
"v_dot2c_f32_bf16 v208, v140, v6\n"
"v_dot2c_f32_bf16 v209, v140, v7\n"
"v_dot2c_f32_bf16 v210, v141, v6\n"
"v_dot2c_f32_bf16 v211, v141, v7\n"
"v_dot2c_f32_bf16 v212, v142, v6\n"
"v_dot2c_f32_bf16 v213, v142, v7\n"
"v_dot2c_f32_bf16 v214, v143, v6\n"
"v_dot2c_f32_bf16 v215, v143, v7\n"
"s_waitcnt vmcnt(0)\n"
"v_dot2c_f32_bf16 v192, v144, v6\n"
"v_dot2c_f32_bf16 v193, v144, v7\n"
"v_dot2c_f32_bf16 v194, v145, v6\n"
"v_dot2c_f32_bf16 v195, v145, v7\n"
"v_dot2c_f32_bf16 v196, v146, v6\n"
"v_dot2c_f32_bf16 v197, v146, v7\n"
"v_dot2c_f32_bf16 v198, v147, v6\n"
"v_dot2c_f32_bf16 v199, v147, v7\n"
"v_dot2c_f32_bf16 v200, v148, v6\n"
"v_dot2c_f32_bf16 v201, v148, v7\n"
"v_dot2c_f32_bf16 v202, v149, v6\n"
"v_dot2c_f32_bf16 v203, v149, v7\n"
"v_dot2c_f32_bf16 v204, v150, v6\n"
"v_dot2c_f32_bf16 v205, v150, v7\n"
"v_dot2c_f32_bf16 v206, v151, v6\n"
"v_dot2c_f32_bf16 v207, v151, v7\n"
"v_dot2c_f32_bf16 v208, v152, v6\n"
"v_dot2c_f32_bf16 v209, v152, v7\n"
"v_dot2c_f32_bf16 v210, v153, v6\n"
"v_dot2c_f32_bf16 v211, v153, v7\n"
"v_dot2c_f32_bf16 v212, v154, v6\n"
"v_dot2c_f32_bf16 v213, v154, v7\n"
"v_dot2c_f32_bf16 v214, v155, v6\n"
"v_dot2c_f32_bf16 v215, v155, v7\n"
"s_nop 0\n"
"v_fmac_f32 v96, v29, v192\n"
"v_fmac_f32 v97, v29, v193\n"
"v_fmac_f32 v98, v29, v194\n"
"v_fmac_f32 v99, v29, v195\n"
"v_fmac_f32 v100, v29, v196\n"
"v_fmac_f32 v101, v29, v197\n"
"v_fmac_f32 v102, v29, v198\n"
"v_fmac_f32 v103, v29, v199\n"
"v_cvt_pk_bf16_f32 v228, v96, v97\n"
"v_cvt_pk_bf16_f32 v229, v98, v99\n"
"v_cvt_pk_bf16_f32 v230, v100, v101\n"
"v_cvt_pk_bf16_f32 v231, v102, v103\n"
"s_nop 1\n"
"v_permlane16_swap_b32 v228, v230\n"
"v_permlane16_swap_b32 v229, v231\n"
"buffer_store_dwordx4 v[228:231], v9, s[40:43], s61 offen offset:0\n"
"v_fmac_f32 v104, v29, v200\n"
"v_fmac_f32 v105, v29, v201\n"
"v_fmac_f32 v106, v29, v202\n"
"v_fmac_f32 v107, v29, v203\n"
"v_fmac_f32 v108, v29, v204\n"
"v_fmac_f32 v109, v29, v205\n"
"v_fmac_f32 v110, v29, v206\n"
"v_fmac_f32 v111, v29, v207\n"
"v_cvt_pk_bf16_f32 v232, v104, v105\n"
"v_cvt_pk_bf16_f32 v233, v106, v107\n"
"v_cvt_pk_bf16_f32 v234, v108, v109\n"
"v_cvt_pk_bf16_f32 v235, v110, v111\n"
"s_nop 1\n"
"v_permlane16_swap_b32 v232, v234\n"
"v_permlane16_swap_b32 v233, v235\n"
"buffer_store_dwordx4 v[232:235], v9, s[40:43], s61 offen offset:64\n"
"v_fmac_f32 v112, v29, v208\n"
"v_fmac_f32 v113, v29, v209\n"
"v_fmac_f32 v114, v29, v210\n"
"v_fmac_f32 v115, v29, v211\n"
"v_fmac_f32 v116, v29, v212\n"
"v_fmac_f32 v117, v29, v213\n"
"v_fmac_f32 v118, v29, v214\n"
"v_fmac_f32 v119, v29, v215\n"
"v_cvt_pk_bf16_f32 v236, v112, v113\n"
"v_cvt_pk_bf16_f32 v237, v114, v115\n"
"v_cvt_pk_bf16_f32 v238, v116, v117\n"
"v_cvt_pk_bf16_f32 v239, v118, v119\n"
"s_nop 1\n"
"v_permlane16_swap_b32 v236, v238\n"
"v_permlane16_swap_b32 v237, v239\n"
"buffer_store_dwordx4 v[236:239], v9, s[40:43], s61 offen offset:128\n"
"Lfin%=:\n"
"s_branch Lexit%=\n"
"Lc0_slow%=:\n"
"s_mov_b32 s52, 0\n"
"Lc0_sl2%=:\n"
"buffer_load_dwordx2 v[54:55], v53, s[36:39], 0 offen sc1\n"
"s_waitcnt vmcnt(0)\n"
"v_cmp_eq_u64 vcc, s[82:83], v[54:55]\n"
"s_nop 0\n"
"s_lshr_b64 s[56:57], vcc, s44\n"
"s_and_b32 s56, s56, 7\n"
"s_cmp_eq_u32 s56, 7\n"
"s_cbranch_scc1 Lc0_ok%=\n"
"s_add_u32 s52, s52, 1\n"
"s_cmp_lt_u32 s52, 0x40000\n"
"s_cbranch_scc1 Lc0_sl2%=\n"
"s_branch Lc0_ok%=\n"
"Lcp2a_slow%=:\n"
"s_mov_b32 s52, 0\n"
"Lcp2a_sl2%=:\n"
"buffer_load_dwordx2 v[54:55], v53, s[36:39], 0 offen sc1\n"
"s_waitcnt vmcnt(0)\n"
"v_cmp_eq_u64 vcc, s[82:83], v[54:55]\n"
"s_nop 0\n"
"s_lshr_b64 s[56:57], vcc, s44\n"
"s_and_b32 s56, s56, 7\n"
"s_cmp_eq_u32 s56, 7\n"
"s_cbranch_scc1 Lcp2a_ok%=\n"
"s_add_u32 s52, s52, 1\n"
"s_cmp_lt_u32 s52, 0x40000\n"
"s_cbranch_scc1 Lcp2a_sl2%=\n"
"s_branch Lcp2a_ok%=\n"
"Lcp2b_slow%=:\n"
"s_mov_b32 s52, 0\n"
"Lcp2b_sl2%=:\n"
"buffer_load_dwordx2 v[54:55], v53, s[36:39], 0 offen sc1\n"
"s_waitcnt vmcnt(0)\n"
"v_cmp_eq_u64 vcc, s[82:83], v[54:55]\n"
"s_nop 0\n"
"s_lshr_b64 s[56:57], vcc, s44\n"
"s_and_b32 s56, s56, 7\n"
"s_cmp_eq_u32 s56, 7\n"
"s_cbranch_scc1 Lcp2b_ok%=\n"
"s_add_u32 s52, s52, 1\n"
"s_cmp_lt_u32 s52, 0x40000\n"
"s_cbranch_scc1 Lcp2b_sl2%=\n"
"s_branch Lcp2b_ok%=\n"
"Lcy_slow%=:\n"
"s_mov_b32 s52, 0\n"
"Lcy_sl2%=:\n"
"buffer_load_dwordx2 v[54:55], v53, s[36:39], 0 offen sc1\n"
"s_waitcnt vmcnt(0)\n"
"v_cmp_eq_u64 vcc, s[82:83], v[54:55]\n"
"s_nop 0\n"
"s_cmp_eq_u32 vcc_hi, -1\n"
"s_cbranch_scc1 Lcy_ok%=\n"
"s_add_u32 s52, s52, 1\n"
"s_cmp_lt_u32 s52, 0x40000\n"
"s_cbranch_scc1 Lcy_sl2%=\n"
"s_branch Lcy_ok%=\n"
"LkA_slow%=:\n"
"s_mov_b32 s52, 0\n"
"LkA_sl2%=:\n"
"buffer_load_dwordx2 v[54:55], v53, s[36:39], 0 offen sc1\n"
"s_waitcnt vmcnt(0)\n"
"v_cmp_eq_u64 vcc, s[82:83], v[54:55]\n"
"s_nop 0\n"
"s_bitcmp1_b32 vcc_lo, 0\n"
"s_cbranch_scc1 LkA_ok%=\n"
"s_add_u32 s52, s52, 1\n"
"s_cmp_lt_u32 s52, 0x40000\n"
"s_cbranch_scc1 LkA_sl2%=\n"
"s_branch LkA_ok%=\n"
"LxA_slow%=:\n"
"s_mov_b32 s52, 0\n"
"LxA_sl2%=:\n"
"buffer_load_dwordx2 v[54:55], v53, s[36:39], 0 offen sc1\n"
"s_waitcnt vmcnt(0)\n"
"v_cmp_eq_u64 vcc, s[82:83], v[54:55]\n"
"s_nop 0\n"
"s_cmp_eq_u64 vcc, exec\n"
"s_cbranch_scc1 LxA_ok%=\n"
"s_add_u32 s52, s52, 1\n"
"s_cmp_lt_u32 s52, 0x40000\n"
"s_cbranch_scc1 LxA_sl2%=\n"
"s_branch LxA_ok%=\n"
"LkB_slow%=:\n"
"s_mov_b32 s52, 0\n"
"LkB_sl2%=:\n"
"buffer_load_dwordx2 v[54:55], v53, s[36:39], 0 offen sc1\n"
"s_waitcnt vmcnt(0)\n"
"v_cmp_eq_u64 vcc, s[82:83], v[54:55]\n"
"s_nop 0\n"
"s_bitcmp1_b32 vcc_lo, 0\n"
"s_cbranch_scc1 LkB_ok%=\n"
"s_add_u32 s52, s52, 1\n"
"s_cmp_lt_u32 s52, 0x40000\n"
"s_cbranch_scc1 LkB_sl2%=\n"
"s_branch LkB_ok%=\n"
"LxB_slow%=:\n"
"s_mov_b32 s52, 0\n"
"LxB_sl2%=:\n"
"buffer_load_dwordx2 v[54:55], v53, s[36:39], 0 offen sc1\n"
"s_waitcnt vmcnt(0)\n"
"v_cmp_eq_u64 vcc, s[82:83], v[54:55]\n"
"s_nop 0\n"
"s_cmp_eq_u64 vcc, exec\n"
"s_cbranch_scc1 LxB_ok%=\n"
"s_add_u32 s52, s52, 1\n"
"s_cmp_lt_u32 s52, 0x40000\n"
"s_cbranch_scc1 LxB_sl2%=\n"
"s_branch LxB_ok%=\n"
"Lpri%=:\n"
"s_and_b32 s48, s19, 1\n"
"s_lshl_b32 s49, s48, 3\n"
"s_add_u32 s49, s49, 64\n"
"s_load_dwordx2 s[56:57], s[16:17], s49\n"
"s_load_dwordx2 s[58:59], s[16:17], 80\n"
"s_mul_i32 s62, s48, 46080\n"
"s_lshr_b32 s64, s34, 1\n"
"s_lshl_b32 s64, s64, 4\n"
"s_add_u32 s64, s64, s66\n"
"s_lshl_b32 s64, s64, 1\n"
"s_and_b32 s48, s34, 1\n"
"s_add_u32 s64, s64, s48\n"
"s_lshl_b32 s48, s64, 5\n"
"s_sub_u32 s48, s48, 8\n"
"s_mul_i32 s48, s48, 1152\n"
"s_lshr_b32 s49, s19, 1\n"
"s_lshl_b32 s50, s49, 7\n"
"s_add_u32 s48, s48, s50\n"
"v_lshrrev_b32 v190, 3, v0\n"
"v_mul_u32_u24 v190, 1152, v190\n"
"v_bfe_u32 v191, v0, 4, 1\n"
"v_lshlrev_b32 v191, 1, v191\n"
"v_and_b32 v192, 7, v0\n"
"v_xor_b32 v191, v191, v192\n"
"v_xor_b32 v192, 4, v191\n"
"v_lshl_add_u32 v191, v191, 4, v190\n"
"v_lshl_add_u32 v192, v192, 4, v190\n"
"v_add_u32 v184, s48, v191\n"
"s_add_u32 s48, s48, 9216\n"
"v_add_u32 v185, s48, v192\n"
"s_add_u32 s48, s48, 9216\n"
"v_add_u32 v186, s48, v191\n"
"s_add_u32 s48, s48, 9216\n"
"v_add_u32 v187, s48, v192\n"
"s_add_u32 s48, s48, 9216\n"
"v_add_u32 v188, s48, v191\n"
"s_mul_i32 s49, s49, 5120\n"
"s_add_u32 s62, s62, s49\n"
"s_add_u32 s62, s62, 32768\n"
"v_and_b32 v193, 15, v0\n"
"s_lshl_b32 s48, s19, 4\n"
"v_add_u32 v193, s48, v193\n"
"v_lshlrev_b32 v194, 3, v193\n"
"v_add_u32 v195, 4096, v194\n"
"s_waitcnt lgkmcnt(0)\n"
"s_mov_b32 s44, s56\n"
"s_and_b32 s45, s57, 0xffff\n"
"s_mov_b32 s46, 2359296\n"
"s_mov_b32 s47, 0x20000\n"
"s_mov_b32 s52, s58\n"
"s_and_b32 s53, s59, 0xffff\n"
"s_mov_b32 s54, 4608\n"
"s_mov_b32 s55, 0x20000\n"
"buffer_load_dwordx2 v[64:65], v194, s[52:55], 0 offen offset:0\n"
"buffer_load_dwordx2 v[66:67], v194, s[52:55], 0 offen offset:512\n"
"buffer_load_dwordx2 v[68:69], v194, s[52:55], 0 offen offset:1024\n"
"buffer_load_dwordx2 v[70:71], v194, s[52:55], 0 offen offset:1536\n"
"buffer_load_dwordx2 v[72:73], v194, s[52:55], 0 offen offset:2048\n"
"buffer_load_dwordx2 v[74:75], v194, s[52:55], 0 offen offset:2560\n"
"buffer_load_dwordx2 v[76:77], v194, s[52:55], 0 offen offset:3072\n"
"buffer_load_dwordx2 v[78:79], v194, s[52:55], 0 offen offset:3584\n"
"buffer_load_dwordx2 v[80:81], v195, s[52:55], 0 offen offset:0\n"
"s_mov_b64 s[92:93], s[44:45]\n"
"s_mov_b32 s63, s62\n"
"s_branch Lprb%=\n"
"Lprep%=:\n"
"s_mov_b64 s[88:89], s[92:93]\n"
"s_mov_b32 s90, 2359296\n"
"s_mov_b32 s91, 0x20000\n"
"s_add_u32 m0, s63, 0\n"
"s_movk_i32 s84, 0\n"
"buffer_load_dwordx4 v184, s[88:91], s84 offen lds\n"
"s_add_u32 m0, s63, 1024\n"
"s_movk_i32 s84, 0\n"
"buffer_load_dwordx4 v185, s[88:91], s84 offen lds\n"
"s_add_u32 m0, s63, 2048\n"
"s_movk_i32 s84, 0\n"
"buffer_load_dwordx4 v186, s[88:91], s84 offen lds\n"
"s_add_u32 m0, s63, 3072\n"
"s_movk_i32 s84, 0\n"
"buffer_load_dwordx4 v187, s[88:91], s84 offen lds\n"
"s_add_u32 m0, s63, 4096\n"
"s_movk_i32 s84, 0\n"
"buffer_load_dwordx4 v188, s[88:91], s84 offen lds\n"
"s_add_u32 m0, s63, 10240\n"
"s_movk_i32 s84, 256\n"
"buffer_load_dwordx4 v184, s[88:91], s84 offen lds\n"
"s_add_u32 m0, s63, 11264\n"
"s_movk_i32 s84, 256\n"
"buffer_load_dwordx4 v185, s[88:91], s84 offen lds\n"
"s_add_u32 m0, s63, 12288\n"
"s_movk_i32 s84, 256\n"
"buffer_load_dwordx4 v186, s[88:91], s84 offen lds\n"
"s_add_u32 m0, s63, 13312\n"
"s_movk_i32 s84, 256\n"
"buffer_load_dwordx4 v187, s[88:91], s84 offen lds\n"
"s_add_u32 m0, s63, 14336\n"
"s_movk_i32 s84, 256\n"
"buffer_load_dwordx4 v188, s[88:91], s84 offen lds\n"
"s_add_u32 m0, s63, 20480\n"
"s_movk_i32 s84, 512\n"
"buffer_load_dwordx4 v184, s[88:91], s84 offen lds\n"
"s_add_u32 m0, s63, 21504\n"
"s_movk_i32 s84, 512\n"
"buffer_load_dwordx4 v185, s[88:91], s84 offen lds\n"
"s_add_u32 m0, s63, 22528\n"
"s_movk_i32 s84, 512\n"
"buffer_load_dwordx4 v186, s[88:91], s84 offen lds\n"
"s_add_u32 m0, s63, 23552\n"
"s_movk_i32 s84, 512\n"
"buffer_load_dwordx4 v187, s[88:91], s84 offen lds\n"
"s_add_u32 m0, s63, 24576\n"
"s_movk_i32 s84, 512\n"
"buffer_load_dwordx4 v188, s[88:91], s84 offen lds\n"
"s_add_u32 m0, s63, 30720\n"
"s_movk_i32 s84, 768\n"
"buffer_load_dwordx4 v184, s[88:91], s84 offen lds\n"
"s_add_u32 m0, s63, 31744\n"
"s_movk_i32 s84, 768\n"
"buffer_load_dwordx4 v185, s[88:91], s84 offen lds\n"
"s_add_u32 m0, s63, 32768\n"
"s_movk_i32 s84, 768\n"
"buffer_load_dwordx4 v186, s[88:91], s84 offen lds\n"
"s_add_u32 m0, s63, 33792\n"
"s_movk_i32 s84, 768\n"
"buffer_load_dwordx4 v187, s[88:91], s84 offen lds\n"
"s_add_u32 m0, s63, 34816\n"
"s_movk_i32 s84, 768\n"
"buffer_load_dwordx4 v188, s[88:91], s84 offen lds\n"
"s_cmp_gt_u32 s19, 1\n"
"s_cbranch_scc1 Lpr4%=\n"
"s_add_u32 m0, s63, 40960\n"
"s_movk_i32 s84, 1024\n"
"buffer_load_dwordx4 v184, s[88:91], s84 offen lds\n"
"s_add_u32 m0, s63, 41984\n"
"s_movk_i32 s84, 1024\n"
"buffer_load_dwordx4 v185, s[88:91], s84 offen lds\n"
"s_add_u32 m0, s63, 43008\n"
"s_movk_i32 s84, 1024\n"
"buffer_load_dwordx4 v186, s[88:91], s84 offen lds\n"
"s_add_u32 m0, s63, 44032\n"
"s_movk_i32 s84, 1024\n"
"buffer_load_dwordx4 v187, s[88:91], s84 offen lds\n"
"s_add_u32 m0, s63, 45056\n"
"s_movk_i32 s84, 1024\n"
"buffer_load_dwordx4 v188, s[88:91], s84 offen lds\n"
"Lpr4%=:\n"
"s_lshr_b32 s69, s34, 1\n"
"s_lshl_b32 s69, s69, 4\n"
"s_add_u32 s69, s69, s66\n"
"s_lshl_b32 s69, s69, 1\n"
"s_and_b32 s71, s34, 1\n"
"s_add_u32 s69, s69, s71\n"
"v_lshrrev_b32 v190, 4, v0\n"
"v_bfe_u32 v191, v0, 2, 2\n"
"v_lshlrev_b32 v192, 10, v190\n"
"v_lshl_add_u32 v192, v191, 7, v192\n"
"v_and_b32 v195, 1, v0\n"
"v_lshl_add_u32 v192, v195, 3, v192\n"
"v_and_b32 v191, 2, v191\n"
"v_and_b32 v195, 1, v190\n"
"v_lshl_add_u32 v191, v195, 2, v191\n"
"v_bfe_u32 v195, v0, 1, 1\n"
"s_lshl_b32 s71, s19, 1\n"
"v_or_b32 v195, s71, v195\n"
"v_xor_b32 v195, v195, v191\n"
"v_lshl_add_u32 v82, v195, 4, v192\n"
"v_xor_b32 v83, 64, v82\n"
"v_add_u32 v82, 33280, v82\n"
"v_add_u32 v83, 33792, v83\n"
"s_lshr_b32 s71, s69, 1\n"
"s_and_b32 s71, s71, 2\n"
"v_xor_b32 v190, s71, v190\n"
"v_and_b32 v193, 7, v0\n"
"s_lshl_b32 s71, s19, 4\n"
"v_add_u32 v193, s71, v193\n"
"v_lshlrev_b32 v84, 13, v193\n"
"v_lshl_add_u32 v84, v190, 4, v84\n"
"v_and_b32 v193, 8, v0\n"
"v_lshl_add_u32 v84, v193, 3, v84\n"
"v_xor_b32 v85, 64, v84\n"
"v_add_u32 v85, 65536, v85\n"
"s_mov_b32 s86, 0x00ff00ff\n"
"s_mov_b32 s87, 0x00ff00ff\n"
"v_add_u32 v190, 46080, v82\n"
"v_add_u32 v191, 46080, v83\n"
"s_lshl_b32 s63, s69, 7\n"
"s_add_u32 s63, s63, 18874368\n"
"s_waitcnt vmcnt(10)\n"
"s_barrier\n"
"ds_read_b64_tr_b16 v[96:97], v82 offset:0\n"
"ds_read_b64_tr_b16 v[98:99], v83 offset:0\n"
"ds_read_b64_tr_b16 v[100:101], v83 offset:512\n"
"ds_read_b64_tr_b16 v[102:103], v190 offset:0\n"
"ds_read_b64_tr_b16 v[104:105], v191 offset:0\n"
"ds_read_b64_tr_b16 v[106:107], v191 offset:512\n"
"ds_read_b64_tr_b16 v[108:109], v82 offset:5120\n"
"ds_read_b64_tr_b16 v[110:111], v83 offset:5120\n"
"ds_read_b64_tr_b16 v[112:113], v83 offset:5632\n"
"ds_read_b64_tr_b16 v[114:115], v190 offset:5120\n"
"ds_read_b64_tr_b16 v[116:117], v191 offset:5120\n"
"ds_read_b64_tr_b16 v[118:119], v191 offset:5632\n"
"s_waitcnt lgkmcnt(6)\n"
"v_alignbit_b32 v126, v64, v64, 16\n"
"v_alignbit_b32 v127, v65, v65, 16\n"
"v_alignbit_b32 v120, v97, v96, 16\n"
"v_alignbit_b32 v121, v98, v97, 16\n"
"v_alignbit_b32 v122, v99, v98, 16\n"
"v_alignbit_b32 v123, v100, v99, 16\n"
"v_alignbit_b32 v124, v101, v100, 16\n"
"v_dot2_f32_bf16 v128, v96, v127, 0\n"
"v_dot2_f32_bf16 v129, v120, v127, 0\n"
"v_dot2_f32_bf16 v130, v97, v127, 0\n"
"v_dot2_f32_bf16 v131, v121, v127, 0\n"
"v_dot2_f32_bf16 v132, v98, v127, 0\n"
"v_dot2_f32_bf16 v133, v122, v127, 0\n"
"v_dot2_f32_bf16 v134, v99, v127, 0\n"
"v_dot2_f32_bf16 v135, v123, v127, 0\n"
"v_dot2_f32_bf16 v136, v100, v127, 0\n"
"v_and_b32 v138, 0xffff0000, v103\n"
"v_lshlrev_b32 v139, 16, v104\n"
"v_and_b32 v140, 0xffff0000, v104\n"
"v_lshlrev_b32 v141, 16, v105\n"
"v_and_b32 v142, 0xffff0000, v105\n"
"v_lshlrev_b32 v143, 16, v106\n"
"v_and_b32 v144, 0xffff0000, v106\n"
"v_lshlrev_b32 v145, 16, v107\n"
"v_and_b32 v146, 0xffff0000, v107\n"
"v_dot2c_f32_bf16 v128, v97, v126\n"
"v_dot2c_f32_bf16 v129, v121, v126\n"
"v_dot2c_f32_bf16 v130, v98, v126\n"
"v_dot2c_f32_bf16 v131, v122, v126\n"
"v_dot2c_f32_bf16 v132, v99, v126\n"
"v_dot2c_f32_bf16 v133, v123, v126\n"
"v_dot2c_f32_bf16 v134, v100, v126\n"
"v_dot2c_f32_bf16 v135, v124, v126\n"
"v_dot2c_f32_bf16 v136, v101, v126\n"
"s_mov_b64 vcc, s[86:87]\n"
"v_mul_f32 v128, v128, v138\n"
"v_mul_f32 v129, v129, v139\n"
"v_mul_f32 v130, v130, v140\n"
"v_mul_f32 v131, v131, v141\n"
"v_mul_f32 v132, v132, v142\n"
"v_mul_f32 v133, v133, v143\n"
"v_mul_f32 v134, v134, v144\n"
"v_mul_f32 v135, v135, v145\n"
"v_mul_f32 v136, v136, v146\n"
"v_cvt_pk_bf16_f32 v152, v136, v135\n"
"v_cvt_pk_bf16_f32 v153, v134, v133\n"
"v_cvt_pk_bf16_f32 v154, v132, v131\n"
"v_cvt_pk_bf16_f32 v155, v130, v129\n"
"v_cvt_pk_bf16_f32 v148, v135, v134\n"
"v_cvt_pk_bf16_f32 v149, v133, v132\n"
"v_cvt_pk_bf16_f32 v150, v131, v130\n"
"v_cvt_pk_bf16_f32 v151, v129, v128\n"
"v_cndmask_b32_dpp v200, v152, v148, vcc row_ror:8 row_mask:0xf bank_mask:0xf\n"
"v_cndmask_b32_dpp v201, v153, v149, vcc row_ror:8 row_mask:0xf bank_mask:0xf\n"
"v_cndmask_b32_dpp v202, v154, v150, vcc row_ror:8 row_mask:0xf bank_mask:0xf\n"
"v_cndmask_b32_dpp v203, v155, v151, vcc row_ror:8 row_mask:0xf bank_mask:0xf\n"
"s_not_b64 vcc, vcc\n"
"buffer_store_dwordx4 v[200:203], v84, s[36:39], s63 offen sc1\n"
"v_cndmask_b32_dpp v204, v152, v148, vcc row_ror:8 row_mask:0xf bank_mask:0xf\n"
"v_cndmask_b32_dpp v205, v153, v149, vcc row_ror:8 row_mask:0xf bank_mask:0xf\n"
"v_cndmask_b32_dpp v206, v154, v150, vcc row_ror:8 row_mask:0xf bank_mask:0xf\n"
"v_cndmask_b32_dpp v207, v155, v151, vcc row_ror:8 row_mask:0xf bank_mask:0xf\n"
"buffer_store_dwordx4 v[204:207], v85, s[36:39], s63 offen sc1\n"
"s_add_u32 s63, s63, 524288\n"
"ds_read_b64_tr_b16 v[96:97], v82 offset:10240\n"
"ds_read_b64_tr_b16 v[98:99], v83 offset:10240\n"
"ds_read_b64_tr_b16 v[100:101], v83 offset:10752\n"
"ds_read_b64_tr_b16 v[102:103], v190 offset:10240\n"
"ds_read_b64_tr_b16 v[104:105], v191 offset:10240\n"
"ds_read_b64_tr_b16 v[106:107], v191 offset:10752\n"
"s_waitcnt lgkmcnt(6)\n"
"v_alignbit_b32 v126, v66, v66, 16\n"
"v_alignbit_b32 v127, v67, v67, 16\n"
"v_alignbit_b32 v120, v109, v108, 16\n"
"v_alignbit_b32 v121, v110, v109, 16\n"
"v_alignbit_b32 v122, v111, v110, 16\n"
"v_alignbit_b32 v123, v112, v111, 16\n"
"v_alignbit_b32 v124, v113, v112, 16\n"
"v_dot2_f32_bf16 v128, v108, v127, 0\n"
"v_dot2_f32_bf16 v129, v120, v127, 0\n"
"v_dot2_f32_bf16 v130, v109, v127, 0\n"
"v_dot2_f32_bf16 v131, v121, v127, 0\n"
"v_dot2_f32_bf16 v132, v110, v127, 0\n"
"v_dot2_f32_bf16 v133, v122, v127, 0\n"
"v_dot2_f32_bf16 v134, v111, v127, 0\n"
"v_dot2_f32_bf16 v135, v123, v127, 0\n"
"v_dot2_f32_bf16 v136, v112, v127, 0\n"
"v_and_b32 v138, 0xffff0000, v115\n"
"v_lshlrev_b32 v139, 16, v116\n"
"v_and_b32 v140, 0xffff0000, v116\n"
"v_lshlrev_b32 v141, 16, v117\n"
"v_and_b32 v142, 0xffff0000, v117\n"
"v_lshlrev_b32 v143, 16, v118\n"
"v_and_b32 v144, 0xffff0000, v118\n"
"v_lshlrev_b32 v145, 16, v119\n"
"v_and_b32 v146, 0xffff0000, v119\n"
"v_dot2c_f32_bf16 v128, v109, v126\n"
"v_dot2c_f32_bf16 v129, v121, v126\n"
"v_dot2c_f32_bf16 v130, v110, v126\n"
"v_dot2c_f32_bf16 v131, v122, v126\n"
"v_dot2c_f32_bf16 v132, v111, v126\n"
"v_dot2c_f32_bf16 v133, v123, v126\n"
"v_dot2c_f32_bf16 v134, v112, v126\n"
"v_dot2c_f32_bf16 v135, v124, v126\n"
"v_dot2c_f32_bf16 v136, v113, v126\n"
"s_mov_b64 vcc, s[86:87]\n"
"v_mul_f32 v128, v128, v138\n"
"v_mul_f32 v129, v129, v139\n"
"v_mul_f32 v130, v130, v140\n"
"v_mul_f32 v131, v131, v141\n"
"v_mul_f32 v132, v132, v142\n"
"v_mul_f32 v133, v133, v143\n"
"v_mul_f32 v134, v134, v144\n"
"v_mul_f32 v135, v135, v145\n"
"v_mul_f32 v136, v136, v146\n"
"v_cvt_pk_bf16_f32 v152, v136, v135\n"
"v_cvt_pk_bf16_f32 v153, v134, v133\n"
"v_cvt_pk_bf16_f32 v154, v132, v131\n"
"v_cvt_pk_bf16_f32 v155, v130, v129\n"
"v_cvt_pk_bf16_f32 v148, v135, v134\n"
"v_cvt_pk_bf16_f32 v149, v133, v132\n"
"v_cvt_pk_bf16_f32 v150, v131, v130\n"
"v_cvt_pk_bf16_f32 v151, v129, v128\n"
"v_cndmask_b32_dpp v208, v152, v148, vcc row_ror:8 row_mask:0xf bank_mask:0xf\n"
"v_cndmask_b32_dpp v209, v153, v149, vcc row_ror:8 row_mask:0xf bank_mask:0xf\n"
"v_cndmask_b32_dpp v210, v154, v150, vcc row_ror:8 row_mask:0xf bank_mask:0xf\n"
"v_cndmask_b32_dpp v211, v155, v151, vcc row_ror:8 row_mask:0xf bank_mask:0xf\n"
"s_not_b64 vcc, vcc\n"
"buffer_store_dwordx4 v[208:211], v84, s[36:39], s63 offen sc1\n"
"v_cndmask_b32_dpp v212, v152, v148, vcc row_ror:8 row_mask:0xf bank_mask:0xf\n"
"v_cndmask_b32_dpp v213, v153, v149, vcc row_ror:8 row_mask:0xf bank_mask:0xf\n"
"v_cndmask_b32_dpp v214, v154, v150, vcc row_ror:8 row_mask:0xf bank_mask:0xf\n"
"v_cndmask_b32_dpp v215, v155, v151, vcc row_ror:8 row_mask:0xf bank_mask:0xf\n"
"buffer_store_dwordx4 v[212:215], v85, s[36:39], s63 offen sc1\n"
"s_add_u32 s63, s63, 524288\n"
"ds_read_b64_tr_b16 v[108:109], v82 offset:15360\n"
"ds_read_b64_tr_b16 v[110:111], v83 offset:15360\n"
"ds_read_b64_tr_b16 v[112:113], v83 offset:15872\n"
"ds_read_b64_tr_b16 v[114:115], v190 offset:15360\n"
"ds_read_b64_tr_b16 v[116:117], v191 offset:15360\n"
"ds_read_b64_tr_b16 v[118:119], v191 offset:15872\n"
"s_waitcnt lgkmcnt(6)\n"
"v_alignbit_b32 v126, v68, v68, 16\n"
"v_alignbit_b32 v127, v69, v69, 16\n"
"v_alignbit_b32 v120, v97, v96, 16\n"
"v_alignbit_b32 v121, v98, v97, 16\n"
"v_alignbit_b32 v122, v99, v98, 16\n"
"v_alignbit_b32 v123, v100, v99, 16\n"
"v_alignbit_b32 v124, v101, v100, 16\n"
"v_dot2_f32_bf16 v128, v96, v127, 0\n"
"v_dot2_f32_bf16 v129, v120, v127, 0\n"
"v_dot2_f32_bf16 v130, v97, v127, 0\n"
"v_dot2_f32_bf16 v131, v121, v127, 0\n"
"v_dot2_f32_bf16 v132, v98, v127, 0\n"
"v_dot2_f32_bf16 v133, v122, v127, 0\n"
"v_dot2_f32_bf16 v134, v99, v127, 0\n"
"v_dot2_f32_bf16 v135, v123, v127, 0\n"
"v_dot2_f32_bf16 v136, v100, v127, 0\n"
"v_and_b32 v138, 0xffff0000, v103\n"
"v_lshlrev_b32 v139, 16, v104\n"
"v_and_b32 v140, 0xffff0000, v104\n"
"v_lshlrev_b32 v141, 16, v105\n"
"v_and_b32 v142, 0xffff0000, v105\n"
"v_lshlrev_b32 v143, 16, v106\n"
"v_and_b32 v144, 0xffff0000, v106\n"
"v_lshlrev_b32 v145, 16, v107\n"
"v_and_b32 v146, 0xffff0000, v107\n"
"v_dot2c_f32_bf16 v128, v97, v126\n"
"v_dot2c_f32_bf16 v129, v121, v126\n"
"v_dot2c_f32_bf16 v130, v98, v126\n"
"v_dot2c_f32_bf16 v131, v122, v126\n"
"v_dot2c_f32_bf16 v132, v99, v126\n"
"v_dot2c_f32_bf16 v133, v123, v126\n"
"v_dot2c_f32_bf16 v134, v100, v126\n"
"v_dot2c_f32_bf16 v135, v124, v126\n"
"v_dot2c_f32_bf16 v136, v101, v126\n"
"s_mov_b64 vcc, s[86:87]\n"
"v_mul_f32 v128, v128, v138\n"
"v_mul_f32 v129, v129, v139\n"
"v_mul_f32 v130, v130, v140\n"
"v_mul_f32 v131, v131, v141\n"
"v_mul_f32 v132, v132, v142\n"
"v_mul_f32 v133, v133, v143\n"
"v_mul_f32 v134, v134, v144\n"
"v_mul_f32 v135, v135, v145\n"
"v_mul_f32 v136, v136, v146\n"
"v_cvt_pk_bf16_f32 v152, v136, v135\n"
"v_cvt_pk_bf16_f32 v153, v134, v133\n"
"v_cvt_pk_bf16_f32 v154, v132, v131\n"
"v_cvt_pk_bf16_f32 v155, v130, v129\n"
"v_cvt_pk_bf16_f32 v148, v135, v134\n"
"v_cvt_pk_bf16_f32 v149, v133, v132\n"
"v_cvt_pk_bf16_f32 v150, v131, v130\n"
"v_cvt_pk_bf16_f32 v151, v129, v128\n"
"v_cndmask_b32_dpp v200, v152, v148, vcc row_ror:8 row_mask:0xf bank_mask:0xf\n"
"v_cndmask_b32_dpp v201, v153, v149, vcc row_ror:8 row_mask:0xf bank_mask:0xf\n"
"v_cndmask_b32_dpp v202, v154, v150, vcc row_ror:8 row_mask:0xf bank_mask:0xf\n"
"v_cndmask_b32_dpp v203, v155, v151, vcc row_ror:8 row_mask:0xf bank_mask:0xf\n"
"s_not_b64 vcc, vcc\n"
"buffer_store_dwordx4 v[200:203], v84, s[36:39], s63 offen sc1\n"
"v_cndmask_b32_dpp v204, v152, v148, vcc row_ror:8 row_mask:0xf bank_mask:0xf\n"
"v_cndmask_b32_dpp v205, v153, v149, vcc row_ror:8 row_mask:0xf bank_mask:0xf\n"
"v_cndmask_b32_dpp v206, v154, v150, vcc row_ror:8 row_mask:0xf bank_mask:0xf\n"
"v_cndmask_b32_dpp v207, v155, v151, vcc row_ror:8 row_mask:0xf bank_mask:0xf\n"
"buffer_store_dwordx4 v[204:207], v85, s[36:39], s63 offen sc1\n"
"s_add_u32 s63, s63, 524288\n"
"s_waitcnt vmcnt(6)\n"
"s_barrier\n"
"ds_read_b64_tr_b16 v[96:97], v82 offset:20480\n"
"ds_read_b64_tr_b16 v[98:99], v83 offset:20480\n"
"ds_read_b64_tr_b16 v[100:101], v83 offset:20992\n"
"ds_read_b64_tr_b16 v[102:103], v190 offset:20480\n"
"ds_read_b64_tr_b16 v[104:105], v191 offset:20480\n"
"ds_read_b64_tr_b16 v[106:107], v191 offset:20992\n"
"s_waitcnt lgkmcnt(6)\n"
"v_alignbit_b32 v126, v70, v70, 16\n"
"v_alignbit_b32 v127, v71, v71, 16\n"
"v_alignbit_b32 v120, v109, v108, 16\n"
"v_alignbit_b32 v121, v110, v109, 16\n"
"v_alignbit_b32 v122, v111, v110, 16\n"
"v_alignbit_b32 v123, v112, v111, 16\n"
"v_alignbit_b32 v124, v113, v112, 16\n"
"v_dot2_f32_bf16 v128, v108, v127, 0\n"
"v_dot2_f32_bf16 v129, v120, v127, 0\n"
"v_dot2_f32_bf16 v130, v109, v127, 0\n"
"v_dot2_f32_bf16 v131, v121, v127, 0\n"
"v_dot2_f32_bf16 v132, v110, v127, 0\n"
"v_dot2_f32_bf16 v133, v122, v127, 0\n"
"v_dot2_f32_bf16 v134, v111, v127, 0\n"
"v_dot2_f32_bf16 v135, v123, v127, 0\n"
"v_dot2_f32_bf16 v136, v112, v127, 0\n"
"v_and_b32 v138, 0xffff0000, v115\n"
"v_lshlrev_b32 v139, 16, v116\n"
"v_and_b32 v140, 0xffff0000, v116\n"
"v_lshlrev_b32 v141, 16, v117\n"
"v_and_b32 v142, 0xffff0000, v117\n"
"v_lshlrev_b32 v143, 16, v118\n"
"v_and_b32 v144, 0xffff0000, v118\n"
"v_lshlrev_b32 v145, 16, v119\n"
"v_and_b32 v146, 0xffff0000, v119\n"
"v_dot2c_f32_bf16 v128, v109, v126\n"
"v_dot2c_f32_bf16 v129, v121, v126\n"
"v_dot2c_f32_bf16 v130, v110, v126\n"
"v_dot2c_f32_bf16 v131, v122, v126\n"
"v_dot2c_f32_bf16 v132, v111, v126\n"
"v_dot2c_f32_bf16 v133, v123, v126\n"
"v_dot2c_f32_bf16 v134, v112, v126\n"
"v_dot2c_f32_bf16 v135, v124, v126\n"
"v_dot2c_f32_bf16 v136, v113, v126\n"
"s_mov_b64 vcc, s[86:87]\n"
"v_mul_f32 v128, v128, v138\n"
"v_mul_f32 v129, v129, v139\n"
"v_mul_f32 v130, v130, v140\n"
"v_mul_f32 v131, v131, v141\n"
"v_mul_f32 v132, v132, v142\n"
"v_mul_f32 v133, v133, v143\n"
"v_mul_f32 v134, v134, v144\n"
"v_mul_f32 v135, v135, v145\n"
"v_mul_f32 v136, v136, v146\n"
"v_cvt_pk_bf16_f32 v152, v136, v135\n"
"v_cvt_pk_bf16_f32 v153, v134, v133\n"
"v_cvt_pk_bf16_f32 v154, v132, v131\n"
"v_cvt_pk_bf16_f32 v155, v130, v129\n"
"v_cvt_pk_bf16_f32 v148, v135, v134\n"
"v_cvt_pk_bf16_f32 v149, v133, v132\n"
"v_cvt_pk_bf16_f32 v150, v131, v130\n"
"v_cvt_pk_bf16_f32 v151, v129, v128\n"
"v_cndmask_b32_dpp v208, v152, v148, vcc row_ror:8 row_mask:0xf bank_mask:0xf\n"
"v_cndmask_b32_dpp v209, v153, v149, vcc row_ror:8 row_mask:0xf bank_mask:0xf\n"
"v_cndmask_b32_dpp v210, v154, v150, vcc row_ror:8 row_mask:0xf bank_mask:0xf\n"
"v_cndmask_b32_dpp v211, v155, v151, vcc row_ror:8 row_mask:0xf bank_mask:0xf\n"
"s_not_b64 vcc, vcc\n"
"buffer_store_dwordx4 v[208:211], v84, s[36:39], s63 offen sc1\n"
"v_cndmask_b32_dpp v212, v152, v148, vcc row_ror:8 row_mask:0xf bank_mask:0xf\n"
"v_cndmask_b32_dpp v213, v153, v149, vcc row_ror:8 row_mask:0xf bank_mask:0xf\n"
"v_cndmask_b32_dpp v214, v154, v150, vcc row_ror:8 row_mask:0xf bank_mask:0xf\n"
"v_cndmask_b32_dpp v215, v155, v151, vcc row_ror:8 row_mask:0xf bank_mask:0xf\n"
"buffer_store_dwordx4 v[212:215], v85, s[36:39], s63 offen sc1\n"
"s_add_u32 s63, s63, 524288\n"
"ds_read_b64_tr_b16 v[108:109], v82 offset:25600\n"
"ds_read_b64_tr_b16 v[110:111], v83 offset:25600\n"
"ds_read_b64_tr_b16 v[112:113], v83 offset:26112\n"
"ds_read_b64_tr_b16 v[114:115], v190 offset:25600\n"
"ds_read_b64_tr_b16 v[116:117], v191 offset:25600\n"
"ds_read_b64_tr_b16 v[118:119], v191 offset:26112\n"
"s_waitcnt lgkmcnt(6)\n"
"v_alignbit_b32 v126, v72, v72, 16\n"
"v_alignbit_b32 v127, v73, v73, 16\n"
"v_alignbit_b32 v120, v97, v96, 16\n"
"v_alignbit_b32 v121, v98, v97, 16\n"
"v_alignbit_b32 v122, v99, v98, 16\n"
"v_alignbit_b32 v123, v100, v99, 16\n"
"v_alignbit_b32 v124, v101, v100, 16\n"
"v_dot2_f32_bf16 v128, v96, v127, 0\n"
"v_dot2_f32_bf16 v129, v120, v127, 0\n"
"v_dot2_f32_bf16 v130, v97, v127, 0\n"
"v_dot2_f32_bf16 v131, v121, v127, 0\n"
"v_dot2_f32_bf16 v132, v98, v127, 0\n"
"v_dot2_f32_bf16 v133, v122, v127, 0\n"
"v_dot2_f32_bf16 v134, v99, v127, 0\n"
"v_dot2_f32_bf16 v135, v123, v127, 0\n"
"v_dot2_f32_bf16 v136, v100, v127, 0\n"
"v_and_b32 v138, 0xffff0000, v103\n"
"v_lshlrev_b32 v139, 16, v104\n"
"v_and_b32 v140, 0xffff0000, v104\n"
"v_lshlrev_b32 v141, 16, v105\n"
"v_and_b32 v142, 0xffff0000, v105\n"
"v_lshlrev_b32 v143, 16, v106\n"
"v_and_b32 v144, 0xffff0000, v106\n"
"v_lshlrev_b32 v145, 16, v107\n"
"v_and_b32 v146, 0xffff0000, v107\n"
"v_dot2c_f32_bf16 v128, v97, v126\n"
"v_dot2c_f32_bf16 v129, v121, v126\n"
"v_dot2c_f32_bf16 v130, v98, v126\n"
"v_dot2c_f32_bf16 v131, v122, v126\n"
"v_dot2c_f32_bf16 v132, v99, v126\n"
"v_dot2c_f32_bf16 v133, v123, v126\n"
"v_dot2c_f32_bf16 v134, v100, v126\n"
"v_dot2c_f32_bf16 v135, v124, v126\n"
"v_dot2c_f32_bf16 v136, v101, v126\n"
"s_mov_b64 vcc, s[86:87]\n"
"v_mul_f32 v128, v128, v138\n"
"v_mul_f32 v129, v129, v139\n"
"v_mul_f32 v130, v130, v140\n"
"v_mul_f32 v131, v131, v141\n"
"v_mul_f32 v132, v132, v142\n"
"v_mul_f32 v133, v133, v143\n"
"v_mul_f32 v134, v134, v144\n"
"v_mul_f32 v135, v135, v145\n"
"v_mul_f32 v136, v136, v146\n"
"v_cvt_pk_bf16_f32 v152, v136, v135\n"
"v_cvt_pk_bf16_f32 v153, v134, v133\n"
"v_cvt_pk_bf16_f32 v154, v132, v131\n"
"v_cvt_pk_bf16_f32 v155, v130, v129\n"
"v_cvt_pk_bf16_f32 v148, v135, v134\n"
"v_cvt_pk_bf16_f32 v149, v133, v132\n"
"v_cvt_pk_bf16_f32 v150, v131, v130\n"
"v_cvt_pk_bf16_f32 v151, v129, v128\n"
"v_cndmask_b32_dpp v200, v152, v148, vcc row_ror:8 row_mask:0xf bank_mask:0xf\n"
"v_cndmask_b32_dpp v201, v153, v149, vcc row_ror:8 row_mask:0xf bank_mask:0xf\n"
"v_cndmask_b32_dpp v202, v154, v150, vcc row_ror:8 row_mask:0xf bank_mask:0xf\n"
"v_cndmask_b32_dpp v203, v155, v151, vcc row_ror:8 row_mask:0xf bank_mask:0xf\n"
"s_not_b64 vcc, vcc\n"
"buffer_store_dwordx4 v[200:203], v84, s[36:39], s63 offen sc1\n"
"v_cndmask_b32_dpp v204, v152, v148, vcc row_ror:8 row_mask:0xf bank_mask:0xf\n"
"v_cndmask_b32_dpp v205, v153, v149, vcc row_ror:8 row_mask:0xf bank_mask:0xf\n"
"v_cndmask_b32_dpp v206, v154, v150, vcc row_ror:8 row_mask:0xf bank_mask:0xf\n"
"v_cndmask_b32_dpp v207, v155, v151, vcc row_ror:8 row_mask:0xf bank_mask:0xf\n"
"buffer_store_dwordx4 v[204:207], v85, s[36:39], s63 offen sc1\n"
"s_add_u32 s63, s63, 524288\n"
"ds_read_b64_tr_b16 v[96:97], v82 offset:30720\n"
"ds_read_b64_tr_b16 v[98:99], v83 offset:30720\n"
"ds_read_b64_tr_b16 v[100:101], v83 offset:31232\n"
"ds_read_b64_tr_b16 v[102:103], v190 offset:30720\n"
"ds_read_b64_tr_b16 v[104:105], v191 offset:30720\n"
"ds_read_b64_tr_b16 v[106:107], v191 offset:31232\n"
"s_waitcnt lgkmcnt(6)\n"
"v_alignbit_b32 v126, v74, v74, 16\n"
"v_alignbit_b32 v127, v75, v75, 16\n"
"v_alignbit_b32 v120, v109, v108, 16\n"
"v_alignbit_b32 v121, v110, v109, 16\n"
"v_alignbit_b32 v122, v111, v110, 16\n"
"v_alignbit_b32 v123, v112, v111, 16\n"
"v_alignbit_b32 v124, v113, v112, 16\n"
"v_dot2_f32_bf16 v128, v108, v127, 0\n"
"v_dot2_f32_bf16 v129, v120, v127, 0\n"
"v_dot2_f32_bf16 v130, v109, v127, 0\n"
"v_dot2_f32_bf16 v131, v121, v127, 0\n"
"v_dot2_f32_bf16 v132, v110, v127, 0\n"
"v_dot2_f32_bf16 v133, v122, v127, 0\n"
"v_dot2_f32_bf16 v134, v111, v127, 0\n"
"v_dot2_f32_bf16 v135, v123, v127, 0\n"
"v_dot2_f32_bf16 v136, v112, v127, 0\n"
"v_and_b32 v138, 0xffff0000, v115\n"
"v_lshlrev_b32 v139, 16, v116\n"
"v_and_b32 v140, 0xffff0000, v116\n"
"v_lshlrev_b32 v141, 16, v117\n"
"v_and_b32 v142, 0xffff0000, v117\n"
"v_lshlrev_b32 v143, 16, v118\n"
"v_and_b32 v144, 0xffff0000, v118\n"
"v_lshlrev_b32 v145, 16, v119\n"
"v_and_b32 v146, 0xffff0000, v119\n"
"v_dot2c_f32_bf16 v128, v109, v126\n"
"v_dot2c_f32_bf16 v129, v121, v126\n"
"v_dot2c_f32_bf16 v130, v110, v126\n"
"v_dot2c_f32_bf16 v131, v122, v126\n"
"v_dot2c_f32_bf16 v132, v111, v126\n"
"v_dot2c_f32_bf16 v133, v123, v126\n"
"v_dot2c_f32_bf16 v134, v112, v126\n"
"v_dot2c_f32_bf16 v135, v124, v126\n"
"v_dot2c_f32_bf16 v136, v113, v126\n"
"s_mov_b64 vcc, s[86:87]\n"
"v_mul_f32 v128, v128, v138\n"
"v_mul_f32 v129, v129, v139\n"
"v_mul_f32 v130, v130, v140\n"
"v_mul_f32 v131, v131, v141\n"
"v_mul_f32 v132, v132, v142\n"
"v_mul_f32 v133, v133, v143\n"
"v_mul_f32 v134, v134, v144\n"
"v_mul_f32 v135, v135, v145\n"
"v_mul_f32 v136, v136, v146\n"
"v_cvt_pk_bf16_f32 v152, v136, v135\n"
"v_cvt_pk_bf16_f32 v153, v134, v133\n"
"v_cvt_pk_bf16_f32 v154, v132, v131\n"
"v_cvt_pk_bf16_f32 v155, v130, v129\n"
"v_cvt_pk_bf16_f32 v148, v135, v134\n"
"v_cvt_pk_bf16_f32 v149, v133, v132\n"
"v_cvt_pk_bf16_f32 v150, v131, v130\n"
"v_cvt_pk_bf16_f32 v151, v129, v128\n"
"v_cndmask_b32_dpp v208, v152, v148, vcc row_ror:8 row_mask:0xf bank_mask:0xf\n"
"v_cndmask_b32_dpp v209, v153, v149, vcc row_ror:8 row_mask:0xf bank_mask:0xf\n"
"v_cndmask_b32_dpp v210, v154, v150, vcc row_ror:8 row_mask:0xf bank_mask:0xf\n"
"v_cndmask_b32_dpp v211, v155, v151, vcc row_ror:8 row_mask:0xf bank_mask:0xf\n"
"s_not_b64 vcc, vcc\n"
"buffer_store_dwordx4 v[208:211], v84, s[36:39], s63 offen sc1\n"
"v_cndmask_b32_dpp v212, v152, v148, vcc row_ror:8 row_mask:0xf bank_mask:0xf\n"
"v_cndmask_b32_dpp v213, v153, v149, vcc row_ror:8 row_mask:0xf bank_mask:0xf\n"
"v_cndmask_b32_dpp v214, v154, v150, vcc row_ror:8 row_mask:0xf bank_mask:0xf\n"
"v_cndmask_b32_dpp v215, v155, v151, vcc row_ror:8 row_mask:0xf bank_mask:0xf\n"
"buffer_store_dwordx4 v[212:215], v85, s[36:39], s63 offen sc1\n"
"s_add_u32 s63, s63, 524288\n"
"s_add_u32 m0, s62, 49152\n"
"s_nop 0\n"
"buffer_load_dwordx4 v56, s[20:23], s52 offen offset:0 lds\n"
"buffer_load_dwordx4 v57, s[20:23], s52 offen offset:1024 lds\n"
"buffer_load_dwordx4 v58, s[20:23], s52 offen offset:2048 lds\n"
"buffer_load_dwordx4 v59, s[20:23], s52 offen offset:3072 lds\n"
"ds_read_b64_tr_b16 v[108:109], v82 offset:35840\n"
"ds_read_b64_tr_b16 v[110:111], v83 offset:35840\n"
"ds_read_b64_tr_b16 v[112:113], v83 offset:36352\n"
"ds_read_b64_tr_b16 v[114:115], v190 offset:35840\n"
"ds_read_b64_tr_b16 v[116:117], v191 offset:35840\n"
"ds_read_b64_tr_b16 v[118:119], v191 offset:36352\n"
"s_waitcnt lgkmcnt(6)\n"
"v_alignbit_b32 v126, v76, v76, 16\n"
"v_alignbit_b32 v127, v77, v77, 16\n"
"v_alignbit_b32 v120, v97, v96, 16\n"
"v_alignbit_b32 v121, v98, v97, 16\n"
"v_alignbit_b32 v122, v99, v98, 16\n"
"v_alignbit_b32 v123, v100, v99, 16\n"
"v_alignbit_b32 v124, v101, v100, 16\n"
"v_dot2_f32_bf16 v128, v96, v127, 0\n"
"v_dot2_f32_bf16 v129, v120, v127, 0\n"
"v_dot2_f32_bf16 v130, v97, v127, 0\n"
"v_dot2_f32_bf16 v131, v121, v127, 0\n"
"v_dot2_f32_bf16 v132, v98, v127, 0\n"
"v_dot2_f32_bf16 v133, v122, v127, 0\n"
"v_dot2_f32_bf16 v134, v99, v127, 0\n"
"v_dot2_f32_bf16 v135, v123, v127, 0\n"
"v_dot2_f32_bf16 v136, v100, v127, 0\n"
"v_and_b32 v138, 0xffff0000, v103\n"
"v_lshlrev_b32 v139, 16, v104\n"
"v_and_b32 v140, 0xffff0000, v104\n"
"v_lshlrev_b32 v141, 16, v105\n"
"v_and_b32 v142, 0xffff0000, v105\n"
"v_lshlrev_b32 v143, 16, v106\n"
"v_and_b32 v144, 0xffff0000, v106\n"
"v_lshlrev_b32 v145, 16, v107\n"
"v_and_b32 v146, 0xffff0000, v107\n"
"v_dot2c_f32_bf16 v128, v97, v126\n"
"v_dot2c_f32_bf16 v129, v121, v126\n"
"v_dot2c_f32_bf16 v130, v98, v126\n"
"v_dot2c_f32_bf16 v131, v122, v126\n"
"v_dot2c_f32_bf16 v132, v99, v126\n"
"v_dot2c_f32_bf16 v133, v123, v126\n"
"v_dot2c_f32_bf16 v134, v100, v126\n"
"v_dot2c_f32_bf16 v135, v124, v126\n"
"v_dot2c_f32_bf16 v136, v101, v126\n"
"s_mov_b64 vcc, s[86:87]\n"
"v_mul_f32 v128, v128, v138\n"
"v_mul_f32 v129, v129, v139\n"
"v_mul_f32 v130, v130, v140\n"
"v_mul_f32 v131, v131, v141\n"
"v_mul_f32 v132, v132, v142\n"
"v_mul_f32 v133, v133, v143\n"
"v_mul_f32 v134, v134, v144\n"
"v_mul_f32 v135, v135, v145\n"
"v_mul_f32 v136, v136, v146\n"
"v_cvt_pk_bf16_f32 v152, v136, v135\n"
"v_cvt_pk_bf16_f32 v153, v134, v133\n"
"v_cvt_pk_bf16_f32 v154, v132, v131\n"
"v_cvt_pk_bf16_f32 v155, v130, v129\n"
"v_cvt_pk_bf16_f32 v148, v135, v134\n"
"v_cvt_pk_bf16_f32 v149, v133, v132\n"
"v_cvt_pk_bf16_f32 v150, v131, v130\n"
"v_cvt_pk_bf16_f32 v151, v129, v128\n"
"v_cndmask_b32_dpp v200, v152, v148, vcc row_ror:8 row_mask:0xf bank_mask:0xf\n"
"v_cndmask_b32_dpp v201, v153, v149, vcc row_ror:8 row_mask:0xf bank_mask:0xf\n"
"v_cndmask_b32_dpp v202, v154, v150, vcc row_ror:8 row_mask:0xf bank_mask:0xf\n"
"v_cndmask_b32_dpp v203, v155, v151, vcc row_ror:8 row_mask:0xf bank_mask:0xf\n"
"s_not_b64 vcc, vcc\n"
"buffer_store_dwordx4 v[200:203], v84, s[36:39], s63 offen sc1\n"
"v_cndmask_b32_dpp v204, v152, v148, vcc row_ror:8 row_mask:0xf bank_mask:0xf\n"
"v_cndmask_b32_dpp v205, v153, v149, vcc row_ror:8 row_mask:0xf bank_mask:0xf\n"
"v_cndmask_b32_dpp v206, v154, v150, vcc row_ror:8 row_mask:0xf bank_mask:0xf\n"
"v_cndmask_b32_dpp v207, v155, v151, vcc row_ror:8 row_mask:0xf bank_mask:0xf\n"
"buffer_store_dwordx4 v[204:207], v85, s[36:39], s63 offen sc1\n"
"s_add_u32 s63, s63, 524288\n"
"s_add_u32 m0, s61, 0\n"
"s_add_u32 s51, s40, s49\n"
"buffer_load_dwordx4 v10, s[24:27], s51 offen lds\n"
"buffer_load_dwordx4 v10, s[28:31], s51 offen offset:1024 lds\n"
"buffer_load_dwordx4 v11, s[24:27], s51 offen offset:2048 lds\n"
"buffer_load_dwordx4 v11, s[28:31], s51 offen offset:3072 lds\n"
"ds_read_b64_tr_b16 v[96:97], v82 offset:40960\n"
"ds_read_b64_tr_b16 v[98:99], v83 offset:40960\n"
"ds_read_b64_tr_b16 v[100:101], v83 offset:41472\n"
"ds_read_b64_tr_b16 v[102:103], v190 offset:40960\n"
"ds_read_b64_tr_b16 v[104:105], v191 offset:40960\n"
"ds_read_b64_tr_b16 v[106:107], v191 offset:41472\n"
"s_waitcnt lgkmcnt(6)\n"
"v_alignbit_b32 v126, v78, v78, 16\n"
"v_alignbit_b32 v127, v79, v79, 16\n"
"v_alignbit_b32 v120, v109, v108, 16\n"
"v_alignbit_b32 v121, v110, v109, 16\n"
"v_alignbit_b32 v122, v111, v110, 16\n"
"v_alignbit_b32 v123, v112, v111, 16\n"
"v_alignbit_b32 v124, v113, v112, 16\n"
"v_dot2_f32_bf16 v128, v108, v127, 0\n"
"v_dot2_f32_bf16 v129, v120, v127, 0\n"
"v_dot2_f32_bf16 v130, v109, v127, 0\n"
"v_dot2_f32_bf16 v131, v121, v127, 0\n"
"v_dot2_f32_bf16 v132, v110, v127, 0\n"
"v_dot2_f32_bf16 v133, v122, v127, 0\n"
"v_dot2_f32_bf16 v134, v111, v127, 0\n"
"v_dot2_f32_bf16 v135, v123, v127, 0\n"
"v_dot2_f32_bf16 v136, v112, v127, 0\n"
"v_and_b32 v138, 0xffff0000, v115\n"
"v_lshlrev_b32 v139, 16, v116\n"
"v_and_b32 v140, 0xffff0000, v116\n"
"v_lshlrev_b32 v141, 16, v117\n"
"v_and_b32 v142, 0xffff0000, v117\n"
"v_lshlrev_b32 v143, 16, v118\n"
"v_and_b32 v144, 0xffff0000, v118\n"
"v_lshlrev_b32 v145, 16, v119\n"
"v_and_b32 v146, 0xffff0000, v119\n"
"v_dot2c_f32_bf16 v128, v109, v126\n"
"v_dot2c_f32_bf16 v129, v121, v126\n"
"v_dot2c_f32_bf16 v130, v110, v126\n"
"v_dot2c_f32_bf16 v131, v122, v126\n"
"v_dot2c_f32_bf16 v132, v111, v126\n"
"v_dot2c_f32_bf16 v133, v123, v126\n"
"v_dot2c_f32_bf16 v134, v112, v126\n"
"v_dot2c_f32_bf16 v135, v124, v126\n"
"v_dot2c_f32_bf16 v136, v113, v126\n"
"s_mov_b64 vcc, s[86:87]\n"
"v_mul_f32 v128, v128, v138\n"
"v_mul_f32 v129, v129, v139\n"
"v_mul_f32 v130, v130, v140\n"
"v_mul_f32 v131, v131, v141\n"
"v_mul_f32 v132, v132, v142\n"
"v_mul_f32 v133, v133, v143\n"
"v_mul_f32 v134, v134, v144\n"
"v_mul_f32 v135, v135, v145\n"
"v_mul_f32 v136, v136, v146\n"
"v_cvt_pk_bf16_f32 v152, v136, v135\n"
"v_cvt_pk_bf16_f32 v153, v134, v133\n"
"v_cvt_pk_bf16_f32 v154, v132, v131\n"
"v_cvt_pk_bf16_f32 v155, v130, v129\n"
"v_cvt_pk_bf16_f32 v148, v135, v134\n"
"v_cvt_pk_bf16_f32 v149, v133, v132\n"
"v_cvt_pk_bf16_f32 v150, v131, v130\n"
"v_cvt_pk_bf16_f32 v151, v129, v128\n"
"v_cndmask_b32_dpp v208, v152, v148, vcc row_ror:8 row_mask:0xf bank_mask:0xf\n"
"v_cndmask_b32_dpp v209, v153, v149, vcc row_ror:8 row_mask:0xf bank_mask:0xf\n"
"v_cndmask_b32_dpp v210, v154, v150, vcc row_ror:8 row_mask:0xf bank_mask:0xf\n"
"v_cndmask_b32_dpp v211, v155, v151, vcc row_ror:8 row_mask:0xf bank_mask:0xf\n"
"s_not_b64 vcc, vcc\n"
"buffer_store_dwordx4 v[208:211], v84, s[36:39], s63 offen sc1\n"
"v_cndmask_b32_dpp v212, v152, v148, vcc row_ror:8 row_mask:0xf bank_mask:0xf\n"
"v_cndmask_b32_dpp v213, v153, v149, vcc row_ror:8 row_mask:0xf bank_mask:0xf\n"
"v_cndmask_b32_dpp v214, v154, v150, vcc row_ror:8 row_mask:0xf bank_mask:0xf\n"
"v_cndmask_b32_dpp v215, v155, v151, vcc row_ror:8 row_mask:0xf bank_mask:0xf\n"
"buffer_store_dwordx4 v[212:215], v85, s[36:39], s63 offen sc1\n"
"s_add_u32 s63, s63, 524288\n"
"s_add_u32 m0, s62, 32768\n"
"s_nop 0\n"
"buffer_load_dwordx4 v56, s[20:23], s53 offen offset:0 lds\n"
"buffer_load_dwordx4 v57, s[20:23], s53 offen offset:1024 lds\n"
"buffer_load_dwordx4 v58, s[20:23], s53 offen offset:2048 lds\n"
"buffer_load_dwordx4 v59, s[20:23], s53 offen offset:3072 lds\n"
"s_waitcnt lgkmcnt(0)\n"
"v_alignbit_b32 v126, v80, v80, 16\n"
"v_alignbit_b32 v127, v81, v81, 16\n"
"v_alignbit_b32 v120, v97, v96, 16\n"
"v_alignbit_b32 v121, v98, v97, 16\n"
"v_alignbit_b32 v122, v99, v98, 16\n"
"v_alignbit_b32 v123, v100, v99, 16\n"
"v_alignbit_b32 v124, v101, v100, 16\n"
"v_dot2_f32_bf16 v128, v96, v127, 0\n"
"v_dot2_f32_bf16 v129, v120, v127, 0\n"
"v_dot2_f32_bf16 v130, v97, v127, 0\n"
"v_dot2_f32_bf16 v131, v121, v127, 0\n"
"v_dot2_f32_bf16 v132, v98, v127, 0\n"
"v_dot2_f32_bf16 v133, v122, v127, 0\n"
"v_dot2_f32_bf16 v134, v99, v127, 0\n"
"v_dot2_f32_bf16 v135, v123, v127, 0\n"
"v_dot2_f32_bf16 v136, v100, v127, 0\n"
"v_and_b32 v138, 0xffff0000, v103\n"
"v_lshlrev_b32 v139, 16, v104\n"
"v_and_b32 v140, 0xffff0000, v104\n"
"v_lshlrev_b32 v141, 16, v105\n"
"v_and_b32 v142, 0xffff0000, v105\n"
"v_lshlrev_b32 v143, 16, v106\n"
"v_and_b32 v144, 0xffff0000, v106\n"
"v_lshlrev_b32 v145, 16, v107\n"
"v_and_b32 v146, 0xffff0000, v107\n"
"v_dot2c_f32_bf16 v128, v97, v126\n"
"v_dot2c_f32_bf16 v129, v121, v126\n"
"v_dot2c_f32_bf16 v130, v98, v126\n"
"v_dot2c_f32_bf16 v131, v122, v126\n"
"v_dot2c_f32_bf16 v132, v99, v126\n"
"v_dot2c_f32_bf16 v133, v123, v126\n"
"v_dot2c_f32_bf16 v134, v100, v126\n"
"v_dot2c_f32_bf16 v135, v124, v126\n"
"v_dot2c_f32_bf16 v136, v101, v126\n"
"s_mov_b64 vcc, s[86:87]\n"
"v_mul_f32 v128, v128, v138\n"
"v_mul_f32 v129, v129, v139\n"
"v_mul_f32 v130, v130, v140\n"
"v_mul_f32 v131, v131, v141\n"
"v_mul_f32 v132, v132, v142\n"
"v_mul_f32 v133, v133, v143\n"
"v_mul_f32 v134, v134, v144\n"
"v_mul_f32 v135, v135, v145\n"
"v_mul_f32 v136, v136, v146\n"
"v_cvt_pk_bf16_f32 v152, v136, v135\n"
"v_cvt_pk_bf16_f32 v153, v134, v133\n"
"v_cvt_pk_bf16_f32 v154, v132, v131\n"
"v_cvt_pk_bf16_f32 v155, v130, v129\n"
"v_cvt_pk_bf16_f32 v148, v135, v134\n"
"v_cvt_pk_bf16_f32 v149, v133, v132\n"
"v_cvt_pk_bf16_f32 v150, v131, v130\n"
"v_cvt_pk_bf16_f32 v151, v129, v128\n"
"v_cndmask_b32_dpp v200, v152, v148, vcc row_ror:8 row_mask:0xf bank_mask:0xf\n"
"v_cndmask_b32_dpp v201, v153, v149, vcc row_ror:8 row_mask:0xf bank_mask:0xf\n"
"v_cndmask_b32_dpp v202, v154, v150, vcc row_ror:8 row_mask:0xf bank_mask:0xf\n"
"v_cndmask_b32_dpp v203, v155, v151, vcc row_ror:8 row_mask:0xf bank_mask:0xf\n"
"s_not_b64 vcc, vcc\n"
"buffer_store_dwordx4 v[200:203], v84, s[36:39], s63 offen sc1\n"
"v_cndmask_b32_dpp v204, v152, v148, vcc row_ror:8 row_mask:0xf bank_mask:0xf\n"
"v_cndmask_b32_dpp v205, v153, v149, vcc row_ror:8 row_mask:0xf bank_mask:0xf\n"
"v_cndmask_b32_dpp v206, v154, v150, vcc row_ror:8 row_mask:0xf bank_mask:0xf\n"
"v_cndmask_b32_dpp v207, v155, v151, vcc row_ror:8 row_mask:0xf bank_mask:0xf\n"
"buffer_store_dwordx4 v[204:207], v85, s[36:39], s63 offen sc1\n"
"s_add_u32 m0, s61, 16384\n"
"s_add_u32 s51, s41, s49\n"
"buffer_load_dwordx4 v10, s[24:27], s51 offen lds\n"
"buffer_load_dwordx4 v10, s[28:31], s51 offen offset:1024 lds\n"
"buffer_load_dwordx4 v11, s[24:27], s51 offen offset:2048 lds\n"
"buffer_load_dwordx4 v11, s[28:31], s51 offen offset:3072 lds\n"
"s_lshl_b32 s63, s69, 3\n"
"s_add_u32 s76, s63, 33064960\n"
"s_branch Lp1go%=\n"
"Lcpro%=:\n"
"v_mov_b32 v4, 0\n"
"v_mov_b32 v5, 0\n"
"v_mov_b32 v6, 0\n"
"v_mov_b32 v7, 0\n"
"v_lshlrev_b32 v8, 4, v0\n"
"s_sub_u32 s58, s54, 1024\n"
"v_add_u32 v8, s58, v8\n"
"s_mov_b32 s50, s71\n"
"v_and_b32 v65, 3, v0\n"
"v_mul_u32_u24 v65, 4160, v65\n"
"v_bfe_u32 v66, v0, 2, 2\n"
"v_lshrrev_b32 v67, 4, v0\n"
"v_sub_u32 v66, v66, v67\n"
"v_lshl_add_u32 v65, v66, 4, v65\n"
"s_lshl_b32 s58, s19, 8\n"
"s_add_u32 s58, s58, 48\n"
"v_add_u32 v16, s58, v65\n"
"v_and_b32 v65, 15, v0\n"
"s_lshl_b32 s58, s19, 2\n"
"s_add_u32 s58, s58, 0\n"
"v_subrev_u32 v66, s58, v65\n"
"v_lshrrev_b32 v17, 1, v66\n"
"v_and_b32 v17, 2, v17\n"
"v_xor_b32 v17, v17, v67\n"
"v_lshlrev_b32 v17, 4, v17\n"
"v_lshl_add_u32 v17, v66, 6, v17\n"
"v_add_u32 v17, 17664, v17\n"
"s_lshl_b32 s58, s19, 2\n"
"s_add_u32 s58, s58, 1\n"
"v_subrev_u32 v66, s58, v65\n"
"v_lshrrev_b32 v18, 1, v66\n"
"v_and_b32 v18, 2, v18\n"
"v_xor_b32 v18, v18, v67\n"
"v_lshlrev_b32 v18, 4, v18\n"
"v_lshl_add_u32 v18, v66, 6, v18\n"
"v_add_u32 v18, 17664, v18\n"
"s_lshl_b32 s58, s19, 2\n"
"s_add_u32 s58, s58, 2\n"
"v_subrev_u32 v66, s58, v65\n"
"v_lshrrev_b32 v19, 1, v66\n"
"v_and_b32 v19, 2, v19\n"
"v_xor_b32 v19, v19, v67\n"
"v_lshlrev_b32 v19, 4, v19\n"
"v_lshl_add_u32 v19, v66, 6, v19\n"
"v_add_u32 v19, 17664, v19\n"
"s_lshl_b32 s58, s19, 2\n"
"s_add_u32 s58, s58, 3\n"
"v_subrev_u32 v66, s58, v65\n"
"v_lshrrev_b32 v20, 1, v66\n"
"v_and_b32 v20, 2, v20\n"
"v_xor_b32 v20, v20, v67\n"
"v_lshlrev_b32 v20, 4, v20\n"
"v_lshl_add_u32 v20, v66, 6, v20\n"
"v_add_u32 v20, 17664, v20\n"
"s_lshl_b32 s58, s19, 4\n"
"v_add_u32 v66, s58, v65\n"
"v_lshrrev_b32 v21, 1, v66\n"
"v_and_b32 v21, 2, v21\n"
"v_xor_b32 v21, v21, v67\n"
"v_lshlrev_b32 v21, 4, v21\n"
"v_lshl_add_u32 v21, v66, 6, v21\n"
"v_add_u32 v21, 17664, v21\n"
"v_lshrrev_b32 v31, 1, v66\n"
"v_mul_u32_u24 v31, 73728, v31\n"
"v_and_b32 v66, 1, v66\n"
"v_lshl_add_u32 v31, v66, 6, v31\n"
"v_lshl_add_u32 v31, v67, 4, v31\n"
"v_add_u32 v28, 4294859776, v46\n"
"v_lshlrev_b32 v29, 4, v0\n"
"s_lshl_b32 s58, s19, 11\n"
"v_add_u32 v29, s58, v29\n"
"s_waitcnt vmcnt(0)\n"
"s_barrier\n"
"buffer_store_dwordx2 v[62:63], v61, s[36:39], s77 offen sc1\n"
"ds_write_b128 v8, v[4:7] offset:0\n"
"ds_write_b128 v8, v[4:7] offset:53760\n"
"s_cmp_eq_u32 s81, 0\n"
"s_cbranch_scc1 LfA%=\n"
"v_mul_f32 v160, s65, v160\n"
"v_mul_f32 v161, s65, v161\n"
"v_mul_f32 v162, s65, v162\n"
"v_mul_f32 v163, s65, v163\n"
"v_mul_f32 v192, s65, v192\n"
"v_mul_f32 v193, s65, v193\n"
"v_mul_f32 v194, s65, v194\n"
"v_mul_f32 v195, s65, v195\n"
"v_exp_f32 v160, v160\n"
"v_exp_f32 v161, v161\n"
"v_exp_f32 v162, v162\n"
"v_exp_f32 v163, v163\n"
"v_exp_f32 v192, v192\n"
"v_exp_f32 v193, v193\n"
"v_exp_f32 v194, v194\n"
"v_exp_f32 v195, v195\n"
"v_add_f32 v160, 1.0, v160\n"
"v_add_f32 v161, 1.0, v161\n"
"v_add_f32 v162, 1.0, v162\n"
"v_add_f32 v163, 1.0, v163\n"
"v_add_f32 v192, 1.0, v192\n"
"v_add_f32 v193, 1.0, v193\n"
"v_add_f32 v194, 1.0, v194\n"
"v_add_f32 v195, 1.0, v195\n"
"v_rcp_f32 v160, v160\n"
"v_rcp_f32 v161, v161\n"
"v_rcp_f32 v162, v162\n"
"v_rcp_f32 v163, v163\n"
"v_rcp_f32 v192, v192\n"
"v_rcp_f32 v193, v193\n"
"v_rcp_f32 v194, v194\n"
"v_rcp_f32 v195, v195\n"
"v_mul_f32 v160, v176, v160\n"
"v_mul_f32 v161, v177, v161\n"
"v_mul_f32 v162, v178, v162\n"
"v_mul_f32 v163, v179, v163\n"
"v_mul_f32 v192, v208, v192\n"
"v_mul_f32 v193, v209, v193\n"
"v_mul_f32 v194, v210, v194\n"
"v_mul_f32 v195, v211, v195\n"
"v_cvt_pk_bf16_f32 v176, v160, v161\n"
"v_cvt_pk_bf16_f32 v177, v162, v163\n"
"v_cvt_pk_bf16_f32 v178, v192, v193\n"
"v_cvt_pk_bf16_f32 v179, v194, v195\n"
"buffer_store_dwordx4 v[176:179], v24, s[36:39], s63 offen sc1\n"
"v_mul_f32 v164, s65, v164\n"
"v_mul_f32 v165, s65, v165\n"
"v_mul_f32 v166, s65, v166\n"
"v_mul_f32 v167, s65, v167\n"
"v_mul_f32 v196, s65, v196\n"
"v_mul_f32 v197, s65, v197\n"
"v_mul_f32 v198, s65, v198\n"
"v_mul_f32 v199, s65, v199\n"
"v_exp_f32 v164, v164\n"
"v_exp_f32 v165, v165\n"
"v_exp_f32 v166, v166\n"
"v_exp_f32 v167, v167\n"
"v_exp_f32 v196, v196\n"
"v_exp_f32 v197, v197\n"
"v_exp_f32 v198, v198\n"
"v_exp_f32 v199, v199\n"
"v_add_f32 v164, 1.0, v164\n"
"v_add_f32 v165, 1.0, v165\n"
"v_add_f32 v166, 1.0, v166\n"
"v_add_f32 v167, 1.0, v167\n"
"v_add_f32 v196, 1.0, v196\n"
"v_add_f32 v197, 1.0, v197\n"
"v_add_f32 v198, 1.0, v198\n"
"v_add_f32 v199, 1.0, v199\n"
"v_rcp_f32 v164, v164\n"
"v_rcp_f32 v165, v165\n"
"v_rcp_f32 v166, v166\n"
"v_rcp_f32 v167, v167\n"
"v_rcp_f32 v196, v196\n"
"v_rcp_f32 v197, v197\n"
"v_rcp_f32 v198, v198\n"
"v_rcp_f32 v199, v199\n"
"v_mul_f32 v164, v180, v164\n"
"v_mul_f32 v165, v181, v165\n"
"v_mul_f32 v166, v182, v166\n"
"v_mul_f32 v167, v183, v167\n"
"v_mul_f32 v196, v212, v196\n"
"v_mul_f32 v197, v213, v197\n"
"v_mul_f32 v198, v214, v198\n"
"v_mul_f32 v199, v215, v199\n"
"v_cvt_pk_bf16_f32 v180, v164, v165\n"
"v_cvt_pk_bf16_f32 v181, v166, v167\n"
"v_cvt_pk_bf16_f32 v182, v196, v197\n"
"v_cvt_pk_bf16_f32 v183, v198, v199\n"
"buffer_store_dwordx4 v[180:183], v25, s[36:39], s63 offen sc1\n"
"v_mul_f32 v168, s65, v168\n"
"v_mul_f32 v169, s65, v169\n"
"v_mul_f32 v170, s65, v170\n"
"v_mul_f32 v171, s65, v171\n"
"v_mul_f32 v200, s65, v200\n"
"v_mul_f32 v201, s65, v201\n"
"v_mul_f32 v202, s65, v202\n"
"v_mul_f32 v203, s65, v203\n"
"v_exp_f32 v168, v168\n"
"v_exp_f32 v169, v169\n"
"v_exp_f32 v170, v170\n"
"v_exp_f32 v171, v171\n"
"v_exp_f32 v200, v200\n"
"v_exp_f32 v201, v201\n"
"v_exp_f32 v202, v202\n"
"v_exp_f32 v203, v203\n"
"v_add_f32 v168, 1.0, v168\n"
"v_add_f32 v169, 1.0, v169\n"
"v_add_f32 v170, 1.0, v170\n"
"v_add_f32 v171, 1.0, v171\n"
"v_add_f32 v200, 1.0, v200\n"
"v_add_f32 v201, 1.0, v201\n"
"v_add_f32 v202, 1.0, v202\n"
"v_add_f32 v203, 1.0, v203\n"
"v_rcp_f32 v168, v168\n"
"v_rcp_f32 v169, v169\n"
"v_rcp_f32 v170, v170\n"
"v_rcp_f32 v171, v171\n"
"v_rcp_f32 v200, v200\n"
"v_rcp_f32 v201, v201\n"
"v_rcp_f32 v202, v202\n"
"v_rcp_f32 v203, v203\n"
"v_mul_f32 v168, v184, v168\n"
"v_mul_f32 v169, v185, v169\n"
"v_mul_f32 v170, v186, v170\n"
"v_mul_f32 v171, v187, v171\n"
"v_mul_f32 v200, v216, v200\n"
"v_mul_f32 v201, v217, v201\n"
"v_mul_f32 v202, v218, v202\n"
"v_mul_f32 v203, v219, v203\n"
"v_cvt_pk_bf16_f32 v184, v168, v169\n"
"v_cvt_pk_bf16_f32 v185, v170, v171\n"
"v_cvt_pk_bf16_f32 v186, v200, v201\n"
"v_cvt_pk_bf16_f32 v187, v202, v203\n"
"buffer_store_dwordx4 v[184:187], v26, s[36:39], s63 offen sc1\n"
"v_mul_f32 v172, s65, v172\n"
"v_mul_f32 v173, s65, v173\n"
"v_mul_f32 v174, s65, v174\n"
"v_mul_f32 v175, s65, v175\n"
"v_mul_f32 v204, s65, v204\n"
"v_mul_f32 v205, s65, v205\n"
"v_mul_f32 v206, s65, v206\n"
"v_mul_f32 v207, s65, v207\n"
"v_exp_f32 v172, v172\n"
"v_exp_f32 v173, v173\n"
"v_exp_f32 v174, v174\n"
"v_exp_f32 v175, v175\n"
"v_exp_f32 v204, v204\n"
"v_exp_f32 v205, v205\n"
"v_exp_f32 v206, v206\n"
"v_exp_f32 v207, v207\n"
"v_add_f32 v172, 1.0, v172\n"
"v_add_f32 v173, 1.0, v173\n"
"v_add_f32 v174, 1.0, v174\n"
"v_add_f32 v175, 1.0, v175\n"
"v_add_f32 v204, 1.0, v204\n"
"v_add_f32 v205, 1.0, v205\n"
"v_add_f32 v206, 1.0, v206\n"
"v_add_f32 v207, 1.0, v207\n"
"v_rcp_f32 v172, v172\n"
"v_rcp_f32 v173, v173\n"
"v_rcp_f32 v174, v174\n"
"v_rcp_f32 v175, v175\n"
"v_rcp_f32 v204, v204\n"
"v_rcp_f32 v205, v205\n"
"v_rcp_f32 v206, v206\n"
"v_rcp_f32 v207, v207\n"
"v_mul_f32 v172, v188, v172\n"
"v_mul_f32 v173, v189, v173\n"
"v_mul_f32 v174, v190, v174\n"
"v_mul_f32 v175, v191, v175\n"
"v_mul_f32 v204, v220, v204\n"
"v_mul_f32 v205, v221, v205\n"
"v_mul_f32 v206, v222, v206\n"
"v_mul_f32 v207, v223, v207\n"
"v_cvt_pk_bf16_f32 v188, v172, v173\n"
"v_cvt_pk_bf16_f32 v189, v174, v175\n"
"v_cvt_pk_bf16_f32 v190, v204, v205\n"
"v_cvt_pk_bf16_f32 v191, v206, v207\n"
"buffer_store_dwordx4 v[188:191], v27, s[36:39], s63 offen sc1\n"
"s_branch Lconv%=\n"
"LfA%=:\n"
"v_mul_f32 v96, s65, v96\n"
"v_mul_f32 v97, s65, v97\n"
"v_mul_f32 v98, s65, v98\n"
"v_mul_f32 v99, s65, v99\n"
"v_mul_f32 v128, s65, v128\n"
"v_mul_f32 v129, s65, v129\n"
"v_mul_f32 v130, s65, v130\n"
"v_mul_f32 v131, s65, v131\n"
"v_exp_f32 v96, v96\n"
"v_exp_f32 v97, v97\n"
"v_exp_f32 v98, v98\n"
"v_exp_f32 v99, v99\n"
"v_exp_f32 v128, v128\n"
"v_exp_f32 v129, v129\n"
"v_exp_f32 v130, v130\n"
"v_exp_f32 v131, v131\n"
"v_add_f32 v96, 1.0, v96\n"
"v_add_f32 v97, 1.0, v97\n"
"v_add_f32 v98, 1.0, v98\n"
"v_add_f32 v99, 1.0, v99\n"
"v_add_f32 v128, 1.0, v128\n"
"v_add_f32 v129, 1.0, v129\n"
"v_add_f32 v130, 1.0, v130\n"
"v_add_f32 v131, 1.0, v131\n"
"v_rcp_f32 v96, v96\n"
"v_rcp_f32 v97, v97\n"
"v_rcp_f32 v98, v98\n"
"v_rcp_f32 v99, v99\n"
"v_rcp_f32 v128, v128\n"
"v_rcp_f32 v129, v129\n"
"v_rcp_f32 v130, v130\n"
"v_rcp_f32 v131, v131\n"
"v_mul_f32 v96, v112, v96\n"
"v_mul_f32 v97, v113, v97\n"
"v_mul_f32 v98, v114, v98\n"
"v_mul_f32 v99, v115, v99\n"
"v_mul_f32 v128, v144, v128\n"
"v_mul_f32 v129, v145, v129\n"
"v_mul_f32 v130, v146, v130\n"
"v_mul_f32 v131, v147, v131\n"
"v_cvt_pk_bf16_f32 v112, v96, v97\n"
"v_cvt_pk_bf16_f32 v113, v98, v99\n"
"v_cvt_pk_bf16_f32 v114, v128, v129\n"
"v_cvt_pk_bf16_f32 v115, v130, v131\n"
"buffer_store_dwordx4 v[112:115], v24, s[36:39], s63 offen sc1\n"
"v_mul_f32 v100, s65, v100\n"
"v_mul_f32 v101, s65, v101\n"
"v_mul_f32 v102, s65, v102\n"
"v_mul_f32 v103, s65, v103\n"
"v_mul_f32 v132, s65, v132\n"
"v_mul_f32 v133, s65, v133\n"
"v_mul_f32 v134, s65, v134\n"
"v_mul_f32 v135, s65, v135\n"
"v_exp_f32 v100, v100\n"
"v_exp_f32 v101, v101\n"
"v_exp_f32 v102, v102\n"
"v_exp_f32 v103, v103\n"
"v_exp_f32 v132, v132\n"
"v_exp_f32 v133, v133\n"
"v_exp_f32 v134, v134\n"
"v_exp_f32 v135, v135\n"
"v_add_f32 v100, 1.0, v100\n"
"v_add_f32 v101, 1.0, v101\n"
"v_add_f32 v102, 1.0, v102\n"
"v_add_f32 v103, 1.0, v103\n"
"v_add_f32 v132, 1.0, v132\n"
"v_add_f32 v133, 1.0, v133\n"
"v_add_f32 v134, 1.0, v134\n"
"v_add_f32 v135, 1.0, v135\n"
"v_rcp_f32 v100, v100\n"
"v_rcp_f32 v101, v101\n"
"v_rcp_f32 v102, v102\n"
"v_rcp_f32 v103, v103\n"
"v_rcp_f32 v132, v132\n"
"v_rcp_f32 v133, v133\n"
"v_rcp_f32 v134, v134\n"
"v_rcp_f32 v135, v135\n"
"v_mul_f32 v100, v116, v100\n"
"v_mul_f32 v101, v117, v101\n"
"v_mul_f32 v102, v118, v102\n"
"v_mul_f32 v103, v119, v103\n"
"v_mul_f32 v132, v148, v132\n"
"v_mul_f32 v133, v149, v133\n"
"v_mul_f32 v134, v150, v134\n"
"v_mul_f32 v135, v151, v135\n"
"v_cvt_pk_bf16_f32 v116, v100, v101\n"
"v_cvt_pk_bf16_f32 v117, v102, v103\n"
"v_cvt_pk_bf16_f32 v118, v132, v133\n"
"v_cvt_pk_bf16_f32 v119, v134, v135\n"
"buffer_store_dwordx4 v[116:119], v25, s[36:39], s63 offen sc1\n"
"v_mul_f32 v104, s65, v104\n"
"v_mul_f32 v105, s65, v105\n"
"v_mul_f32 v106, s65, v106\n"
"v_mul_f32 v107, s65, v107\n"
"v_mul_f32 v136, s65, v136\n"
"v_mul_f32 v137, s65, v137\n"
"v_mul_f32 v138, s65, v138\n"
"v_mul_f32 v139, s65, v139\n"
"v_exp_f32 v104, v104\n"
"v_exp_f32 v105, v105\n"
"v_exp_f32 v106, v106\n"
"v_exp_f32 v107, v107\n"
"v_exp_f32 v136, v136\n"
"v_exp_f32 v137, v137\n"
"v_exp_f32 v138, v138\n"
"v_exp_f32 v139, v139\n"
"v_add_f32 v104, 1.0, v104\n"
"v_add_f32 v105, 1.0, v105\n"
"v_add_f32 v106, 1.0, v106\n"
"v_add_f32 v107, 1.0, v107\n"
"v_add_f32 v136, 1.0, v136\n"
"v_add_f32 v137, 1.0, v137\n"
"v_add_f32 v138, 1.0, v138\n"
"v_add_f32 v139, 1.0, v139\n"
"v_rcp_f32 v104, v104\n"
"v_rcp_f32 v105, v105\n"
"v_rcp_f32 v106, v106\n"
"v_rcp_f32 v107, v107\n"
"v_rcp_f32 v136, v136\n"
"v_rcp_f32 v137, v137\n"
"v_rcp_f32 v138, v138\n"
"v_rcp_f32 v139, v139\n"
"v_mul_f32 v104, v120, v104\n"
"v_mul_f32 v105, v121, v105\n"
"v_mul_f32 v106, v122, v106\n"
"v_mul_f32 v107, v123, v107\n"
"v_mul_f32 v136, v152, v136\n"
"v_mul_f32 v137, v153, v137\n"
"v_mul_f32 v138, v154, v138\n"
"v_mul_f32 v139, v155, v139\n"
"v_cvt_pk_bf16_f32 v120, v104, v105\n"
"v_cvt_pk_bf16_f32 v121, v106, v107\n"
"v_cvt_pk_bf16_f32 v122, v136, v137\n"
"v_cvt_pk_bf16_f32 v123, v138, v139\n"
"buffer_store_dwordx4 v[120:123], v26, s[36:39], s63 offen sc1\n"
"v_mul_f32 v108, s65, v108\n"
"v_mul_f32 v109, s65, v109\n"
"v_mul_f32 v110, s65, v110\n"
"v_mul_f32 v111, s65, v111\n"
"v_mul_f32 v140, s65, v140\n"
"v_mul_f32 v141, s65, v141\n"
"v_mul_f32 v142, s65, v142\n"
"v_mul_f32 v143, s65, v143\n"
"v_exp_f32 v108, v108\n"
"v_exp_f32 v109, v109\n"
"v_exp_f32 v110, v110\n"
"v_exp_f32 v111, v111\n"
"v_exp_f32 v140, v140\n"
"v_exp_f32 v141, v141\n"
"v_exp_f32 v142, v142\n"
"v_exp_f32 v143, v143\n"
"v_add_f32 v108, 1.0, v108\n"
"v_add_f32 v109, 1.0, v109\n"
"v_add_f32 v110, 1.0, v110\n"
"v_add_f32 v111, 1.0, v111\n"
"v_add_f32 v140, 1.0, v140\n"
"v_add_f32 v141, 1.0, v141\n"
"v_add_f32 v142, 1.0, v142\n"
"v_add_f32 v143, 1.0, v143\n"
"v_rcp_f32 v108, v108\n"
"v_rcp_f32 v109, v109\n"
"v_rcp_f32 v110, v110\n"
"v_rcp_f32 v111, v111\n"
"v_rcp_f32 v140, v140\n"
"v_rcp_f32 v141, v141\n"
"v_rcp_f32 v142, v142\n"
"v_rcp_f32 v143, v143\n"
"v_mul_f32 v108, v124, v108\n"
"v_mul_f32 v109, v125, v109\n"
"v_mul_f32 v110, v126, v110\n"
"v_mul_f32 v111, v127, v111\n"
"v_mul_f32 v140, v156, v140\n"
"v_mul_f32 v141, v157, v141\n"
"v_mul_f32 v142, v158, v142\n"
"v_mul_f32 v143, v159, v143\n"
"v_cvt_pk_bf16_f32 v124, v108, v109\n"
"v_cvt_pk_bf16_f32 v125, v110, v111\n"
"v_cvt_pk_bf16_f32 v126, v140, v141\n"
"v_cvt_pk_bf16_f32 v127, v142, v143\n"
"buffer_store_dwordx4 v[124:127], v27, s[36:39], s63 offen sc1\n"
"Lconv%=:\n"
"s_waitcnt lgkmcnt(0)\n"
"s_barrier\n"
"ds_read_b128 a[0:3], v32 offset:53760\n"
"ds_read_b128 a[16:19], v33 offset:53760\n"
"ds_read_b128 a[20:23], v33 offset:58880\n"
"ds_read_b128 a[24:27], v33 offset:54784\n"
"ds_read_b128 a[28:31], v33 offset:59904\n"
"ds_read_b128 a[32:35], v33 offset:55808\n"
"ds_read_b128 a[36:39], v33 offset:60928\n"
"ds_read_b128 a[40:43], v33 offset:56832\n"
"ds_read_b128 a[44:47], v33 offset:61952\n"
"ds_read_b128 a[4:7], v32 offset:54784\n"
"ds_read_b128 a[8:11], v32 offset:55808\n"
"ds_read_b128 a[12:15], v32 offset:56832\n"
"s_waitcnt lgkmcnt(0)\n"
"s_branch LcvB%=\n"
"Lcv%=:\n"
"v_mfma_f32_16x16x32_bf16 v[96:99], a[0:3], a[16:19], 0\n"
"ds_read_b128 a[48:51], v32 offset:64\n"
"v_mfma_f32_16x16x32_bf16 v[100:103], a[0:3], a[20:23], 0\n"
"ds_read_b128 a[64:67], v34 offset:0\n"
"v_mfma_f32_16x16x32_bf16 v[108:111], a[0:3], a[28:31], 0\n"
"ds_read_b128 a[68:71], v34 offset:5120\n"
"v_mfma_f32_16x16x32_bf16 v[108:111], a[4:7], a[20:23], v[108:111]\n"
"ds_read_b128 a[72:75], v34 offset:1024\n"
"v_mfma_f32_16x16x32_bf16 v[104:107], a[4:7], a[16:19], 0\n"
"ds_read_b128 a[76:79], v34 offset:6144\n"
"v_mfma_f32_16x16x32_bf16 v[104:107], a[0:3], a[24:27], v[104:107]\n"
"ds_read_b128 a[80:83], v34 offset:2048\n"
"v_mfma_f32_16x16x32_bf16 v[112:115], a[0:3], a[32:35], 0\n"
"ds_read_b128 a[84:87], v34 offset:7168\n"
"v_mfma_f32_16x16x32_bf16 v[112:115], a[4:7], a[24:27], v[112:115]\n"
"ds_read_b128 a[88:91], v34 offset:3072\n"
"v_mfma_f32_16x16x32_bf16 v[112:115], a[8:11], a[16:19], v[112:115]\n"
"ds_read_b128 a[92:95], v34 offset:8192\n"
"v_mfma_f32_16x16x32_bf16 v[116:119], a[8:11], a[20:23], 0\n"
"ds_read_b128 a[52:55], v32 offset:1088\n"
"v_mfma_f32_16x16x32_bf16 v[116:119], a[4:7], a[28:31], v[116:119]\n"
"ds_read_b128 a[56:59], v32 offset:2112\n"
"s_add_u32 m0, s54, 53760\n"
"v_mfma_f32_16x16x32_bf16 v[116:119], a[0:3], a[36:39], v[116:119]\n"
"ds_read_b128 a[60:63], v32 offset:3136\n"
"s_lshl_b32 s45, s46, 13\n"
"v_mfma_f32_16x16x32_bf16 v[124:127], a[0:3], a[44:47], 0\n"
"buffer_load_dwordx4 v43, s[36:39], s45 offen offset:0 sc1 lds\n"
"v_mfma_f32_16x16x32_bf16 v[124:127], a[4:7], a[36:39], v[124:127]\n"
"ds_write_b128 v46, v[128:131] offset:0\n"
"v_mfma_f32_16x16x32_bf16 v[124:127], a[8:11], a[28:31], v[124:127]\n"
"s_add_u32 s45, s45, 1024\n"
"v_mfma_f32_16x16x32_bf16 v[124:127], a[12:15], a[20:23], v[124:127]\n"
"buffer_load_dwordx4 v43, s[36:39], s45 offen offset:1024 sc1 lds\n"
"v_mfma_f32_16x16x32_bf16 v[120:123], a[12:15], a[16:19], 0\n"
"ds_write_b128 v46, v[132:135] offset:1024\n"
"v_mfma_f32_16x16x32_bf16 v[120:123], a[8:11], a[24:27], v[120:123]\n"
"s_add_u32 s45, s45, 1024\n"
"v_mfma_f32_16x16x32_bf16 v[120:123], a[4:7], a[32:35], v[120:123]\n"
"buffer_load_dwordx4 v43, s[36:39], s45 offen offset:2048 sc1 lds\n"
"v_mfma_f32_16x16x32_bf16 v[120:123], a[0:3], a[40:43], v[120:123]\n"
"s_waitcnt lgkmcnt(2)\n"
"v_mfma_f32_16x16x32_bf16 v[96:99], a[48:51], a[64:67], v[96:99]\n"
"ds_read_b128 a[0:3], v32 offset:128\n"
"v_mfma_f32_16x16x32_bf16 v[100:103], a[48:51], a[68:71], v[100:103]\n"
"ds_read_b128 a[16:19], v35 offset:0\n"
"v_mfma_f32_16x16x32_bf16 v[108:111], a[48:51], a[76:79], v[108:111]\n"
"ds_read_b128 a[20:23], v35 offset:5120\n"
"v_mfma_f32_16x16x32_bf16 v[108:111], a[52:55], a[68:71], v[108:111]\n"
"ds_read_b128 a[24:27], v35 offset:1024\n"
"v_mfma_f32_16x16x32_bf16 v[104:107], a[52:55], a[64:67], v[104:107]\n"
"ds_read_b128 a[28:31], v35 offset:6144\n"
"v_mfma_f32_16x16x32_bf16 v[104:107], a[48:51], a[72:75], v[104:107]\n"
"ds_read_b128 a[32:35], v35 offset:2048\n"
"v_mfma_f32_16x16x32_bf16 v[112:115], a[48:51], a[80:83], v[112:115]\n"
"ds_read_b128 a[36:39], v35 offset:7168\n"
"v_mfma_f32_16x16x32_bf16 v[112:115], a[52:55], a[72:75], v[112:115]\n"
"ds_read_b128 a[40:43], v35 offset:3072\n"
"v_mfma_f32_16x16x32_bf16 v[112:115], a[56:59], a[64:67], v[112:115]\n"
"ds_read_b128 a[44:47], v35 offset:8192\n"
"v_mfma_f32_16x16x32_bf16 v[116:119], a[56:59], a[68:71], v[116:119]\n"
"ds_read_b128 a[4:7], v32 offset:1152\n"
"v_mfma_f32_16x16x32_bf16 v[116:119], a[52:55], a[76:79], v[116:119]\n"
"ds_read_b128 a[8:11], v32 offset:2176\n"
"v_mfma_f32_16x16x32_bf16 v[116:119], a[48:51], a[84:87], v[116:119]\n"
"ds_read_b128 a[12:15], v32 offset:3200\n"
"s_add_u32 s45, s45, 1024\n"
"v_mfma_f32_16x16x32_bf16 v[124:127], a[48:51], a[92:95], v[124:127]\n"
"buffer_load_dwordx4 v43, s[36:39], s45 offen offset:3072 sc1 lds\n"
"v_mfma_f32_16x16x32_bf16 v[124:127], a[52:55], a[84:87], v[124:127]\n"
"ds_write_b128 v46, v[136:139] offset:2048\n"
"v_mfma_f32_16x16x32_bf16 v[124:127], a[56:59], a[76:79], v[124:127]\n"
"s_add_u32 m0, s53, 53760\n"
"s_lshl_b32 s48, s46, 12\n"
"v_mfma_f32_16x16x32_bf16 v[124:127], a[60:63], a[68:71], v[124:127]\n"
"buffer_load_dword v44, s[24:27], s48 offen lds\n"
"v_mfma_f32_16x16x32_bf16 v[120:123], a[60:63], a[64:67], v[120:123]\n"
"ds_write_b128 v46, v[140:143] offset:3072\n"
"v_mfma_f32_16x16x32_bf16 v[120:123], a[56:59], a[72:75], v[120:123]\n"
"s_add_u32 m0, s53, 53824\n"
"v_mfma_f32_16x16x32_bf16 v[120:123], a[52:55], a[80:83], v[120:123]\n"
"buffer_load_dwordx4 v42, s[24:27], s48 offen offset:0 lds\n"
"v_mfma_f32_16x16x32_bf16 v[120:123], a[48:51], a[88:91], v[120:123]\n"
"s_waitcnt lgkmcnt(2)\n"
"v_mfma_f32_16x16x32_bf16 v[96:99], a[0:3], a[16:19], v[96:99]\n"
"ds_read_b128 a[48:51], v32 offset:192\n"
"v_mfma_f32_16x16x32_bf16 v[100:103], a[0:3], a[20:23], v[100:103]\n"
"ds_read_b128 a[64:67], v36 offset:0\n"
"v_mfma_f32_16x16x32_bf16 v[108:111], a[0:3], a[28:31], v[108:111]\n"
"ds_read_b128 a[68:71], v36 offset:5120\n"
"v_mfma_f32_16x16x32_bf16 v[108:111], a[4:7], a[20:23], v[108:111]\n"
"ds_read_b128 a[72:75], v36 offset:1024\n"
"v_mfma_f32_16x16x32_bf16 v[104:107], a[4:7], a[16:19], v[104:107]\n"
"ds_read_b128 a[76:79], v36 offset:6144\n"
"v_mfma_f32_16x16x32_bf16 v[104:107], a[0:3], a[24:27], v[104:107]\n"
"ds_read_b128 a[80:83], v36 offset:2048\n"
"v_mfma_f32_16x16x32_bf16 v[112:115], a[0:3], a[32:35], v[112:115]\n"
"ds_read_b128 a[84:87], v36 offset:7168\n"
"v_mfma_f32_16x16x32_bf16 v[112:115], a[4:7], a[24:27], v[112:115]\n"
"ds_read_b128 a[88:91], v36 offset:3072\n"
"v_mfma_f32_16x16x32_bf16 v[112:115], a[8:11], a[16:19], v[112:115]\n"
"ds_read_b128 a[92:95], v36 offset:8192\n"
"v_mfma_f32_16x16x32_bf16 v[116:119], a[8:11], a[20:23], v[116:119]\n"
"ds_read_b128 a[52:55], v32 offset:1216\n"
"v_mfma_f32_16x16x32_bf16 v[116:119], a[4:7], a[28:31], v[116:119]\n"
"ds_read_b128 a[56:59], v32 offset:2240\n"
"v_mfma_f32_16x16x32_bf16 v[116:119], a[0:3], a[36:39], v[116:119]\n"
"ds_read_b128 a[60:63], v32 offset:3264\n"
"v_mfma_f32_16x16x32_bf16 v[124:127], a[0:3], a[44:47], v[124:127]\n"
"buffer_load_dwordx4 v42, s[24:27], s48 offen offset:1024 lds\n"
"v_mfma_f32_16x16x32_bf16 v[124:127], a[4:7], a[36:39], v[124:127]\n"
"ds_write_b128 v46, v[144:147] offset:4096\n"
"v_mfma_f32_16x16x32_bf16 v[124:127], a[8:11], a[28:31], v[124:127]\n"
"v_mfma_f32_16x16x32_bf16 v[124:127], a[12:15], a[20:23], v[124:127]\n"
"buffer_load_dwordx4 v42, s[24:27], s48 offen offset:2048 lds\n"
"v_mfma_f32_16x16x32_bf16 v[120:123], a[12:15], a[16:19], v[120:123]\n"
"ds_write_b128 v46, v[148:151] offset:5120\n"
"v_mfma_f32_16x16x32_bf16 v[120:123], a[8:11], a[24:27], v[120:123]\n"
"v_mfma_f32_16x16x32_bf16 v[120:123], a[4:7], a[32:35], v[120:123]\n"
"buffer_load_dwordx4 v42, s[24:27], s48 offen offset:3072 lds\n"
"v_mfma_f32_16x16x32_bf16 v[120:123], a[0:3], a[40:43], v[120:123]\n"
"s_waitcnt lgkmcnt(2)\n"
"v_mfma_f32_16x16x32_bf16 v[96:99], a[48:51], a[64:67], v[96:99]\n"
"ds_read_b128 a[0:3], v32 offset:256\n"
"v_mfma_f32_16x16x32_bf16 v[100:103], a[48:51], a[68:71], v[100:103]\n"
"ds_read_b128 a[16:19], v37 offset:0\n"
"v_mfma_f32_16x16x32_bf16 v[108:111], a[48:51], a[76:79], v[108:111]\n"
"ds_read_b128 a[20:23], v37 offset:5120\n"
"v_mfma_f32_16x16x32_bf16 v[108:111], a[52:55], a[68:71], v[108:111]\n"
"ds_read_b128 a[24:27], v37 offset:1024\n"
"v_mfma_f32_16x16x32_bf16 v[104:107], a[52:55], a[64:67], v[104:107]\n"
"ds_read_b128 a[28:31], v37 offset:6144\n"
"v_mfma_f32_16x16x32_bf16 v[104:107], a[48:51], a[72:75], v[104:107]\n"
"ds_read_b128 a[32:35], v37 offset:2048\n"
"v_mfma_f32_16x16x32_bf16 v[112:115], a[48:51], a[80:83], v[112:115]\n"
"ds_read_b128 a[36:39], v37 offset:7168\n"
"ds_read_b128 v[160:163], v41 offset:0\n"
"v_mfma_f32_16x16x32_bf16 v[112:115], a[52:55], a[72:75], v[112:115]\n"
"ds_read_b128 a[40:43], v37 offset:3072\n"
"ds_read_b128 v[164:167], v41 offset:5120\n"
"v_mfma_f32_16x16x32_bf16 v[112:115], a[56:59], a[64:67], v[112:115]\n"
"ds_read_b128 a[44:47], v37 offset:8192\n"
"ds_read_b128 v[168:171], v41 offset:1024\n"
"v_mfma_f32_16x16x32_bf16 v[116:119], a[56:59], a[68:71], v[116:119]\n"
"ds_read_b128 a[4:7], v32 offset:1280\n"
"ds_read_b128 v[172:175], v41 offset:6144\n"
"v_mfma_f32_16x16x32_bf16 v[116:119], a[52:55], a[76:79], v[116:119]\n"
"ds_read_b128 a[8:11], v32 offset:2304\n"
"s_add_u32 m0, s53, 57920\n"
"v_mfma_f32_16x16x32_bf16 v[116:119], a[48:51], a[84:87], v[116:119]\n"
"ds_read_b128 a[12:15], v32 offset:3328\n"
"s_add_u32 s46, s46, 2\n"
"v_mfma_f32_16x16x32_bf16 v[124:127], a[48:51], a[92:95], v[124:127]\n"
"buffer_load_dword v56, s[24:27], s48 offen lds\n"
"v_mfma_f32_16x16x32_bf16 v[124:127], a[52:55], a[84:87], v[124:127]\n"
"ds_write_b128 v46, v[152:155] offset:6144\n"
"v_mfma_f32_16x16x32_bf16 v[124:127], a[56:59], a[76:79], v[124:127]\n"
"s_add_u32 m0, s53, 57984\n"
"s_min_u32 s46, s46, 575\n"
"v_mfma_f32_16x16x32_bf16 v[124:127], a[60:63], a[68:71], v[124:127]\n"
"buffer_load_dwordx4 v57, s[24:27], s48 offen offset:0 lds\n"
"v_mfma_f32_16x16x32_bf16 v[120:123], a[60:63], a[64:67], v[120:123]\n"
"ds_write_b128 v46, v[156:159] offset:7168\n"
"v_mfma_f32_16x16x32_bf16 v[120:123], a[56:59], a[72:75], v[120:123]\n"
"v_mfma_f32_16x16x32_bf16 v[120:123], a[52:55], a[80:83], v[120:123]\n"
"buffer_load_dwordx4 v57, s[24:27], s48 offen offset:1024 lds\n"
"v_mfma_f32_16x16x32_bf16 v[120:123], a[48:51], a[88:91], v[120:123]\n"
"s_waitcnt lgkmcnt(2)\n"
"v_mfma_f32_16x16x32_bf16 v[96:99], a[0:3], a[16:19], v[96:99]\n"
"ds_read_b128 a[48:51], v32 offset:320\n"
"v_readlane_b32 s51, v48, s44\n"
"v_mfma_f32_16x16x32_bf16 v[100:103], a[0:3], a[20:23], v[100:103]\n"
"ds_read_b128 a[64:67], v38 offset:0\n"
"s_add_u32 s44, s44, 2\n"
"v_mfma_f32_16x16x32_bf16 v[108:111], a[0:3], a[28:31], v[108:111]\n"
"ds_read_b128 a[68:71], v38 offset:5120\n"
"v_mfma_f32_16x16x32_bf16 v[108:111], a[4:7], a[20:23], v[108:111]\n"
"ds_read_b128 a[72:75], v38 offset:1024\n"
"v_mfma_f32_16x16x32_bf16 v[104:107], a[4:7], a[16:19], v[104:107]\n"
"ds_read_b128 a[76:79], v38 offset:6144\n"
"v_and_b32 v4, s51, v49\n"
"v_mfma_f32_16x16x32_bf16 v[104:107], a[0:3], a[24:27], v[104:107]\n"
"ds_read_b128 a[80:83], v38 offset:2048\n"
"v_and_b32 v5, s51, v50\n"
"v_mfma_f32_16x16x32_bf16 v[112:115], a[0:3], a[32:35], v[112:115]\n"
"ds_read_b128 a[84:87], v38 offset:7168\n"
"v_and_b32 v6, s51, v51\n"
"v_mfma_f32_16x16x32_bf16 v[112:115], a[4:7], a[24:27], v[112:115]\n"
"ds_read_b128 a[88:91], v38 offset:3072\n"
"v_and_b32 v7, s51, v52\n"
"v_mfma_f32_16x16x32_bf16 v[112:115], a[8:11], a[16:19], v[112:115]\n"
"ds_read_b128 a[92:95], v38 offset:8192\n"
"v_mfma_f32_16x16x32_bf16 v[116:119], a[8:11], a[20:23], v[116:119]\n"
"ds_read_b128 a[52:55], v32 offset:1344\n"
"v_mfma_f32_16x16x32_bf16 v[116:119], a[4:7], a[28:31], v[116:119]\n"
"ds_read_b128 a[56:59], v32 offset:2368\n"
"v_mfma_f32_16x16x32_bf16 v[116:119], a[0:3], a[36:39], v[116:119]\n"
"ds_read_b128 a[60:63], v32 offset:3392\n"
"v_mfma_f32_16x16x32_bf16 v[124:127], a[0:3], a[44:47], v[124:127]\n"
"buffer_load_dwordx4 v57, s[24:27], s48 offen offset:2048 lds\n"
"v_mfma_f32_16x16x32_bf16 v[124:127], a[4:7], a[36:39], v[124:127]\n"
"v_mfma_f32_16x16x32_bf16 v[124:127], a[8:11], a[28:31], v[124:127]\n"
"v_mfma_f32_16x16x32_bf16 v[124:127], a[12:15], a[20:23], v[124:127]\n"
"v_mfma_f32_16x16x32_bf16 v[120:123], a[12:15], a[16:19], v[120:123]\n"
"v_mfma_f32_16x16x32_bf16 v[120:123], a[8:11], a[24:27], v[120:123]\n"
"v_mfma_f32_16x16x32_bf16 v[120:123], a[4:7], a[32:35], v[120:123]\n"
"buffer_load_dwordx4 v57, s[24:27], s48 offen offset:3072 lds\n"
"v_mfma_f32_16x16x32_bf16 v[120:123], a[0:3], a[40:43], v[120:123]\n"
"s_waitcnt lgkmcnt(0)\n"
"s_barrier\n"
"v_mfma_f32_16x16x32_bf16 v[96:99], a[48:51], a[64:67], v[96:99]\n"
"ds_read_b128 a[0:3], v32 offset:384\n"
"s_cmp_eq_u32 s47, 2\n"
"v_mfma_f32_16x16x32_bf16 v[100:103], a[48:51], a[68:71], v[100:103]\n"
"ds_read_b128 a[16:19], v39 offset:0\n"
"s_cselect_b64 vcc, -1, 0\n"
"v_mfma_f32_16x16x32_bf16 v[108:111], a[48:51], a[76:79], v[108:111]\n"
"ds_read_b128 a[20:23], v39 offset:5120\n"
"v_cndmask_b32 v22, v32, v16, vcc\n"
"v_mfma_f32_16x16x32_bf16 v[108:111], a[52:55], a[68:71], v[108:111]\n"
"ds_read_b128 a[24:27], v39 offset:1024\n"
"v_cndmask_b32 v23, v33, v17, vcc\n"
"v_mfma_f32_16x16x32_bf16 v[104:107], a[52:55], a[64:67], v[104:107]\n"
"ds_read_b128 a[28:31], v39 offset:6144\n"
"v_mfma_f32_16x16x32_bf16 v[104:107], a[48:51], a[72:75], v[104:107]\n"
"ds_read_b128 a[32:35], v39 offset:2048\n"
"v_mfma_f32_16x16x32_bf16 v[112:115], a[48:51], a[80:83], v[112:115]\n"
"ds_read_b128 a[36:39], v39 offset:7168\n"
"v_mfma_f32_16x16x32_bf16 v[112:115], a[52:55], a[72:75], v[112:115]\n"
"ds_read_b128 a[40:43], v39 offset:3072\n"
"v_mfma_f32_16x16x32_bf16 v[112:115], a[56:59], a[64:67], v[112:115]\n"
"ds_read_b128 a[44:47], v39 offset:8192\n"
"v_mfma_f32_16x16x32_bf16 v[116:119], a[56:59], a[68:71], v[116:119]\n"
"ds_read_b128 a[4:7], v32 offset:1408\n"
"v_mfma_f32_16x16x32_bf16 v[116:119], a[52:55], a[76:79], v[116:119]\n"
"ds_read_b128 a[8:11], v32 offset:2432\n"
"v_mfma_f32_16x16x32_bf16 v[116:119], a[48:51], a[84:87], v[116:119]\n"
"ds_read_b128 a[12:15], v32 offset:3456\n"
"v_mfma_f32_16x16x32_bf16 v[124:127], a[48:51], a[92:95], v[124:127]\n"
"ds_read_b128 v[192:195], v47 offset:0\n"
"ds_read_b128 v[196:199], v47 offset:1024\n"
"v_mfma_f32_16x16x32_bf16 v[124:127], a[52:55], a[84:87], v[124:127]\n"
"ds_read_b128 v[200:203], v47 offset:2048\n"
"ds_read_b128 v[204:207], v47 offset:3072\n"
"v_mfma_f32_16x16x32_bf16 v[124:127], a[56:59], a[76:79], v[124:127]\n"
"ds_read_b128 v[208:211], v47 offset:8192\n"
"ds_read_b128 v[212:215], v47 offset:9216\n"
"v_mfma_f32_16x16x32_bf16 v[124:127], a[60:63], a[68:71], v[124:127]\n"
"ds_read_b128 v[216:219], v47 offset:10240\n"
"ds_read_b128 v[220:223], v47 offset:11264\n"
"v_mfma_f32_16x16x32_bf16 v[120:123], a[60:63], a[64:67], v[120:123]\n"
"v_mfma_f32_16x16x32_bf16 v[120:123], a[56:59], a[72:75], v[120:123]\n"
"v_mfma_f32_16x16x32_bf16 v[120:123], a[52:55], a[80:83], v[120:123]\n"
"v_mfma_f32_16x16x32_bf16 v[120:123], a[48:51], a[88:91], v[120:123]\n"
"s_waitcnt lgkmcnt(0)\n"
"v_mfma_f32_16x16x32_bf16 v[96:99], a[0:3], a[16:19], v[96:99]\n"
"ds_read_b128 a[48:51], v32 offset:448\n"
"v_add_f32 v192, v192, v208\n"
"v_mfma_f32_16x16x32_bf16 v[100:103], a[0:3], a[20:23], v[100:103]\n"
"ds_read_b128 a[64:67], v40 offset:0\n"
"v_add_f32 v193, v193, v209\n"
"v_mfma_f32_16x16x32_bf16 v[108:111], a[0:3], a[28:31], v[108:111]\n"
"ds_read_b128 a[68:71], v40 offset:5120\n"
"v_add_f32 v194, v194, v210\n"
"v_mfma_f32_16x16x32_bf16 v[108:111], a[4:7], a[20:23], v[108:111]\n"
"ds_read_b128 a[72:75], v40 offset:1024\n"
"v_add_f32 v195, v195, v211\n"
"v_mfma_f32_16x16x32_bf16 v[104:107], a[4:7], a[16:19], v[104:107]\n"
"ds_read_b128 a[76:79], v40 offset:6144\n"
"v_add_f32 v196, v196, v212\n"
"v_mfma_f32_16x16x32_bf16 v[104:107], a[0:3], a[24:27], v[104:107]\n"
"ds_read_b128 a[80:83], v40 offset:2048\n"
"v_add_f32 v197, v197, v213\n"
"v_mfma_f32_16x16x32_bf16 v[112:115], a[0:3], a[32:35], v[112:115]\n"
"ds_read_b128 a[84:87], v40 offset:7168\n"
"v_add_f32 v198, v198, v214\n"
"v_mfma_f32_16x16x32_bf16 v[112:115], a[4:7], a[24:27], v[112:115]\n"
"ds_read_b128 a[88:91], v40 offset:3072\n"
"v_add_f32 v199, v199, v215\n"
"v_mfma_f32_16x16x32_bf16 v[112:115], a[8:11], a[16:19], v[112:115]\n"
"ds_read_b128 a[92:95], v40 offset:8192\n"
"v_add_f32 v200, v200, v216\n"
"v_mfma_f32_16x16x32_bf16 v[116:119], a[8:11], a[20:23], v[116:119]\n"
"ds_read_b128 a[52:55], v32 offset:1472\n"
"v_add_f32 v201, v201, v217\n"
"v_mfma_f32_16x16x32_bf16 v[116:119], a[4:7], a[28:31], v[116:119]\n"
"ds_read_b128 a[56:59], v32 offset:2496\n"
"v_add_f32 v202, v202, v218\n"
"v_mfma_f32_16x16x32_bf16 v[116:119], a[0:3], a[36:39], v[116:119]\n"
"ds_read_b128 a[60:63], v32 offset:3520\n"
"v_add_f32 v203, v203, v219\n"
"v_mfma_f32_16x16x32_bf16 v[124:127], a[0:3], a[44:47], v[124:127]\n"
"v_add_f32 v204, v204, v220\n"
"v_add_f32 v205, v205, v221\n"
"v_mfma_f32_16x16x32_bf16 v[124:127], a[4:7], a[36:39], v[124:127]\n"
"v_add_f32 v206, v206, v222\n"
"v_add_f32 v207, v207, v223\n"
"v_mfma_f32_16x16x32_bf16 v[124:127], a[8:11], a[28:31], v[124:127]\n"
"v_mfma_f32_16x16x32_bf16 v[124:127], a[12:15], a[20:23], v[124:127]\n"
"v_mfma_f32_16x16x32_bf16 v[192:195], v[4:7], v[176:179], v[192:195]\n"
"v_mfma_f32_16x16x32_bf16 v[120:123], a[12:15], a[16:19], v[120:123]\n"
"v_mfma_f32_16x16x32_bf16 v[196:199], v[4:7], v[180:183], v[196:199]\n"
"v_mfma_f32_16x16x32_bf16 v[120:123], a[8:11], a[24:27], v[120:123]\n"
"v_mfma_f32_16x16x32_bf16 v[200:203], v[4:7], v[184:187], v[200:203]\n"
"v_mfma_f32_16x16x32_bf16 v[120:123], a[4:7], a[32:35], v[120:123]\n"
"v_mfma_f32_16x16x32_bf16 v[204:207], v[4:7], v[188:191], v[204:207]\n"
"v_mfma_f32_16x16x32_bf16 v[120:123], a[0:3], a[40:43], v[120:123]\n"
"s_waitcnt vmcnt(0) lgkmcnt(0)\n"
"s_barrier\n"
"v_mfma_f32_16x16x32_bf16 v[96:99], a[48:51], a[64:67], v[96:99]\n"
"ds_read_b128 a[0:3], v22 offset:53760\n"
"v_mfma_f32_16x16x32_bf16 v[100:103], a[48:51], a[68:71], v[100:103]\n"
"ds_read_b128 a[16:19], v23 offset:53760\n"
"v_mfma_f32_16x16x32_bf16 v[108:111], a[48:51], a[76:79], v[108:111]\n"
"ds_read_b128 a[20:23], v23 offset:58880\n"
"v_mfma_f32_16x16x32_bf16 v[108:111], a[52:55], a[68:71], v[108:111]\n"
"ds_read_b128 a[24:27], v23 offset:54784\n"
"v_cvt_pk_bf16_f32 v8, v192, v196\n"
"v_mfma_f32_16x16x32_bf16 v[104:107], a[52:55], a[64:67], v[104:107]\n"
"ds_read_b128 a[28:31], v23 offset:59904\n"
"v_cvt_pk_bf16_f32 v9, v193, v197\n"
"v_mfma_f32_16x16x32_bf16 v[104:107], a[48:51], a[72:75], v[104:107]\n"
"ds_read_b128 a[32:35], v23 offset:55808\n"
"v_cvt_pk_bf16_f32 v10, v194, v198\n"
"v_mfma_f32_16x16x32_bf16 v[112:115], a[48:51], a[80:83], v[112:115]\n"
"ds_read_b128 a[36:39], v23 offset:60928\n"
"v_cvt_pk_bf16_f32 v11, v195, v199\n"
"v_mfma_f32_16x16x32_bf16 v[112:115], a[52:55], a[72:75], v[112:115]\n"
"ds_read_b128 a[40:43], v23 offset:56832\n"
"v_cvt_pk_bf16_f32 v12, v200, v204\n"
"v_mfma_f32_16x16x32_bf16 v[112:115], a[56:59], a[64:67], v[112:115]\n"
"ds_read_b128 a[44:47], v23 offset:61952\n"
"v_cvt_pk_bf16_f32 v13, v201, v205\n"
"v_mfma_f32_16x16x32_bf16 v[116:119], a[56:59], a[68:71], v[116:119]\n"
"ds_read_b128 a[4:7], v22 offset:54784\n"
"v_cvt_pk_bf16_f32 v14, v202, v206\n"
"v_mfma_f32_16x16x32_bf16 v[116:119], a[52:55], a[76:79], v[116:119]\n"
"ds_read_b128 a[8:11], v22 offset:55808\n"
"v_cvt_pk_bf16_f32 v15, v203, v207\n"
"v_mfma_f32_16x16x32_bf16 v[116:119], a[48:51], a[84:87], v[116:119]\n"
"ds_read_b128 a[12:15], v22 offset:56832\n"
"v_mfma_f32_16x16x32_bf16 v[124:127], a[48:51], a[92:95], v[124:127]\n"
"v_mfma_f32_16x16x32_bf16 v[124:127], a[52:55], a[84:87], v[124:127]\n"
"buffer_store_dwordx4 v[8:11], v45, s[28:31], s50 offen sc1\n"
"v_mfma_f32_16x16x32_bf16 v[124:127], a[56:59], a[76:79], v[124:127]\n"
"v_mfma_f32_16x16x32_bf16 v[124:127], a[60:63], a[68:71], v[124:127]\n"
"v_mfma_f32_16x16x32_bf16 v[120:123], a[60:63], a[64:67], v[120:123]\n"
"buffer_store_dwordx4 v[12:15], v58, s[28:31], s50 offen sc1\n"
"v_mfma_f32_16x16x32_bf16 v[120:123], a[56:59], a[72:75], v[120:123]\n"
"s_sub_u32 s47, s47, 2\n"
"v_mfma_f32_16x16x32_bf16 v[120:123], a[52:55], a[80:83], v[120:123]\n"
"s_add_u32 s50, s50, 256\n"
"s_mov_b32 s30, s38\n"
"v_mfma_f32_16x16x32_bf16 v[120:123], a[48:51], a[88:91], v[120:123]\n"
"s_waitcnt lgkmcnt(0)\n"
"s_cmp_eq_u32 s47, 0\n"
"s_cbranch_scc1 LcvfA%=\n"
"LcvB%=:\n"
"v_mfma_f32_16x16x32_bf16 v[128:131], a[0:3], a[16:19], 0\n"
"ds_read_b128 a[48:51], v32 offset:53824\n"
"v_mfma_f32_16x16x32_bf16 v[132:135], a[0:3], a[20:23], 0\n"
"ds_read_b128 a[64:67], v34 offset:53760\n"
"v_mfma_f32_16x16x32_bf16 v[140:143], a[0:3], a[28:31], 0\n"
"ds_read_b128 a[68:71], v34 offset:58880\n"
"v_mfma_f32_16x16x32_bf16 v[140:143], a[4:7], a[20:23], v[140:143]\n"
"ds_read_b128 a[72:75], v34 offset:54784\n"
"v_mfma_f32_16x16x32_bf16 v[136:139], a[4:7], a[16:19], 0\n"
"ds_read_b128 a[76:79], v34 offset:59904\n"
"v_mfma_f32_16x16x32_bf16 v[136:139], a[0:3], a[24:27], v[136:139]\n"
"ds_read_b128 a[80:83], v34 offset:55808\n"
"v_mfma_f32_16x16x32_bf16 v[144:147], a[0:3], a[32:35], 0\n"
"ds_read_b128 a[84:87], v34 offset:60928\n"
"v_mfma_f32_16x16x32_bf16 v[144:147], a[4:7], a[24:27], v[144:147]\n"
"ds_read_b128 a[88:91], v34 offset:56832\n"
"v_mfma_f32_16x16x32_bf16 v[144:147], a[8:11], a[16:19], v[144:147]\n"
"ds_read_b128 a[92:95], v34 offset:61952\n"
"v_mfma_f32_16x16x32_bf16 v[148:151], a[8:11], a[20:23], 0\n"
"ds_read_b128 a[52:55], v32 offset:54848\n"
"v_mfma_f32_16x16x32_bf16 v[148:151], a[4:7], a[28:31], v[148:151]\n"
"ds_read_b128 a[56:59], v32 offset:55872\n"
"s_add_u32 m0, s54, 0\n"
"v_mfma_f32_16x16x32_bf16 v[148:151], a[0:3], a[36:39], v[148:151]\n"
"ds_read_b128 a[60:63], v32 offset:56896\n"
"s_lshl_b32 s45, s46, 13\n"
"v_mfma_f32_16x16x32_bf16 v[156:159], a[0:3], a[44:47], 0\n"
"buffer_load_dwordx4 v43, s[36:39], s45 offen offset:0 sc1 lds\n"
"v_mfma_f32_16x16x32_bf16 v[156:159], a[4:7], a[36:39], v[156:159]\n"
"ds_write_b128 v46, v[96:99] offset:0\n"
"v_mfma_f32_16x16x32_bf16 v[156:159], a[8:11], a[28:31], v[156:159]\n"
"s_add_u32 s45, s45, 1024\n"
"v_mfma_f32_16x16x32_bf16 v[156:159], a[12:15], a[20:23], v[156:159]\n"
"buffer_load_dwordx4 v43, s[36:39], s45 offen offset:1024 sc1 lds\n"
"v_mfma_f32_16x16x32_bf16 v[152:155], a[12:15], a[16:19], 0\n"
"ds_write_b128 v46, v[100:103] offset:1024\n"
"v_mfma_f32_16x16x32_bf16 v[152:155], a[8:11], a[24:27], v[152:155]\n"
"s_add_u32 s45, s45, 1024\n"
"v_mfma_f32_16x16x32_bf16 v[152:155], a[4:7], a[32:35], v[152:155]\n"
"buffer_load_dwordx4 v43, s[36:39], s45 offen offset:2048 sc1 lds\n"
"v_mfma_f32_16x16x32_bf16 v[152:155], a[0:3], a[40:43], v[152:155]\n"
"s_waitcnt lgkmcnt(2)\n"
"v_mfma_f32_16x16x32_bf16 v[128:131], a[48:51], a[64:67], v[128:131]\n"
"ds_read_b128 a[0:3], v32 offset:53888\n"
"v_mfma_f32_16x16x32_bf16 v[132:135], a[48:51], a[68:71], v[132:135]\n"
"ds_read_b128 a[16:19], v35 offset:53760\n"
"v_mfma_f32_16x16x32_bf16 v[140:143], a[48:51], a[76:79], v[140:143]\n"
"ds_read_b128 a[20:23], v35 offset:58880\n"
"v_mfma_f32_16x16x32_bf16 v[140:143], a[52:55], a[68:71], v[140:143]\n"
"ds_read_b128 a[24:27], v35 offset:54784\n"
"v_mfma_f32_16x16x32_bf16 v[136:139], a[52:55], a[64:67], v[136:139]\n"
"ds_read_b128 a[28:31], v35 offset:59904\n"
"v_mfma_f32_16x16x32_bf16 v[136:139], a[48:51], a[72:75], v[136:139]\n"
"ds_read_b128 a[32:35], v35 offset:55808\n"
"v_mfma_f32_16x16x32_bf16 v[144:147], a[48:51], a[80:83], v[144:147]\n"
"ds_read_b128 a[36:39], v35 offset:60928\n"
"v_mfma_f32_16x16x32_bf16 v[144:147], a[52:55], a[72:75], v[144:147]\n"
"ds_read_b128 a[40:43], v35 offset:56832\n"
"v_mfma_f32_16x16x32_bf16 v[144:147], a[56:59], a[64:67], v[144:147]\n"
"ds_read_b128 a[44:47], v35 offset:61952\n"
"v_mfma_f32_16x16x32_bf16 v[148:151], a[56:59], a[68:71], v[148:151]\n"
"ds_read_b128 a[4:7], v32 offset:54912\n"
"v_mfma_f32_16x16x32_bf16 v[148:151], a[52:55], a[76:79], v[148:151]\n"
"ds_read_b128 a[8:11], v32 offset:55936\n"
"v_mfma_f32_16x16x32_bf16 v[148:151], a[48:51], a[84:87], v[148:151]\n"
"ds_read_b128 a[12:15], v32 offset:56960\n"
"s_add_u32 s45, s45, 1024\n"
"v_mfma_f32_16x16x32_bf16 v[156:159], a[48:51], a[92:95], v[156:159]\n"
"buffer_load_dwordx4 v43, s[36:39], s45 offen offset:3072 sc1 lds\n"
"v_mfma_f32_16x16x32_bf16 v[156:159], a[52:55], a[84:87], v[156:159]\n"
"ds_write_b128 v46, v[104:107] offset:2048\n"
"v_mfma_f32_16x16x32_bf16 v[156:159], a[56:59], a[76:79], v[156:159]\n"
"s_add_u32 m0, s53, 0\n"
"s_lshl_b32 s48, s46, 12\n"
"v_mfma_f32_16x16x32_bf16 v[156:159], a[60:63], a[68:71], v[156:159]\n"
"buffer_load_dword v44, s[24:27], s48 offen lds\n"
"v_mfma_f32_16x16x32_bf16 v[152:155], a[60:63], a[64:67], v[152:155]\n"
"ds_write_b128 v46, v[108:111] offset:3072\n"
"v_mfma_f32_16x16x32_bf16 v[152:155], a[56:59], a[72:75], v[152:155]\n"
"s_add_u32 m0, s53, 64\n"
"v_mfma_f32_16x16x32_bf16 v[152:155], a[52:55], a[80:83], v[152:155]\n"
"buffer_load_dwordx4 v42, s[24:27], s48 offen offset:0 lds\n"
"v_mfma_f32_16x16x32_bf16 v[152:155], a[48:51], a[88:91], v[152:155]\n"
"s_waitcnt lgkmcnt(2)\n"
"v_mfma_f32_16x16x32_bf16 v[128:131], a[0:3], a[16:19], v[128:131]\n"
"ds_read_b128 a[48:51], v32 offset:53952\n"
"v_mfma_f32_16x16x32_bf16 v[132:135], a[0:3], a[20:23], v[132:135]\n"
"ds_read_b128 a[64:67], v36 offset:53760\n"
"v_mfma_f32_16x16x32_bf16 v[140:143], a[0:3], a[28:31], v[140:143]\n"
"ds_read_b128 a[68:71], v36 offset:58880\n"
"v_mfma_f32_16x16x32_bf16 v[140:143], a[4:7], a[20:23], v[140:143]\n"
"ds_read_b128 a[72:75], v36 offset:54784\n"
"v_mfma_f32_16x16x32_bf16 v[136:139], a[4:7], a[16:19], v[136:139]\n"
"ds_read_b128 a[76:79], v36 offset:59904\n"
"v_mfma_f32_16x16x32_bf16 v[136:139], a[0:3], a[24:27], v[136:139]\n"
"ds_read_b128 a[80:83], v36 offset:55808\n"
"v_mfma_f32_16x16x32_bf16 v[144:147], a[0:3], a[32:35], v[144:147]\n"
"ds_read_b128 a[84:87], v36 offset:60928\n"
"v_mfma_f32_16x16x32_bf16 v[144:147], a[4:7], a[24:27], v[144:147]\n"
"ds_read_b128 a[88:91], v36 offset:56832\n"
"v_mfma_f32_16x16x32_bf16 v[144:147], a[8:11], a[16:19], v[144:147]\n"
"ds_read_b128 a[92:95], v36 offset:61952\n"
"v_mfma_f32_16x16x32_bf16 v[148:151], a[8:11], a[20:23], v[148:151]\n"
"ds_read_b128 a[52:55], v32 offset:54976\n"
"v_mfma_f32_16x16x32_bf16 v[148:151], a[4:7], a[28:31], v[148:151]\n"
"ds_read_b128 a[56:59], v32 offset:56000\n"
"v_mfma_f32_16x16x32_bf16 v[148:151], a[0:3], a[36:39], v[148:151]\n"
"ds_read_b128 a[60:63], v32 offset:57024\n"
"v_mfma_f32_16x16x32_bf16 v[156:159], a[0:3], a[44:47], v[156:159]\n"
"buffer_load_dwordx4 v42, s[24:27], s48 offen offset:1024 lds\n"
"v_mfma_f32_16x16x32_bf16 v[156:159], a[4:7], a[36:39], v[156:159]\n"
"ds_write_b128 v46, v[112:115] offset:4096\n"
"v_mfma_f32_16x16x32_bf16 v[156:159], a[8:11], a[28:31], v[156:159]\n"
"v_mfma_f32_16x16x32_bf16 v[156:159], a[12:15], a[20:23], v[156:159]\n"
"buffer_load_dwordx4 v42, s[24:27], s48 offen offset:2048 lds\n"
"v_mfma_f32_16x16x32_bf16 v[152:155], a[12:15], a[16:19], v[152:155]\n"
"ds_write_b128 v46, v[116:119] offset:5120\n"
"v_mfma_f32_16x16x32_bf16 v[152:155], a[8:11], a[24:27], v[152:155]\n"
"v_mfma_f32_16x16x32_bf16 v[152:155], a[4:7], a[32:35], v[152:155]\n"
"buffer_load_dwordx4 v42, s[24:27], s48 offen offset:3072 lds\n"
"v_mfma_f32_16x16x32_bf16 v[152:155], a[0:3], a[40:43], v[152:155]\n"
"s_waitcnt lgkmcnt(2)\n"
"v_mfma_f32_16x16x32_bf16 v[128:131], a[48:51], a[64:67], v[128:131]\n"
"ds_read_b128 a[0:3], v32 offset:54016\n"
"v_mfma_f32_16x16x32_bf16 v[132:135], a[48:51], a[68:71], v[132:135]\n"
"ds_read_b128 a[16:19], v37 offset:53760\n"
"v_mfma_f32_16x16x32_bf16 v[140:143], a[48:51], a[76:79], v[140:143]\n"
"ds_read_b128 a[20:23], v37 offset:58880\n"
"v_mfma_f32_16x16x32_bf16 v[140:143], a[52:55], a[68:71], v[140:143]\n"
"ds_read_b128 a[24:27], v37 offset:54784\n"
"v_mfma_f32_16x16x32_bf16 v[136:139], a[52:55], a[64:67], v[136:139]\n"
"ds_read_b128 a[28:31], v37 offset:59904\n"
"v_mfma_f32_16x16x32_bf16 v[136:139], a[48:51], a[72:75], v[136:139]\n"
"ds_read_b128 a[32:35], v37 offset:55808\n"
"v_mfma_f32_16x16x32_bf16 v[144:147], a[48:51], a[80:83], v[144:147]\n"
"ds_read_b128 a[36:39], v37 offset:60928\n"
"ds_read_b128 v[176:179], v41 offset:53760\n"
"v_mfma_f32_16x16x32_bf16 v[144:147], a[52:55], a[72:75], v[144:147]\n"
"ds_read_b128 a[40:43], v37 offset:56832\n"
"ds_read_b128 v[180:183], v41 offset:58880\n"
"v_mfma_f32_16x16x32_bf16 v[144:147], a[56:59], a[64:67], v[144:147]\n"
"ds_read_b128 a[44:47], v37 offset:61952\n"
"ds_read_b128 v[184:187], v41 offset:54784\n"
"v_mfma_f32_16x16x32_bf16 v[148:151], a[56:59], a[68:71], v[148:151]\n"
"ds_read_b128 a[4:7], v32 offset:55040\n"
"ds_read_b128 v[188:191], v41 offset:59904\n"
"v_mfma_f32_16x16x32_bf16 v[148:151], a[52:55], a[76:79], v[148:151]\n"
"ds_read_b128 a[8:11], v32 offset:56064\n"
"s_add_u32 m0, s53, 4160\n"
"v_mfma_f32_16x16x32_bf16 v[148:151], a[48:51], a[84:87], v[148:151]\n"
"ds_read_b128 a[12:15], v32 offset:57088\n"
"s_add_u32 s46, s46, 2\n"
"v_mfma_f32_16x16x32_bf16 v[156:159], a[48:51], a[92:95], v[156:159]\n"
"buffer_load_dword v56, s[24:27], s48 offen lds\n"
"v_mfma_f32_16x16x32_bf16 v[156:159], a[52:55], a[84:87], v[156:159]\n"
"ds_write_b128 v46, v[120:123] offset:6144\n"
"v_mfma_f32_16x16x32_bf16 v[156:159], a[56:59], a[76:79], v[156:159]\n"
"s_add_u32 m0, s53, 4224\n"
"s_min_u32 s46, s46, 575\n"
"v_mfma_f32_16x16x32_bf16 v[156:159], a[60:63], a[68:71], v[156:159]\n"
"buffer_load_dwordx4 v57, s[24:27], s48 offen offset:0 lds\n"
"v_mfma_f32_16x16x32_bf16 v[152:155], a[60:63], a[64:67], v[152:155]\n"
"ds_write_b128 v46, v[124:127] offset:7168\n"
"v_mfma_f32_16x16x32_bf16 v[152:155], a[56:59], a[72:75], v[152:155]\n"
"v_mfma_f32_16x16x32_bf16 v[152:155], a[52:55], a[80:83], v[152:155]\n"
"buffer_load_dwordx4 v57, s[24:27], s48 offen offset:1024 lds\n"
"v_mfma_f32_16x16x32_bf16 v[152:155], a[48:51], a[88:91], v[152:155]\n"
"s_waitcnt lgkmcnt(2)\n"
"v_mfma_f32_16x16x32_bf16 v[128:131], a[0:3], a[16:19], v[128:131]\n"
"ds_read_b128 a[48:51], v32 offset:54080\n"
"v_readlane_b32 s51, v48, s44\n"
"v_mfma_f32_16x16x32_bf16 v[132:135], a[0:3], a[20:23], v[132:135]\n"
"ds_read_b128 a[64:67], v38 offset:53760\n"
"s_add_u32 s44, s44, 2\n"
"v_mfma_f32_16x16x32_bf16 v[140:143], a[0:3], a[28:31], v[140:143]\n"
"ds_read_b128 a[68:71], v38 offset:58880\n"
"v_mfma_f32_16x16x32_bf16 v[140:143], a[4:7], a[20:23], v[140:143]\n"
"ds_read_b128 a[72:75], v38 offset:54784\n"
"v_mfma_f32_16x16x32_bf16 v[136:139], a[4:7], a[16:19], v[136:139]\n"
"ds_read_b128 a[76:79], v38 offset:59904\n"
"v_and_b32 v4, s51, v49\n"
"v_mfma_f32_16x16x32_bf16 v[136:139], a[0:3], a[24:27], v[136:139]\n"
"ds_read_b128 a[80:83], v38 offset:55808\n"
"v_and_b32 v5, s51, v50\n"
"v_mfma_f32_16x16x32_bf16 v[144:147], a[0:3], a[32:35], v[144:147]\n"
"ds_read_b128 a[84:87], v38 offset:60928\n"
"v_and_b32 v6, s51, v51\n"
"v_mfma_f32_16x16x32_bf16 v[144:147], a[4:7], a[24:27], v[144:147]\n"
"ds_read_b128 a[88:91], v38 offset:56832\n"
"v_and_b32 v7, s51, v52\n"
"v_mfma_f32_16x16x32_bf16 v[144:147], a[8:11], a[16:19], v[144:147]\n"
"ds_read_b128 a[92:95], v38 offset:61952\n"
"v_mfma_f32_16x16x32_bf16 v[148:151], a[8:11], a[20:23], v[148:151]\n"
"ds_read_b128 a[52:55], v32 offset:55104\n"
"v_mfma_f32_16x16x32_bf16 v[148:151], a[4:7], a[28:31], v[148:151]\n"
"ds_read_b128 a[56:59], v32 offset:56128\n"
"v_mfma_f32_16x16x32_bf16 v[148:151], a[0:3], a[36:39], v[148:151]\n"
"ds_read_b128 a[60:63], v32 offset:57152\n"
"v_mfma_f32_16x16x32_bf16 v[156:159], a[0:3], a[44:47], v[156:159]\n"
"buffer_load_dwordx4 v57, s[24:27], s48 offen offset:2048 lds\n"
"v_mfma_f32_16x16x32_bf16 v[156:159], a[4:7], a[36:39], v[156:159]\n"
"v_mfma_f32_16x16x32_bf16 v[156:159], a[8:11], a[28:31], v[156:159]\n"
"v_mfma_f32_16x16x32_bf16 v[156:159], a[12:15], a[20:23], v[156:159]\n"
"v_mfma_f32_16x16x32_bf16 v[152:155], a[12:15], a[16:19], v[152:155]\n"
"v_mfma_f32_16x16x32_bf16 v[152:155], a[8:11], a[24:27], v[152:155]\n"
"v_mfma_f32_16x16x32_bf16 v[152:155], a[4:7], a[32:35], v[152:155]\n"
"buffer_load_dwordx4 v57, s[24:27], s48 offen offset:3072 lds\n"
"v_mfma_f32_16x16x32_bf16 v[152:155], a[0:3], a[40:43], v[152:155]\n"
"s_waitcnt lgkmcnt(0)\n"
"s_barrier\n"
"v_mfma_f32_16x16x32_bf16 v[128:131], a[48:51], a[64:67], v[128:131]\n"
"ds_read_b128 a[0:3], v32 offset:54144\n"
"v_mfma_f32_16x16x32_bf16 v[132:135], a[48:51], a[68:71], v[132:135]\n"
"ds_read_b128 a[16:19], v39 offset:53760\n"
"v_mfma_f32_16x16x32_bf16 v[140:143], a[48:51], a[76:79], v[140:143]\n"
"ds_read_b128 a[20:23], v39 offset:58880\n"
"v_mfma_f32_16x16x32_bf16 v[140:143], a[52:55], a[68:71], v[140:143]\n"
"ds_read_b128 a[24:27], v39 offset:54784\n"
"v_mfma_f32_16x16x32_bf16 v[136:139], a[52:55], a[64:67], v[136:139]\n"
"ds_read_b128 a[28:31], v39 offset:59904\n"
"v_mfma_f32_16x16x32_bf16 v[136:139], a[48:51], a[72:75], v[136:139]\n"
"ds_read_b128 a[32:35], v39 offset:55808\n"
"v_mfma_f32_16x16x32_bf16 v[144:147], a[48:51], a[80:83], v[144:147]\n"
"ds_read_b128 a[36:39], v39 offset:60928\n"
"v_mfma_f32_16x16x32_bf16 v[144:147], a[52:55], a[72:75], v[144:147]\n"
"ds_read_b128 a[40:43], v39 offset:56832\n"
"v_mfma_f32_16x16x32_bf16 v[144:147], a[56:59], a[64:67], v[144:147]\n"
"ds_read_b128 a[44:47], v39 offset:61952\n"
"v_mfma_f32_16x16x32_bf16 v[148:151], a[56:59], a[68:71], v[148:151]\n"
"ds_read_b128 a[4:7], v32 offset:55168\n"
"v_mfma_f32_16x16x32_bf16 v[148:151], a[52:55], a[76:79], v[148:151]\n"
"ds_read_b128 a[8:11], v32 offset:56192\n"
"v_mfma_f32_16x16x32_bf16 v[148:151], a[48:51], a[84:87], v[148:151]\n"
"ds_read_b128 a[12:15], v32 offset:57216\n"
"v_mfma_f32_16x16x32_bf16 v[156:159], a[48:51], a[92:95], v[156:159]\n"
"ds_read_b128 v[192:195], v47 offset:0\n"
"ds_read_b128 v[196:199], v47 offset:1024\n"
"v_mfma_f32_16x16x32_bf16 v[156:159], a[52:55], a[84:87], v[156:159]\n"
"ds_read_b128 v[200:203], v47 offset:2048\n"
"ds_read_b128 v[204:207], v47 offset:3072\n"
"v_mfma_f32_16x16x32_bf16 v[156:159], a[56:59], a[76:79], v[156:159]\n"
"ds_read_b128 v[208:211], v47 offset:8192\n"
"ds_read_b128 v[212:215], v47 offset:9216\n"
"v_mfma_f32_16x16x32_bf16 v[156:159], a[60:63], a[68:71], v[156:159]\n"
"ds_read_b128 v[216:219], v47 offset:10240\n"
"ds_read_b128 v[220:223], v47 offset:11264\n"
"v_mfma_f32_16x16x32_bf16 v[152:155], a[60:63], a[64:67], v[152:155]\n"
"v_mfma_f32_16x16x32_bf16 v[152:155], a[56:59], a[72:75], v[152:155]\n"
"v_mfma_f32_16x16x32_bf16 v[152:155], a[52:55], a[80:83], v[152:155]\n"
"v_mfma_f32_16x16x32_bf16 v[152:155], a[48:51], a[88:91], v[152:155]\n"
"s_waitcnt lgkmcnt(0)\n"
"v_mfma_f32_16x16x32_bf16 v[128:131], a[0:3], a[16:19], v[128:131]\n"
"ds_read_b128 a[48:51], v32 offset:54208\n"
"v_add_f32 v192, v192, v208\n"
"v_mfma_f32_16x16x32_bf16 v[132:135], a[0:3], a[20:23], v[132:135]\n"
"ds_read_b128 a[64:67], v40 offset:53760\n"
"v_add_f32 v193, v193, v209\n"
"v_mfma_f32_16x16x32_bf16 v[140:143], a[0:3], a[28:31], v[140:143]\n"
"ds_read_b128 a[68:71], v40 offset:58880\n"
"v_add_f32 v194, v194, v210\n"
"v_mfma_f32_16x16x32_bf16 v[140:143], a[4:7], a[20:23], v[140:143]\n"
"ds_read_b128 a[72:75], v40 offset:54784\n"
"v_add_f32 v195, v195, v211\n"
"v_mfma_f32_16x16x32_bf16 v[136:139], a[4:7], a[16:19], v[136:139]\n"
"ds_read_b128 a[76:79], v40 offset:59904\n"
"v_add_f32 v196, v196, v212\n"
"v_mfma_f32_16x16x32_bf16 v[136:139], a[0:3], a[24:27], v[136:139]\n"
"ds_read_b128 a[80:83], v40 offset:55808\n"
"v_add_f32 v197, v197, v213\n"
"v_mfma_f32_16x16x32_bf16 v[144:147], a[0:3], a[32:35], v[144:147]\n"
"ds_read_b128 a[84:87], v40 offset:60928\n"
"v_add_f32 v198, v198, v214\n"
"v_mfma_f32_16x16x32_bf16 v[144:147], a[4:7], a[24:27], v[144:147]\n"
"ds_read_b128 a[88:91], v40 offset:56832\n"
"v_add_f32 v199, v199, v215\n"
"v_mfma_f32_16x16x32_bf16 v[144:147], a[8:11], a[16:19], v[144:147]\n"
"ds_read_b128 a[92:95], v40 offset:61952\n"
"v_add_f32 v200, v200, v216\n"
"v_mfma_f32_16x16x32_bf16 v[148:151], a[8:11], a[20:23], v[148:151]\n"
"ds_read_b128 a[52:55], v32 offset:55232\n"
"v_add_f32 v201, v201, v217\n"
"v_mfma_f32_16x16x32_bf16 v[148:151], a[4:7], a[28:31], v[148:151]\n"
"ds_read_b128 a[56:59], v32 offset:56256\n"
"v_add_f32 v202, v202, v218\n"
"v_mfma_f32_16x16x32_bf16 v[148:151], a[0:3], a[36:39], v[148:151]\n"
"ds_read_b128 a[60:63], v32 offset:57280\n"
"v_add_f32 v203, v203, v219\n"
"v_mfma_f32_16x16x32_bf16 v[156:159], a[0:3], a[44:47], v[156:159]\n"
"v_add_f32 v204, v204, v220\n"
"v_add_f32 v205, v205, v221\n"
"v_mfma_f32_16x16x32_bf16 v[156:159], a[4:7], a[36:39], v[156:159]\n"
"v_add_f32 v206, v206, v222\n"
"v_add_f32 v207, v207, v223\n"
"v_mfma_f32_16x16x32_bf16 v[156:159], a[8:11], a[28:31], v[156:159]\n"
"v_mfma_f32_16x16x32_bf16 v[156:159], a[12:15], a[20:23], v[156:159]\n"
"v_mfma_f32_16x16x32_bf16 v[192:195], v[4:7], v[160:163], v[192:195]\n"
"v_mfma_f32_16x16x32_bf16 v[152:155], a[12:15], a[16:19], v[152:155]\n"
"v_mfma_f32_16x16x32_bf16 v[196:199], v[4:7], v[164:167], v[196:199]\n"
"v_mfma_f32_16x16x32_bf16 v[152:155], a[8:11], a[24:27], v[152:155]\n"
"v_mfma_f32_16x16x32_bf16 v[200:203], v[4:7], v[168:171], v[200:203]\n"
"v_mfma_f32_16x16x32_bf16 v[152:155], a[4:7], a[32:35], v[152:155]\n"
"v_mfma_f32_16x16x32_bf16 v[204:207], v[4:7], v[172:175], v[204:207]\n"
"v_mfma_f32_16x16x32_bf16 v[152:155], a[0:3], a[40:43], v[152:155]\n"
"s_waitcnt vmcnt(0) lgkmcnt(0)\n"
"s_barrier\n"
"v_mfma_f32_16x16x32_bf16 v[128:131], a[48:51], a[64:67], v[128:131]\n"
"ds_read_b128 a[0:3], v32 offset:0\n"
"v_mfma_f32_16x16x32_bf16 v[132:135], a[48:51], a[68:71], v[132:135]\n"
"ds_read_b128 a[16:19], v33 offset:0\n"
"v_mfma_f32_16x16x32_bf16 v[140:143], a[48:51], a[76:79], v[140:143]\n"
"ds_read_b128 a[20:23], v33 offset:5120\n"
"v_mfma_f32_16x16x32_bf16 v[140:143], a[52:55], a[68:71], v[140:143]\n"
"ds_read_b128 a[24:27], v33 offset:1024\n"
"v_cvt_pk_bf16_f32 v8, v192, v196\n"
"v_mfma_f32_16x16x32_bf16 v[136:139], a[52:55], a[64:67], v[136:139]\n"
"ds_read_b128 a[28:31], v33 offset:6144\n"
"v_cvt_pk_bf16_f32 v9, v193, v197\n"
"v_mfma_f32_16x16x32_bf16 v[136:139], a[48:51], a[72:75], v[136:139]\n"
"ds_read_b128 a[32:35], v33 offset:2048\n"
"v_cvt_pk_bf16_f32 v10, v194, v198\n"
"v_mfma_f32_16x16x32_bf16 v[144:147], a[48:51], a[80:83], v[144:147]\n"
"ds_read_b128 a[36:39], v33 offset:7168\n"
"v_cvt_pk_bf16_f32 v11, v195, v199\n"
"v_mfma_f32_16x16x32_bf16 v[144:147], a[52:55], a[72:75], v[144:147]\n"
"ds_read_b128 a[40:43], v33 offset:3072\n"
"v_cvt_pk_bf16_f32 v12, v200, v204\n"
"v_mfma_f32_16x16x32_bf16 v[144:147], a[56:59], a[64:67], v[144:147]\n"
"ds_read_b128 a[44:47], v33 offset:8192\n"
"v_cvt_pk_bf16_f32 v13, v201, v205\n"
"v_mfma_f32_16x16x32_bf16 v[148:151], a[56:59], a[68:71], v[148:151]\n"
"ds_read_b128 a[4:7], v32 offset:1024\n"
"v_cvt_pk_bf16_f32 v14, v202, v206\n"
"v_mfma_f32_16x16x32_bf16 v[148:151], a[52:55], a[76:79], v[148:151]\n"
"ds_read_b128 a[8:11], v32 offset:2048\n"
"v_cvt_pk_bf16_f32 v15, v203, v207\n"
"v_mfma_f32_16x16x32_bf16 v[148:151], a[48:51], a[84:87], v[148:151]\n"
"ds_read_b128 a[12:15], v32 offset:3072\n"
"v_mfma_f32_16x16x32_bf16 v[156:159], a[48:51], a[92:95], v[156:159]\n"
"buffer_store_dwordx2 v[62:63], v61, s[36:39], s76 offen sc1\n"
"v_mfma_f32_16x16x32_bf16 v[156:159], a[52:55], a[84:87], v[156:159]\n"
"buffer_store_dwordx4 v[8:11], v45, s[28:31], s50 offen sc1\n"
"v_mfma_f32_16x16x32_bf16 v[156:159], a[56:59], a[76:79], v[156:159]\n"
"s_lshl_b32 s76, s18, 7\n"
"s_add_u32 s76, s76, 33066112\n"
"v_mfma_f32_16x16x32_bf16 v[156:159], a[60:63], a[68:71], v[156:159]\n"
"v_mfma_f32_16x16x32_bf16 v[152:155], a[60:63], a[64:67], v[152:155]\n"
"buffer_store_dwordx4 v[12:15], v58, s[28:31], s50 offen sc1\n"
"v_mfma_f32_16x16x32_bf16 v[152:155], a[56:59], a[72:75], v[152:155]\n"
"s_sub_u32 s47, s47, 2\n"
"v_mfma_f32_16x16x32_bf16 v[152:155], a[52:55], a[80:83], v[152:155]\n"
"s_add_u32 s50, s50, 256\n"
"s_mov_b32 s30, s38\n"
"v_mfma_f32_16x16x32_bf16 v[152:155], a[48:51], a[88:91], v[152:155]\n"
"s_waitcnt lgkmcnt(0)\n"
"s_cmp_eq_u32 s47, 0\n"
"s_branch Lcv%=\n"
"LcvfA%=:\n"
"v_mfma_f32_16x16x32_bf16 v[128:131], a[0:3], a[16:19], 0\n"
"ds_read_b128 a[48:51], v16 offset:53824\n"
"v_mfma_f32_16x16x32_bf16 v[132:135], a[0:3], a[20:23], 0\n"
"ds_read_b128 a[64:67], v18 offset:53760\n"
"v_mfma_f32_16x16x32_bf16 v[140:143], a[0:3], a[28:31], 0\n"
"ds_read_b128 a[68:71], v18 offset:58880\n"
"v_mfma_f32_16x16x32_bf16 v[140:143], a[4:7], a[20:23], v[140:143]\n"
"ds_read_b128 a[72:75], v18 offset:54784\n"
"v_mfma_f32_16x16x32_bf16 v[136:139], a[4:7], a[16:19], 0\n"
"ds_read_b128 a[76:79], v18 offset:59904\n"
"v_mfma_f32_16x16x32_bf16 v[136:139], a[0:3], a[24:27], v[136:139]\n"
"ds_read_b128 a[80:83], v18 offset:55808\n"
"v_mfma_f32_16x16x32_bf16 v[144:147], a[0:3], a[32:35], 0\n"
"ds_read_b128 a[84:87], v18 offset:60928\n"
"v_mfma_f32_16x16x32_bf16 v[144:147], a[4:7], a[24:27], v[144:147]\n"
"ds_read_b128 a[88:91], v18 offset:56832\n"
"v_mfma_f32_16x16x32_bf16 v[144:147], a[8:11], a[16:19], v[144:147]\n"
"ds_read_b128 a[92:95], v18 offset:61952\n"
"v_mfma_f32_16x16x32_bf16 v[148:151], a[8:11], a[20:23], 0\n"
"ds_read_b128 a[52:55], v16 offset:54848\n"
"v_mfma_f32_16x16x32_bf16 v[148:151], a[4:7], a[28:31], v[148:151]\n"
"ds_read_b128 a[56:59], v16 offset:55872\n"
"v_mfma_f32_16x16x32_bf16 v[148:151], a[0:3], a[36:39], v[148:151]\n"
"ds_read_b128 a[60:63], v16 offset:56896\n"
"v_mfma_f32_16x16x32_bf16 v[156:159], a[0:3], a[44:47], 0\n"
"ds_write_b128 v46, v[96:99] offset:0\n"
"v_mfma_f32_16x16x32_bf16 v[156:159], a[4:7], a[36:39], v[156:159]\n"
"v_mfma_f32_16x16x32_bf16 v[156:159], a[8:11], a[28:31], v[156:159]\n"
"v_mfma_f32_16x16x32_bf16 v[156:159], a[12:15], a[20:23], v[156:159]\n"
"ds_write_b128 v46, v[100:103] offset:1024\n"
"v_mfma_f32_16x16x32_bf16 v[152:155], a[12:15], a[16:19], 0\n"
"v_mfma_f32_16x16x32_bf16 v[152:155], a[8:11], a[24:27], v[152:155]\n"
"v_mfma_f32_16x16x32_bf16 v[152:155], a[4:7], a[32:35], v[152:155]\n"
"ds_write_b128 v46, v[104:107] offset:2048\n"
"v_mfma_f32_16x16x32_bf16 v[152:155], a[0:3], a[40:43], v[152:155]\n"
"s_waitcnt lgkmcnt(3)\n"
"v_mfma_f32_16x16x32_bf16 v[128:131], a[48:51], a[64:67], v[128:131]\n"
"ds_read_b128 a[0:3], v16 offset:53888\n"
"v_readlane_b32 s51, v48, s44\n"
"v_mfma_f32_16x16x32_bf16 v[132:135], a[48:51], a[68:71], v[132:135]\n"
"ds_read_b128 a[16:19], v19 offset:53760\n"
"s_add_u32 s44, s44, 2\n"
"v_mfma_f32_16x16x32_bf16 v[140:143], a[48:51], a[76:79], v[140:143]\n"
"ds_read_b128 a[20:23], v19 offset:58880\n"
"v_mfma_f32_16x16x32_bf16 v[140:143], a[52:55], a[68:71], v[140:143]\n"
"ds_read_b128 a[24:27], v19 offset:54784\n"
"v_mfma_f32_16x16x32_bf16 v[136:139], a[52:55], a[64:67], v[136:139]\n"
"ds_read_b128 a[28:31], v19 offset:59904\n"
"v_and_b32 v4, s51, v49\n"
"v_mfma_f32_16x16x32_bf16 v[136:139], a[48:51], a[72:75], v[136:139]\n"
"ds_read_b128 a[32:35], v19 offset:55808\n"
"v_and_b32 v5, s51, v50\n"
"v_mfma_f32_16x16x32_bf16 v[144:147], a[48:51], a[80:83], v[144:147]\n"
"ds_read_b128 a[36:39], v19 offset:60928\n"
"v_and_b32 v6, s51, v51\n"
"v_mfma_f32_16x16x32_bf16 v[144:147], a[52:55], a[72:75], v[144:147]\n"
"ds_read_b128 a[40:43], v19 offset:56832\n"
"v_and_b32 v7, s51, v52\n"
"v_mfma_f32_16x16x32_bf16 v[144:147], a[56:59], a[64:67], v[144:147]\n"
"ds_read_b128 a[44:47], v19 offset:61952\n"
"v_mfma_f32_16x16x32_bf16 v[148:151], a[56:59], a[68:71], v[148:151]\n"
"ds_read_b128 a[4:7], v16 offset:54912\n"
"v_mfma_f32_16x16x32_bf16 v[148:151], a[52:55], a[76:79], v[148:151]\n"
"ds_read_b128 a[8:11], v16 offset:55936\n"
"v_mfma_f32_16x16x32_bf16 v[148:151], a[48:51], a[84:87], v[148:151]\n"
"ds_read_b128 a[12:15], v16 offset:56960\n"
"v_mfma_f32_16x16x32_bf16 v[156:159], a[48:51], a[92:95], v[156:159]\n"
"ds_write_b128 v46, v[108:111] offset:3072\n"
"v_mfma_f32_16x16x32_bf16 v[156:159], a[52:55], a[84:87], v[156:159]\n"
"v_mfma_f32_16x16x32_bf16 v[156:159], a[56:59], a[76:79], v[156:159]\n"
"v_mfma_f32_16x16x32_bf16 v[156:159], a[60:63], a[68:71], v[156:159]\n"
"ds_write_b128 v46, v[112:115] offset:4096\n"
"v_mfma_f32_16x16x32_bf16 v[152:155], a[60:63], a[64:67], v[152:155]\n"
"v_mfma_f32_16x16x32_bf16 v[152:155], a[56:59], a[72:75], v[152:155]\n"
"v_mfma_f32_16x16x32_bf16 v[152:155], a[52:55], a[80:83], v[152:155]\n"
"ds_write_b128 v46, v[116:119] offset:5120\n"
"v_mfma_f32_16x16x32_bf16 v[152:155], a[48:51], a[88:91], v[152:155]\n"
"s_waitcnt lgkmcnt(3)\n"
"v_mfma_f32_16x16x32_bf16 v[128:131], a[0:3], a[16:19], v[128:131]\n"
"ds_read_b128 a[48:51], v16 offset:53952\n"
"ds_read_b128 v[176:179], v21 offset:53760\n"
"v_mfma_f32_16x16x32_bf16 v[132:135], a[0:3], a[20:23], v[132:135]\n"
"ds_read_b128 a[64:67], v20 offset:53760\n"
"ds_read_b128 v[180:183], v21 offset:58880\n"
"v_mfma_f32_16x16x32_bf16 v[140:143], a[0:3], a[28:31], v[140:143]\n"
"ds_read_b128 a[68:71], v20 offset:58880\n"
"v_mfma_f32_16x16x32_bf16 v[140:143], a[4:7], a[20:23], v[140:143]\n"
"ds_read_b128 a[72:75], v20 offset:54784\n"
"v_mfma_f32_16x16x32_bf16 v[136:139], a[4:7], a[16:19], v[136:139]\n"
"ds_read_b128 a[76:79], v20 offset:59904\n"
"v_mfma_f32_16x16x32_bf16 v[136:139], a[0:3], a[24:27], v[136:139]\n"
"ds_read_b128 a[80:83], v20 offset:55808\n"
"v_mfma_f32_16x16x32_bf16 v[144:147], a[0:3], a[32:35], v[144:147]\n"
"ds_read_b128 a[84:87], v20 offset:60928\n"
"v_mfma_f32_16x16x32_bf16 v[144:147], a[4:7], a[24:27], v[144:147]\n"
"ds_read_b128 a[88:91], v20 offset:56832\n"
"v_mfma_f32_16x16x32_bf16 v[144:147], a[8:11], a[16:19], v[144:147]\n"
"ds_read_b128 a[92:95], v20 offset:61952\n"
"v_mfma_f32_16x16x32_bf16 v[148:151], a[8:11], a[20:23], v[148:151]\n"
"ds_read_b128 a[52:55], v16 offset:54976\n"
"v_mfma_f32_16x16x32_bf16 v[148:151], a[4:7], a[28:31], v[148:151]\n"
"ds_read_b128 a[56:59], v16 offset:56000\n"
"v_mfma_f32_16x16x32_bf16 v[148:151], a[0:3], a[36:39], v[148:151]\n"
"ds_read_b128 a[60:63], v16 offset:57024\n"
"v_mfma_f32_16x16x32_bf16 v[156:159], a[0:3], a[44:47], v[156:159]\n"
"ds_write_b128 v46, v[120:123] offset:6144\n"
"v_mfma_f32_16x16x32_bf16 v[156:159], a[4:7], a[36:39], v[156:159]\n"
"v_mfma_f32_16x16x32_bf16 v[156:159], a[8:11], a[28:31], v[156:159]\n"
"v_mfma_f32_16x16x32_bf16 v[156:159], a[12:15], a[20:23], v[156:159]\n"
"ds_write_b128 v46, v[124:127] offset:7168\n"
"v_mfma_f32_16x16x32_bf16 v[152:155], a[12:15], a[16:19], v[152:155]\n"
"v_mfma_f32_16x16x32_bf16 v[152:155], a[8:11], a[24:27], v[152:155]\n"
"v_mfma_f32_16x16x32_bf16 v[152:155], a[4:7], a[32:35], v[152:155]\n"
"v_mfma_f32_16x16x32_bf16 v[152:155], a[0:3], a[40:43], v[152:155]\n"
"s_waitcnt lgkmcnt(0)\n"
"s_barrier\n"
"v_mfma_f32_16x16x32_bf16 v[128:131], a[48:51], a[64:67], v[128:131]\n"
"ds_read_b128 v[192:195], v47 offset:0\n"
"ds_read_b128 v[196:199], v47 offset:1024\n"
"v_mfma_f32_16x16x32_bf16 v[132:135], a[48:51], a[68:71], v[132:135]\n"
"ds_read_b128 v[200:203], v47 offset:2048\n"
"ds_read_b128 v[204:207], v47 offset:3072\n"
"v_mfma_f32_16x16x32_bf16 v[140:143], a[48:51], a[76:79], v[140:143]\n"
"ds_read_b128 v[208:211], v47 offset:8192\n"
"ds_read_b128 v[212:215], v47 offset:9216\n"
"v_mfma_f32_16x16x32_bf16 v[140:143], a[52:55], a[68:71], v[140:143]\n"
"ds_read_b128 v[216:219], v47 offset:10240\n"
"ds_read_b128 v[220:223], v47 offset:11264\n"
"v_mfma_f32_16x16x32_bf16 v[136:139], a[52:55], a[64:67], v[136:139]\n"
"v_mfma_f32_16x16x32_bf16 v[136:139], a[48:51], a[72:75], v[136:139]\n"
"v_mfma_f32_16x16x32_bf16 v[144:147], a[48:51], a[80:83], v[144:147]\n"
"v_mfma_f32_16x16x32_bf16 v[144:147], a[52:55], a[72:75], v[144:147]\n"
"s_waitcnt lgkmcnt(0)\n"
"v_add_f32 v192, v192, v208\n"
"v_mfma_f32_16x16x32_bf16 v[144:147], a[56:59], a[64:67], v[144:147]\n"
"v_add_f32 v193, v193, v209\n"
"v_add_f32 v194, v194, v210\n"
"v_mfma_f32_16x16x32_bf16 v[148:151], a[56:59], a[68:71], v[148:151]\n"
"v_add_f32 v195, v195, v211\n"
"v_add_f32 v196, v196, v212\n"
"v_mfma_f32_16x16x32_bf16 v[148:151], a[52:55], a[76:79], v[148:151]\n"
"v_add_f32 v197, v197, v213\n"
"v_add_f32 v198, v198, v214\n"
"v_mfma_f32_16x16x32_bf16 v[148:151], a[48:51], a[84:87], v[148:151]\n"
"v_add_f32 v199, v199, v215\n"
"v_add_f32 v200, v200, v216\n"
"v_mfma_f32_16x16x32_bf16 v[156:159], a[48:51], a[92:95], v[156:159]\n"
"v_add_f32 v201, v201, v217\n"
"v_add_f32 v202, v202, v218\n"
"v_mfma_f32_16x16x32_bf16 v[156:159], a[52:55], a[84:87], v[156:159]\n"
"v_add_f32 v203, v203, v219\n"
"v_add_f32 v204, v204, v220\n"
"v_mfma_f32_16x16x32_bf16 v[156:159], a[56:59], a[76:79], v[156:159]\n"
"v_add_f32 v205, v205, v221\n"
"v_add_f32 v206, v206, v222\n"
"v_mfma_f32_16x16x32_bf16 v[156:159], a[60:63], a[68:71], v[156:159]\n"
"v_add_f32 v207, v207, v223\n"
"v_mfma_f32_16x16x32_bf16 v[152:155], a[60:63], a[64:67], v[152:155]\n"
"v_mfma_f32_16x16x32_bf16 v[152:155], a[56:59], a[72:75], v[152:155]\n"
"v_mfma_f32_16x16x32_bf16 v[152:155], a[52:55], a[80:83], v[152:155]\n"
"v_mfma_f32_16x16x32_bf16 v[152:155], a[48:51], a[88:91], v[152:155]\n"
"v_mfma_f32_16x16x32_bf16 v[192:195], v[4:7], v[160:163], v[192:195]\n"
"v_mfma_f32_16x16x32_bf16 v[196:199], v[4:7], v[164:167], v[196:199]\n"
"v_mfma_f32_16x16x32_bf16 v[200:203], v[4:7], v[168:171], v[200:203]\n"
"v_mfma_f32_16x16x32_bf16 v[204:207], v[4:7], v[172:175], v[204:207]\n"
"v_readlane_b32 s51, v48, 8\n"
"s_nop 2\n"
"ds_write_b128 v28, v[128:131] offset:0\n"
"ds_write_b128 v28, v[132:135] offset:1024\n"
"ds_write_b128 v28, v[136:139] offset:2048\n"
"ds_write_b128 v28, v[140:143] offset:3072\n"
"ds_write_b128 v28, v[144:147] offset:4096\n"
"ds_write_b128 v28, v[148:151] offset:5120\n"
"ds_write_b128 v28, v[152:155] offset:6144\n"
"ds_write_b128 v28, v[156:159] offset:7168\n"
"v_and_b32 v4, s51, v49\n"
"v_and_b32 v5, s51, v50\n"
"v_and_b32 v6, s51, v51\n"
"v_and_b32 v7, s51, v52\n"
"s_lshr_b32 s58, s19, 1\n"
"s_lshl_b32 s58, s58, 7\n"
"s_waitcnt lgkmcnt(0)\n"
"s_barrier\n"
"ds_read_b128 v[96:99], v29 offset:0\n"
"ds_read_b128 v[100:103], v29 offset:1024\n"
"ds_read_b128 v[104:107], v29 offset:8192\n"
"ds_read_b128 v[108:111], v29 offset:9216\n"
"ds_read_b128 v[112:115], v29 offset:16384\n"
"ds_read_b128 v[116:119], v29 offset:17408\n"
"ds_read_b128 v[120:123], v29 offset:24576\n"
"ds_read_b128 v[124:127], v29 offset:25600\n"
"v_cvt_pk_bf16_f32 v8, v192, v196\n"
"v_cvt_pk_bf16_f32 v9, v193, v197\n"
"v_cvt_pk_bf16_f32 v10, v194, v198\n"
"v_cvt_pk_bf16_f32 v11, v195, v199\n"
"buffer_store_dwordx4 v[8:11], v45, s[28:31], s50 offen sc1\n"
"v_cvt_pk_bf16_f32 v12, v200, v204\n"
"v_cvt_pk_bf16_f32 v13, v201, v205\n"
"v_cvt_pk_bf16_f32 v14, v202, v206\n"
"v_cvt_pk_bf16_f32 v15, v203, v207\n"
"buffer_store_dwordx4 v[12:15], v58, s[28:31], s50 offen sc1\n"
"s_add_u32 s50, s50, 256\n"
"s_mov_b32 s30, s38\n"
"s_sub_u32 s50, s50, s58\n"
"s_waitcnt lgkmcnt(0)\n"
"v_add_f32 v96, v96, v104\n"
"v_add_f32 v97, v97, v105\n"
"v_add_f32 v98, v98, v106\n"
"v_add_f32 v99, v99, v107\n"
"v_add_f32 v100, v100, v108\n"
"v_add_f32 v101, v101, v109\n"
"v_add_f32 v102, v102, v110\n"
"v_add_f32 v103, v103, v111\n"
"v_add_f32 v112, v112, v120\n"
"v_add_f32 v113, v113, v121\n"
"v_add_f32 v114, v114, v122\n"
"v_add_f32 v115, v115, v123\n"
"v_add_f32 v116, v116, v124\n"
"v_add_f32 v117, v117, v125\n"
"v_add_f32 v118, v118, v126\n"
"v_add_f32 v119, v119, v127\n"
"v_add_f32 v96, v96, v112\n"
"v_add_f32 v97, v97, v113\n"
"v_add_f32 v98, v98, v114\n"
"v_add_f32 v99, v99, v115\n"
"v_add_f32 v100, v100, v116\n"
"v_add_f32 v101, v101, v117\n"
"v_add_f32 v102, v102, v118\n"
"v_add_f32 v103, v103, v119\n"
"s_nop 1\n"
"v_mfma_f32_16x16x32_bf16 v[96:99], v[4:7], v[176:179], v[96:99]\n"
"v_mfma_f32_16x16x32_bf16 v[100:103], v[4:7], v[180:183], v[100:103]\n"
"s_nop 7\n"
"v_cvt_pk_bf16_f32 v104, v96, v100\n"
"v_cvt_pk_bf16_f32 v105, v97, v101\n"
"v_cvt_pk_bf16_f32 v106, v98, v102\n"
"v_cvt_pk_bf16_f32 v107, v99, v103\n"
"buffer_store_dwordx4 v[104:107], v31, s[28:31], s50 offen sc1\n"
"Lcve%=:\n"
"s_lshl_b32 s56, s55, 3\n"
"s_add_u32 s56, s56, 33065472\n"
"s_waitcnt vmcnt(0)\n"
"s_barrier\n"
"buffer_store_dwordx2 v[62:63], v61, s[36:39], s56 offen sc1\n"
"s_cmp_lg_u32 s18, 255\n"
"s_cbranch_scc1 Lnob%=\n"
"s_cmp_lg_u32 s19, 1\n"
"s_cbranch_scc1 Lnob%=\n"
"v_lshrrev_b32 v22, 4, v0\n"
"v_lshlrev_b32 v22, 3, v22\n"
"v_and_b32 v23, 15, v0\n"
"v_mul_u32_u24 v23, 640, v23\n"
"v_mov_b32 v18, 0xf0a0500\n"
"v_lshrrev_b32 v18, v22, v18\n"
"v_and_b32 v18, 0xff, v18\n"
"v_lshl_add_u32 v18, v18, 3, v23\n"
"v_add_u32 v18, 33030144, v18\n"
"v_mov_b32 v19, 0xf0a0500\n"
"v_lshrrev_b32 v19, v22, v19\n"
"v_and_b32 v19, 0xff, v19\n"
"v_lshl_add_u32 v19, v19, 3, v23\n"
"v_add_u32 v19, 33030288, v19\n"
"v_mov_b32 v20, 0xf0a0500\n"
"v_lshrrev_b32 v20, v22, v20\n"
"v_and_b32 v20, 0xff, v20\n"
"v_lshl_add_u32 v20, v20, 3, v23\n"
"v_add_u32 v20, 33030432, v20\n"
"v_mov_b32 v21, 0xf0a0500\n"
"v_lshrrev_b32 v21, v22, v21\n"
"v_and_b32 v21, 0xff, v21\n"
"v_lshl_add_u32 v21, v21, 3, v23\n"
"v_add_u32 v21, 33030576, v21\n"
"s_mov_b32 s52, 0\n"
"Lbu%=:\n"
"buffer_load_dwordx2 v[84:85], v18, s[36:39], 0 offen sc1\n"
"buffer_load_dwordx2 v[86:87], v19, s[36:39], 0 offen sc1\n"
"buffer_load_dwordx2 v[88:89], v20, s[36:39], 0 offen sc1\n"
"buffer_load_dwordx2 v[90:91], v21, s[36:39], 0 offen sc1\n"
"s_waitcnt vmcnt(0)\n"
"v_cmp_ne_u64 s[56:57], s[92:93], v[84:85]\n"
"v_cmp_ne_u64 vcc, s[92:93], v[86:87]\n"
"s_nop 0\n"
"s_or_b64 s[56:57], s[56:57], vcc\n"
"v_cmp_ne_u64 vcc, s[92:93], v[88:89]\n"
"s_nop 0\n"
"s_or_b64 s[56:57], s[56:57], vcc\n"
"v_cmp_ne_u64 vcc, s[92:93], v[90:91]\n"
"s_nop 0\n"
"s_or_b64 s[56:57], s[56:57], vcc\n"
"s_add_u32 s52, s52, 1\n"
"s_cmp_eq_u64 s[56:57], 0\n"
"s_cbranch_scc1 Lbud%=\n"
"s_cmp_lt_u32 s52, 0x40000\n"
"s_cbranch_scc1 Lbu%=\n"
"Lbud%=:\n"
"v_mov_b32 v26, s92\n"
"v_mov_b32 v27, s93\n"
"s_mov_b32 s52, 33065984\n"
"s_mov_b64 exec, 1\n"
"buffer_store_dwordx2 v[26:27], off, s[36:39], s52 sc1\n"
"s_mov_b64 exec, -1\n"
"Lnob%=:\n"
"s_branch Lfin%=\n"
"Lexit%=:\n"
  :: "s"(__builtin_amdgcn_kernarg_segment_ptr()), "s"(blockIdx.x), "s"(wv), "s"(lb), "s"(tb)
  : "v0","v1","v2","v3","v4","v5","v6","v7","v8","v9","v10","v11","v12","v13","v14","v15","v16","v17","v18","v19","v20","v21","v22","v23","v24","v25","v26","v27","v28","v29","v30","v31","v32","v33","v34","v35","v36","v37","v38","v39","v40","v41","v42","v43","v44","v45","v46","v47","v48","v49","v50","v51","v52","v53","v54","v55","v56","v57","v58","v59","v60","v61","v62","v63","v64","v65","v66","v67","v68","v69","v70","v71","v72","v73","v74","v75","v76","v77","v78","v79","v80","v81","v82","v83","v84","v85","v86","v87","v88","v89","v90","v91","v92","v93","v94","v95","v96","v97","v98","v99","v100","v101","v102","v103","v104","v105","v106","v107","v108","v109","v110","v111","v112","v113","v114","v115","v116","v117","v118","v119","v120","v121","v122","v123","v124","v125","v126","v127","v128","v129","v130","v131","v132","v133","v134","v135","v136","v137","v138","v139","v140","v141","v142","v143","v144","v145","v146","v147","v148","v149","v150","v151","v152","v153","v154","v155","v156","v157","v158","v159","v160","v161","v162","v163","v164","v165","v166","v167","v168","v169","v170","v171","v172","v173","v174","v175","v176","v177","v178","v179","v180","v181","v182","v183","v184","v185","v186","v187","v188","v189","v190","v191","v192","v193","v194","v195","v196","v197","v198","v199","v200","v201","v202","v203","v204","v205","v206","v207","v208","v209","v210","v211","v212","v213","v214","v215","v216","v217","v218","v219","v220","v221","v222","v223","v224","v225","v226","v227","v228","v229","v230","v231","v232","v233","v234","v235","v236","v237","v238","v239","v240","v241","v242","v243","v244","v245","v246","v247","v248","v249","v250","v251","v252","v253","v254","v255","a0","a1","a2","a3","a4","a5","a6","a7","a8","a9","a10","a11","a12","a13","a14","a15","a16","a17","a18","a19","a20","a21","a22","a23","a24","a25","a26","a27","a28","a29","a30","a31","a32","a33","a34","a35","a36","a37","a38","a39","a40","a41","a42","a43","a44","a45","a46","a47","a48","a49","a50","a51","a52","a53","a54","a55","a56","a57","a58","a59","a60","a61","a62","a63","a64","a65","a66","a67","a68","a69","a70","a71","a72","a73","a74","a75","a76","a77","a78","a79","a80","a81","a82","a83","a84","a85","a86","a87","a88","a89","a90","a91","a92","a93","a94","a95","a96","a97","a98","a99","a100","a101","a102","a103","a104","a105","a106","a107","a108","a109","a110","a111","a112","a113","a114","a115","a116","a117","a118","a119","a120","a121","a122","a123","a124","a125","a126","a127","a128","a129","a130","a131","a132","a133","a134","a135","a136","a137","a138","a139","a140","a141","a142","a143","a144","a145","a146","a147","a148","a149","a150","a151","a152","a153","a154","a155","a156","a157","a158","a159","a160","a161","a162","a163","a164","a165","a166","a167","a168","a169","a170","a171","a172","a173","a174","a175","a176","a177","a178","a179","a180","a181","a182","a183","a184","a185","a186","a187","a188","a189","a190","a191","a192","a193","a194","a195","a196","a197","a198","a199","a200","a201","a202","a203","a204","a205","a206","a207","a208","a209","a210","a211","a212","a213","a214","a215","a216","a217","a218","a219","a220","a221","a222","a223","a224","a225","a226","a227","a228","a229","a230","a231","a232","a233","a234","a235","a236","a237","a238","a239","a240","a241","a242","a243","a244","a245","a246","a247","a248","a249","a250","a251","a252","a253","a254","a255","s16","s17","s18","s19","s20","s21","s22","s23","s24","s25","s26","s27","s28","s29","s30","s31","s34","s35","s36","s37","s38","s39","s40","s41","s42","s43","s44","s45","s46","s47","s48","s49","s50","s51","s52","s53","s54","s55","s56","s57","s58","s59","s60","s61","s62","s63","s64","s65","s66","s67","s68","s69","s70","s71","s72","s73","s74","s75","s76","s77","s78","s79","s80","s81","s82","s83","s84","s85","s86","s87","s88","s89","s90","s91","s92","s93","s94","s95","s96","s97","s98","s99","vcc","scc","memory");
}
extern "C" void kernel_launch(void* const* d_in, const int* in_sizes, int n_in, void* d_out, int out_size, void* d_ws, size_t ws_size, hipStream_t stream) {
  Args a;
  a.p[0] = d_in[0];
  a.p[1] = d_in[7];
  a.p[2] = d_in[8];
  a.p[3] = d_ws;
  a.p[4] = d_in[9];
  a.p[5] = d_in[10];
  a.p[6] = d_out;
  a.p[7] = (char*)d_ws + (64 << 20);
  a.p[8] = d_in[1];
  a.p[9] = d_in[2];
  a.p[10] = d_in[3];
  a.p[11] = d_in[4];
  a.p[12] = d_in[5];
  a.p[13] = (char*)d_ws + (64 << 20);
  fused_glu_conv<<<256, 256, 0, stream>>>(a);
}
```
